```python
import torch

def _softmax(x, axis=-1):
    return torch.softmax(x, dim=axis)
def _gelu(x):
    return torch.nn.functional.gelu(x, approximate="tanh")
def _silu(x):
    return torch.nn.functional.silu(x)
def _sigmoid(x):
    return torch.sigmoid(x)
def _layernorm(x, axis=-1, eps=1e-5):
    m = x.mean(dim=axis, keepdim=True)
    v = x.var(dim=axis, keepdim=True, unbiased=False)
    return (x - m) / torch.sqrt(v + eps)
def _scatter_add(tgt, idx, src, axis=0):
    return tgt.clone().index_add_(axis, idx, src)
def _segment_sum(src, seg, n):
    out = torch.zeros((n,) + src.shape[1:], dtype=src.dtype)
    return out.index_add_(0, seg, src)
def _pad(x, pw):
    flat = tuple(v for pair in reversed(pw) for v in pair)
    return torch.nn.functional.pad(x, flat)

kernel_name = 'concat_heads__linear_attn_block__linear_kernel_hedgehog__prompt'

def setup_inputs():

    _w = (lambda a: (lambda t: t.to(torch.bfloat16) if t.is_floating_point() else t)(torch.as_tensor(a)))
    def _A_setup():
        B, S, D = 1, 2048, 576
        return {
            "x": _w((lambda *s: torch.randn(*s, dtype=torch.bfloat16) * 0.05)(B, S, D)),
            "wqkv": _w((lambda *s: torch.randn(*s, dtype=torch.bfloat16) * 0.8)(D, 3 * D)),
            "wo": _w((lambda *s: torch.randn(*s, dtype=torch.bfloat16) * 0.2)(D, D)),
        }
    _A_in = _A_setup()

    def _B_setup():
        B, S, D, H = 1, 2048, 576, 32
        Dh = D // H
        return {
            "x": _w((lambda *s: torch.randn(*s, dtype=torch.bfloat16) * 0.4)(B, S, D)),
            "wqkv": _w((lambda *s: torch.randn(*s, dtype=torch.bfloat16) * 0.4)(D, 3 * D)),
            "w_phi": _w((lambda *s: torch.randn(*s, dtype=torch.bfloat16) * 0.4)(H, Dh, Dh) * (Dh ** -0.5)),
            "wo": _w((lambda *s: torch.randn(*s, dtype=torch.bfloat16) * 0.05)(D, D)),
        }
    _B_in = _B_setup()

    return {
        "A_x": _A_in["x"],
        "A_wqkv": _A_in["wqkv"],
        "A_wo": _A_in["wo"],
        "B_x": _B_in["x"],
        "B_wqkv": _B_in["wqkv"],
        "B_w_phi": _B_in["w_phi"],
        "B_wo": _B_in["wo"],
    }

def reference(A_x, A_wqkv, A_wo, B_x, B_wqkv, B_w_phi, B_wo):
    def _A_ref(x, wqkv, wo):
        B, S, D = x.shape
        H = 32; Dh = D // H
        qkv = (lambda x, *d: x.permute(*d))((x @ wqkv).reshape(B, S, 3, H, Dh), 2, 0, 3, 1, 4)
        q, k, v = qkv[0], qkv[1], qkv[2]
        phi = lambda t: torch.where(t > 0, t + 1, torch.exp(t)) + 1e-6
        pq, pk = phi(q), phi(k)
        kv = torch.einsum("bhsd,bhse->bhde", pk, v)
        z = torch.einsum("bhsd->bhd", pk)
        num = torch.einsum("bhqd,bhde->bhqe", pq, kv)
        den = torch.einsum("bhqd,bhd->bhq", pq, z)[..., None] + 1e-6
        o = num / den
        return (lambda x, *d: x.permute(*d))(o, 0, 2, 1, 3).reshape(B, S, D) @ wo

    def _B_ref(x, wqkv, w_phi, wo):
        B, S, D = x.shape
        H = 32; Dh = D // H
        qkv = (lambda x, *d: x.permute(*d))((x @ wqkv).reshape(B, S, 3, H, Dh), 2, 0, 3, 1, 4)
        q, k, v = qkv[0], qkv[1], qkv[2]
        pq = torch.einsum("bhsd,hdf->bhsf", q, w_phi)
        pk = torch.einsum("bhsd,hdf->bhsf", k, w_phi)
        phi_q = torch.concatenate([_softmax(pq, axis=-1), _softmax(-pq, axis=-1)], axis=-1)
        phi_k = torch.concatenate([_softmax(pk, axis=-1), _softmax(-pk, axis=-1)], axis=-1)
        kv = torch.einsum("bhsf,bhsd->bhfd", phi_k, v)
        z  = torch.einsum("bhsf->bhf", phi_k)
        num = torch.einsum("bhqf,bhfd->bhqd", phi_q, kv)
        den = torch.einsum("bhqf,bhf->bhq", phi_q, z)[..., None] + 1e-6
        o = num / den
        return (lambda x, *d: x.permute(*d))(o, 0, 2, 1, 3).reshape(B, S, D) @ wo

    a = _A_ref(A_x, A_wqkv, A_wo)
    b = _B_ref(B_x, B_wqkv, B_w_phi, B_wo)
    return torch.concatenate([a, b], axis=-1)
```

```cpp
#include <hip/hip_runtime.h>
#include <hip/hip_bf16.h>
typedef __hip_bfloat16 bf16;
constexpr int S=2048, D=576, H=32, DH=18;
__device__ inline float b2f(bf16 x){return __bfloat162float(x);}
__device__ inline float rb(float x){return __bfloat162float(__float2bfloat16(x));}
__global__ void gemm_naive(const bf16* A, const bf16* B, bf16* C, int M, int N, int K, int ldc) {
  __shared__ float As[16][17], Bs[16][17];
  int tx=threadIdx.x, ty=threadIdx.y; int row=blockIdx.y*16+ty, col=blockIdx.x*16+tx;
  float acc=0;
  for(int k0=0;k0<K;k0+=16){
    As[ty][tx]=b2f(A[(size_t)row*K+k0+tx]); Bs[ty][tx]=b2f(B[(size_t)(k0+ty)*N+col]);
    __syncthreads();
    for(int k=0;k<16;k++) acc+=As[ty][k]*Bs[k][tx];
    __syncthreads();
  }
  C[(size_t)row*ldc+col]=__float2bfloat16(acc);
}
__global__ void feat(const bf16* qkv, const bf16* wphi, float* pq, float* pk, int isB) {
  int idx=blockIdx.x*blockDim.x+threadIdx.x; if(idx>=S*H*2) return;
  int which=idx/(S*H); int r=idx%(S*H); int s=r/H,h=r%H;
  const bf16* t=qkv+(size_t)s*3*D+which*D+h*DH;
  float* o=(which?pk:pq)+(size_t)(s*H+h)*36;
  if(!isB){ for(int d=0;d<DH;d++){float x=b2f(t[d]); float y=x>0?rb(x+1.f):rb(expf(x)); o[d]=rb(y+1e-6f);} }
  else { float p[DH]; for(int f=0;f<DH;f++){float a=0; for(int d=0;d<DH;d++) a+=b2f(t[d])*b2f(wphi[(h*DH+d)*DH+f]); p[f]=rb(a);}
    float m=-1e30f,mn=1e30f; for(int f=0;f<DH;f++){m=fmaxf(m,p[f]);mn=fminf(mn,p[f]);}
    float s1=0,s2=0; for(int f=0;f<DH;f++){s1+=expf(p[f]-m); s2+=expf(mn-p[f]);}
    for(int f=0;f<DH;f++){o[f]=rb(expf(p[f]-m)/s1); o[18+f]=rb(expf(mn-p[f])/s2);} }
}
__global__ void kvk(const bf16* qkv, const float* pk, float* kv, float* z, int F) {
  int h=blockIdx.x; int t=threadIdx.x; if(t>=F*19) return; int f=t/19,e=t%19; float a=0;
  for(int s=0;s<S;s++){ float p=pk[(size_t)(s*H+h)*36+f]; float v=e<18?b2f(qkv[(size_t)s*3*D+2*D+h*DH+e]):1.f; a+=p*v;}
  if(e<18) kv[(h*36+f)*18+e]=rb(a); else z[h*36+f]=rb(a);
}
__global__ void ok(const float* pq, const float* kv, const float* z, bf16* o, int F) {
  int idx=blockIdx.x*blockDim.x+threadIdx.x; if(idx>=S*D) return; int s=idx/D,c=idx%D; int h=c/DH,e=c%DH;
  float n=0,d=0; for(int f=0;f<F;f++){float p=pq[(size_t)(s*H+h)*36+f]; n+=p*kv[(h*36+f)*18+e]; d+=p*z[h*36+f];}
  o[idx]=__float2bfloat16(rb(n)/rb(rb(d)+1e-6f));
}
extern "C" void kernel_launch(void* const* d_in, const int* in_sizes, int n_in, void* d_out, int out_size, void* d_ws, size_t ws_size, hipStream_t stream) {
  char* ws=(char*)d_ws; bf16* qkv=(bf16*)ws; float* pq=(float*)(ws+(8<<20)); float* pk=(float*)(ws+(24<<20)); float* kv=(float*)(ws+(40<<20)); float* z=(float*)(ws+(41<<20)); bf16* o=(bf16*)(ws+(42<<20));
  for(int p=0;p<2;p++){
    const bf16* x=(const bf16*)d_in[p?3:0]; const bf16* w=(const bf16*)d_in[p?4:1]; const bf16* wo=(const bf16*)d_in[p?6:2]; const bf16* wphi=(const bf16*)d_in[5];
    gemm_naive<<<dim3(3*D/16,S/16),dim3(16,16),0,stream>>>(x,w,qkv,S,3*D,D,3*D);
    feat<<<(S*H*2+255)/256,256,0,stream>>>(qkv,wphi,pq,pk,p);
    int F=p?36:18;
    kvk<<<H,704,0,stream>>>(qkv,pk,kv,z,F);
    ok<<<(S*D+255)/256,256,0,stream>>>(pq,kv,z,o,F);
    gemm_naive<<<dim3(D/16,S/16),dim3(16,16),0,stream>>>(o,wo,(bf16*)d_out+p*D,S,D,D,2*D);
  }
}
```

```cpp
#include <hip/hip_runtime.h>
#include <hip/hip_bf16.h>
#include <stdint.h>

typedef __bf16 bf16x8 __attribute__((ext_vector_type(8)));
typedef __bf16 bf16x4 __attribute__((ext_vector_type(4)));
typedef __bf16 bf16x2 __attribute__((ext_vector_type(2)));
typedef float f32x4 __attribute__((ext_vector_type(4)));
typedef float f32x2 __attribute__((ext_vector_type(2)));
typedef unsigned u32x4 __attribute__((ext_vector_type(4)));
typedef unsigned u32x2 __attribute__((ext_vector_type(2)));
#define LAS __attribute__((address_space(3)))
#define GAS __attribute__((address_space(1)))
#define DI __device__ __forceinline__
#define RLX_AGENT __ATOMIC_RELAXED, __HIP_MEMORY_SCOPE_AGENT
#define RLX_WG __ATOMIC_RELAXED, __HIP_MEMORY_SCOPE_WORKGROUP
typedef __amdgpu_buffer_rsrc_t rsrc_t;

#define DBG 0

namespace {

constexpr int XROW = 1152;
constexpr int WROW = 3456;
constexpr int OUTROW = 2304;
constexpr int SLOT = 34816, WPART = 16384;
constexpr int L_WQ = 0;
constexpr int L_PHI = 92160;
constexpr int L_V = 129024;
constexpr int L_WF = 149504;
constexpr int L_SYNC = 162304;
constexpr int L_ZERO = 162560;
constexpr int LDS_BYTES = 163840;
enum { SY_WQ0 = 0, SY_WQ1, SY_WQ2, SY_QDONE, SY_PHI, SY_KVDONE, SY_KVIMG, SY_WO };
constexpr int WS_E = 0, WS_F1 = 1024, WS_KVP = 65536, KVP_STRIDE = 6144, WS_O = 2097152, O_STRIDE = 147456;
constexpr int WS_USED = WS_O + 32 * O_STRIDE;
constexpr unsigned SENT = 0xFFFFFFFFu;
constexpr int SPIN_G = 1 << 18, SPIN_L = 1 << 22;
constexpr float LOG2E = 1.4426950408889634f;

struct Args {
  const void* xA; const void* wA; const void* woA; const void* xB; const void* wB; const void* wphi; const void* woB;
  void* out; char* ws;
};

DI f32x4 mfma(bf16x8 a, bf16x8 b, f32x4 c) { return __builtin_amdgcn_mfma_f32_16x16x32_bf16(a, b, c, 0, 0, 0); }
DI unsigned pk2(float a, float b) { f32x2 p = {a, b}; return __builtin_bit_cast(unsigned, __builtin_convertvector(p, bf16x2)); }
DI bf16x8 frag_of(unsigned a, unsigned b, unsigned c, unsigned d) { u32x4 v = {a, b, c, d}; return __builtin_bit_cast(bf16x8, v); }
DI bf16x8 pack_pair(f32x4 lo, f32x4 hi) { return frag_of(pk2(lo[0], lo[1]), pk2(lo[2], lo[3]), pk2(hi[0], hi[1]), pk2(hi[2], hi[3])); }
DI bf16x8 lds128(LAS char* l, int off) { return *(LAS bf16x8*)(l + off); }
DI bf16x8 lds_tr(LAS char* l, int off, int second) {
  bf16x4 a = __builtin_amdgcn_ds_read_tr16_b64_v4bf16((LAS bf16x4*)(l + off));
  bf16x4 b = __builtin_amdgcn_ds_read_tr16_b64_v4bf16((LAS bf16x4*)(l + off + second));
  return __builtin_shufflevector(a, b, 0, 1, 2, 3, 4, 5, 6, 7);
}
DI void dma16(rsrc_t r, unsigned voff, unsigned soff, unsigned ldsaddr) {
  asm volatile("s_mov_b32 m0, %0\n\ts_nop 0\n\tbuffer_load_dwordx4 %1, %2, %3 offen lds" ::"s"(ldsaddr), "v"(voff), "s"(r), "s"(soff) : "memory");
}
template <int N> DI void wait_vm() { asm volatile("s_waitcnt vmcnt(%0)" ::"n"(N) : "memory"); }
DI void wait_lgkm() { asm volatile("s_waitcnt lgkmcnt(0)" ::: "memory"); }
DI void barrier() { asm volatile("s_waitcnt lgkmcnt(0)\n\ts_barrier" ::: "memory"); }
DI void lds_arrive(LAS char* l, int id, int lane) {
  asm volatile("s_waitcnt lgkmcnt(0)" ::: "memory");
  if (lane == 0) __hip_atomic_fetch_add((LAS unsigned*)(l + L_SYNC) + id, 1u, RLX_WG);
  asm volatile("" ::: "memory");
}
DI void lds_wait(LAS char* l, int id) {
  for (int sp = 0; sp < SPIN_L; ++sp) {
    asm volatile("" ::: "memory");
    unsigned v = __hip_atomic_load((LAS unsigned*)(l + L_SYNC) + id, RLX_WG);
    if (__builtin_amdgcn_readfirstlane(v) >= 4u) break;
  }
  asm volatile("" ::: "memory");
}
DI rsrc_t mk_rsrc(const void* p, int bytes) { return __builtin_amdgcn_make_buffer_rsrc((void*)p, 0, bytes, 0x00020000); }
DI void st_sc1(u32x4 v, rsrc_t r, unsigned off) { __builtin_amdgcn_raw_buffer_store_b128(v, r, off, 0, 16); }
DI u32x4 ld_sc1(rsrc_t r, unsigned off, unsigned soff) { return __builtin_amdgcn_raw_buffer_load_b128(r, off, soff, 16); }
DI unsigned umax3(unsigned a, unsigned b, unsigned c) { return max(max(a, b), c); }
DI unsigned nosent(unsigned x) { return min(x, 0xFFFFFFFEu); }
DI void swap16(unsigned& a, unsigned& b) { auto r = __builtin_amdgcn_permlane16_swap(a, b, false, false); a = r[0]; b = r[1]; }

DI int krow(int rho) { return 8 * ((rho >> 2) & 3) + 4 * (rho >> 4) + (rho & 3); }

template <bool ISB> DI void feature_map(const f32x4 (&P)[5], unsigned (&ph)[ISB ? 18 : 10]) {
  float p[18];
#pragma unroll
  for (int f = 0; f < 18; ++f) p[f] = P[f >> 2][f & 3];
  if constexpr (ISB) {
    float mx = p[0], mn = p[0];
#pragma unroll
    for (int f = 1; f < 17; f += 2) { mx = fmaxf(fmaxf(mx, p[f]), p[f + 1]); mn = fminf(fminf(mn, p[f]), p[f + 1]); }
    mx = fmaxf(mx, p[17]); mn = fminf(mn, p[17]);
    const float cp = -mx * LOG2E, cn = mn * LOG2E;
    float ep[18], en[18];
#pragma unroll
    for (int f = 0; f < 18; ++f) {
      ep[f] = __builtin_amdgcn_exp2f(__builtin_fmaf(p[f], LOG2E, cp));
      en[f] = __builtin_amdgcn_exp2f(__builtin_fmaf(p[f], -LOG2E, cn));
    }
    f32x2 sp = {ep[0], ep[1]}, sn = {en[0], en[1]};
#pragma unroll
    for (int f = 2; f < 18; f += 2) { sp += f32x2{ep[f], ep[f + 1]}; sn += f32x2{en[f], en[f + 1]}; }
    const float rp = __builtin_amdgcn_rcpf(sp[0] + sp[1]), rn = __builtin_amdgcn_rcpf(sn[0] + sn[1]);
#pragma unroll
    for (int f = 0; f < 18; f += 2) {
      f32x2 a = f32x2{ep[f], ep[f + 1]} * rp, b = f32x2{en[f], en[f + 1]} * rn;
      ph[f >> 1] = pk2(a[0], a[1]);
      ph[9 + (f >> 1)] = pk2(b[0], b[1]);
    }
  } else {
    float y[18];
#pragma unroll
    for (int f = 0; f < 18; ++f)
      y[f] = (fmaxf(p[f], 0.f) + __builtin_amdgcn_exp2f(fminf(p[f], 0.f) * LOG2E)) + 1e-6f;
#pragma unroll
    for (int f = 0; f < 18; f += 2) ph[f >> 1] = pk2(y[f], y[f + 1]);
    ph[9] = 0u;
  }
}

DI void logits(LAS char* lds, int lane, const f32x4& t0, const f32x4& t1, const f32x4& t2, const f32x4& t3, const f32x4& t4, f32x4 (&P)[5]) {
  const bf16x8 b0 = pack_pair(t0, t1), b1 = pack_pair(t2, t3);
  const bf16x8 b2 = frag_of(pk2(t4[0], t4[1]), pk2(t4[2], t4[3]), 0u, 0u);
#pragma unroll
  for (int t = 0; t < 5; ++t) {
    const bf16x8 a0 = lds128(lds, L_WF + (2 * t) * 1024 + lane * 16);
    const bf16x8 a1 = lds128(lds, L_WF + (2 * t + 1) * 1024 + lane * 16);
    const u32x2 h = *(LAS u32x2*)(lds + L_WF + 10240 + t * 512 + lane * 8);
    const bf16x8 a2 = frag_of(h[0], h[1], 0u, 0u);
    f32x4 c = {0.f, 0.f, 0.f, 0.f};
    c = mfma(a0, b0, c); c = mfma(a1, b1, c); c = mfma(a2, b2, c);
    P[t] = c;
  }
}

template <bool ISB> DI void c_path(const Args& a, LAS char* lds, const int w, const int lane, const int t, const int g) {
  constexpr int prob = ISB ? 1 : 0;
  constexpr int RP = ISB ? 288 : 160;
  constexpr int C5 = ISB ? 5 : 3;
  constexpr int KVROW = C5 * 16, KVHEAD = 19 * KVROW, NCH = 19 * C5;
  const int tid = w * 64 + lane, i = lane & 15, gg = lane >> 4;
  char* ws = a.ws;
  const rsrc_t rws = mk_rsrc(ws, WS_USED);
  const unsigned kvp_grp = WS_KVP + (prob * 8 + g) * 16 * KVP_STRIDE;
  const unsigned obuf = WS_O + (prob * 16 + t) * O_STRIDE;

  const unsigned T = __hip_atomic_load((GAS unsigned*)(ws + WS_E + prob * 256), RLX_AGENT) + 1u;
  unsigned short wel[6];
  if constexpr (ISB) {
    const unsigned short* wp = (const unsigned short*)a.wphi + g * 4 * 324;
#pragma unroll
    for (int k = 0; k < 6; ++k) { const int idx = tid + 256 * k; wel[k] = wp[idx < 1296 ? idx : 0]; }
  }
  {
    const u32x4 s = {SENT, SENT, SENT, SENT};
#pragma unroll
    for (int k = 0; k < (4 * NCH + 255) / 256; ++k) { const int n = tid + 256 * k; if (n < 4 * NCH) st_sc1(s, rws, kvp_grp + t * KVP_STRIDE + n * 16); }
#pragma unroll
    for (int k = 0; k < 5; ++k) {
      const int n = tid + 256 * k;
      if (n < 1152) st_sc1(s, rws, obuf + (((n / 144) * 72 + 9 * g + (n >> 4) % 9) * 16 + (n & 15)) * 16);
    }
  }
  if (tid < 64) *(LAS unsigned*)(lds + L_SYNC + tid * 4) = 0u;
  if (tid < 32) *(LAS unsigned*)(lds + L_ZERO + tid * 4) = 0u;

  f32x4 acc[9][2];
#pragma unroll
  for (int n = 0; n < 9; ++n) { acc[n][0] = f32x4{0.f, 0.f, 0.f, 0.f}; acc[n][1] = f32x4{0.f, 0.f, 0.f, 0.f}; }
  const int xa0 = (32 * w + i) * 128 + ((gg ^ ((i >> 1) & 7)) * 16), xa1 = xa0 ^ 64;
  const int wa = WPART + (4 * gg + (i >> 2)) * 288 + (i & 3) * 8;
  barrier();
#pragma unroll
  for (int c = 0; c < 9; ++c) {
    const int base = ((c + 3) & 3) * SLOT;
#pragma unroll
    for (int s = 0; s < 2; ++s) {
      const bf16x8 xb0 = lds128(lds, base + (s ? xa1 : xa0)), xb1 = lds128(lds, base + (s ? xa1 : xa0) + 2048);
#pragma unroll
      for (int n = 0; n < 9; ++n) {
        const bf16x8 wf = lds_tr(lds, base + wa + s * 9216 + n * 32, 4608);
        acc[n][0] = mfma(wf, xb0, acc[n][0]);
        acc[n][1] = mfma(wf, xb1, acc[n][1]);
      }
    }
    if (c == 0) {
      const u32x4 z = {0u, 0u, 0u, 0u};
#pragma unroll
      for (int k = 0; k < 4; ++k) { const int n = tid + 256 * k; if (n < 800) *(LAS u32x4*)(lds + L_WF + n * 16) = z; }
    }
    if (c == 1) {
      if constexpr (ISB) {
#pragma unroll
        for (int k = 0; k < 6; ++k) {
          const int idx = tid + 256 * k;
          if (idx < 1296) {
            const int h = idx / 324, rem = idx - h * 324, d = rem / 18, f = rem - d * 18, col = 18 * h + d;
            const int s = col >> 5, ln = 16 * ((col >> 2) & 3) + 4 * h + (f & 3), tt = f >> 2;
            const int off = s < 2 ? (tt * 2 + s) * 1024 + ln * 16 + (((col >> 4) & 1) * 4 + (col & 3)) * 2 : 10240 + tt * 512 + ln * 8 + (col & 3) * 2;
            *(LAS unsigned short*)(lds + L_WF + off) = wel[k];
          }
        }
      } else if (tid < 72) {
        const int h = tid / 18, f = tid - h * 18, col = tid;
        const int s = col >> 5, ln = 16 * ((col >> 2) & 3) + 4 * h + (f & 3), tt = f >> 2;
        const int off = s < 2 ? (tt * 2 + s) * 1024 + ln * 16 + (((col >> 4) & 1) * 4 + (col & 3)) * 2 : 10240 + tt * 512 + ln * 8 + (col & 3) * 2;
        *(LAS unsigned short*)(lds + L_WF + off) = (unsigned short)0x3F80;
      }
    }
    if (c == 3) wait_vm<0>();
    if (c == 4 && tid == 0) __hip_atomic_store((GAS unsigned*)(ws + WS_F1 + (prob * 128 + t * 8 + g) * 128), T, RLX_AGENT);
    barrier();
  }

  const int xs = ((i >> 2) & 3) << 3;
#pragma unroll
  for (int m = 0; m < 2; ++m) {
    const int tok = 32 * w + 16 * m + i;
    f32x4 P[5];
    logits(lds, lane, acc[0][m], acc[1][m], acc[2][m], acc[3][m], acc[8][m], P);
    unsigned ph[ISB ? 18 : 10];
    feature_map<ISB>(P, ph);
    LAS char* prow = lds + L_PHI + tok * RP;
    constexpr int NU = ISB ? 9 : 5, UM = NU - 1;
#pragma unroll
    for (int j = 0; j < NU; ++j) {
      const int u = j < UM ? UM * gg + j : 4 * UM + gg;
      *(LAS u32x2*)(prow + ((u * 8) ^ xs)) = u32x2{ph[2 * j], ph[2 * j + 1]};
    }
    LAS char* vrow = lds + L_V + tok * 160;
#pragma unroll
    for (int n = 0; n < 5; ++n) {
      const f32x4 v = n < 4 ? acc[4 + n][m] : acc[8][m];
#pragma unroll
      for (int pp = 0; pp < 2; ++pp) {
        const int col = (n < 4 ? 16 * n + 4 * gg : 64 + 4 * (gg - 2)) + 2 * pp;
        const int h = col / 18, e = col - 18 * h;
        const int off = e < 16 ? (4 * h + (e >> 2)) * 8 + (e & 2) * 2 : (16 + h) * 8;
        if (n < 4 || gg >= 2) *(LAS unsigned*)(vrow + (off ^ xs)) = pk2(v[2 * pp], v[2 * pp + 1]);
      }
    }
    *(LAS unsigned*)(vrow + (((16 + gg) * 8 + 4) ^ xs)) = 0x00003F80u;
  }
  lds_arrive(lds, SY_PHI, lane);
  lds_wait(lds, SY_PHI);

  {
    constexpr int UM = ISB ? 8 : 4;
    const int q = i >> 2, p = i & 3;
    const int pa = L_PHI + (4 * gg + q) * RP, va = L_V + (4 * gg + q) * 160;
    const int ub0 = ((UM * w + p) ^ gg) * 8, ub1 = ((UM * w + 4 + p) ^ gg) * 8, umu = ((4 * UM + p) ^ gg) * 8;
    const int ual = ((4 * w + p) ^ gg) * 8, ula = ((16 + p) ^ gg) * 8;
    f32x4 d0 = {0.f, 0.f, 0.f, 0.f}, d1 = d0, d2 = d0, d3 = d0, d4 = d0, d5 = d0;
#pragma unroll
    for (int ks = 0; ks < 4; ++ks) {
      const bf16x8 al = lds_tr(lds, va + ks * 32 * 160 + ual, 16 * 160), la = lds_tr(lds, va + ks * 32 * 160 + ula, 16 * 160);
      const bf16x8 b0 = lds_tr(lds, pa + ks * 32 * RP + ub0, 16 * RP), mu = lds_tr(lds, pa + ks * 32 * RP + umu, 16 * RP);
      d0 = mfma(b0, al, d0); d2 = mfma(b0, la, d2); d4 = mfma(mu, al, d4); d5 = mfma(mu, la, d5);
      if constexpr (ISB) {
        const bf16x8 b1 = lds_tr(lds, pa + ks * 32 * RP + ub1, 16 * RP);
        d1 = mfma(b1, al, d1); d3 = mfma(b1, la, d3);
      }
    }
    lds_arrive(lds, SY_KVDONE, lane);
    const unsigned mine = kvp_grp + t * KVP_STRIDE + w * KVHEAD;
    const bool lam = (i >> 2) == w && (i & 3) < 3;
    const int el = 16 + (i & 3);
    if constexpr (ISB) {
      unsigned x0 = pk2(d0[0], d0[1]), x1 = pk2(d0[2], d0[3]), y0 = pk2(d1[0], d1[1]), y1 = pk2(d1[2], d1[3]);
      swap16(x0, y0); swap16(x1, y1);
      const int ch = ((gg & 1) << 1) | (gg >> 1);
      st_sc1(u32x4{nosent(x0), nosent(x1), nosent(y0), nosent(y1)}, rws, mine + i * KVROW + ch * 16);
      x0 = pk2(d2[0], d2[1]); x1 = pk2(d2[2], d2[3]); y0 = pk2(d3[0], d3[1]); y1 = pk2(d3[2], d3[3]);
      swap16(x0, y0); swap16(x1, y1);
      if (lam) st_sc1(u32x4{nosent(x0), nosent(x1), nosent(y0), nosent(y1)}, rws, mine + el * KVROW + ch * 16);
    } else {
      unsigned x0 = pk2(d0[0], d0[1]), x1 = pk2(d0[2], d0[3]), y0 = pk2(d2[0], d2[1]), y1 = pk2(d2[2], d2[3]);
      swap16(x0, y0); swap16(x1, y1);
      const bool isl = gg & 1;
      if (!isl || lam) st_sc1(u32x4{nosent(x0), nosent(x1), nosent(y0), nosent(y1)}, rws, mine + (isl ? el : i) * KVROW + (gg >> 1) * 16);
    }
    if (gg == w) st_sc1(u32x4{nosent(pk2(d4[0], d4[1])), nosent(pk2(d4[2], d4[3])), 0u, 0u}, rws, mine + i * KVROW + (C5 - 1) * 16);
    if (gg == w && lam) st_sc1(u32x4{nosent(pk2(d5[0], d5[1])), nosent(pk2(d5[2], d5[3])), 0u, 0u}, rws, mine + el * KVROW + (C5 - 1) * 16);
  }

  bool timed_out = false;
  {
    const int fidx = lane < 16 ? lane * 8 + g : t * 8 + (lane & 7);
    GAS unsigned* fp = (GAS unsigned*)(ws + WS_F1 + (prob * 128 + fidx) * 128);
    int sp = 0;
    for (; sp < SPIN_G; ++sp) {
      const unsigned f = __hip_atomic_load(fp, RLX_AGENT);
      if (__all(f == T || lane >= 24)) break;
    }
    timed_out |= sp == SPIN_G;
    const rsrc_t rkv = mk_rsrc(ws + kvp_grp, 16 * KVP_STRIDE);
    const unsigned poff = lane < 16 ? lane * KVP_STRIDE + w * KVHEAD + 18 * KVROW + (C5 - 1) * 16 : 0x7FFF0000u;
    for (sp = 0; sp < SPIN_G; ++sp) {
      asm volatile("" ::: "memory");
      const unsigned f = __builtin_amdgcn_raw_buffer_load_b32(rkv, poff, 0, 16);
      if (__all(f != SENT)) break;
    }
    timed_out |= sp == SPIN_G;
    constexpr int NR = (NCH + 63) / 64;
    float sum[NR][8];
    unsigned lo1u = 0x00003F80u, hi1u = 0x3F800000u;
    asm volatile("" : "+v"(lo1u), "+v"(hi1u));
    const bf16x2 lo1 = __builtin_bit_cast(bf16x2, lo1u), hi1 = __builtin_bit_cast(bf16x2, hi1u);
    for (sp = 0; sp < 64; ++sp) {
      asm volatile("" ::: "memory");
      u32x4 v[NR][16];
#pragma unroll
      for (int r = 0; r < NR; ++r) {
        const int c = lane + 64 * r;
        const unsigned off = c < NCH ? w * KVHEAD + c * 16 : 0x7FFF0000u;
#pragma unroll
        for (int s = 0; s < 16; ++s) v[r][s] = ld_sc1(rkv, off, s * KVP_STRIDE);
      }
      unsigned mx = 0u;
#pragma unroll
      for (int r = 0; r < NR; ++r) {
#pragma unroll
        for (int j = 0; j < 8; ++j) sum[r][j] = 0.f;
#pragma unroll
        for (int s = 0; s < 16; ++s) {
          mx = umax3(mx, v[r][s][0], v[r][s][1]); mx = umax3(mx, v[r][s][2], v[r][s][3]);
#pragma unroll
          for (int j = 0; j < 4; ++j) {
            const unsigned xu = v[r][s][j];
            const bf16x2 x = __builtin_bit_cast(bf16x2, xu);
            sum[r][2 * j] = __builtin_amdgcn_fdot2_f32_bf16(x, lo1, sum[r][2 * j], false);
            sum[r][2 * j + 1] = __builtin_amdgcn_fdot2_f32_bf16(x, hi1, sum[r][2 * j + 1], false);
          }
        }
      }
      if (__all(mx != SENT)) break;
    }
    timed_out |= sp == 64;
    lds_wait(lds, SY_KVDONE);
#pragma unroll
    for (int r = 0; r < NR; ++r) {
      const int c = lane + 64 * r;
      if (c < NCH)
        *(LAS u32x4*)(lds + L_PHI + w * KVHEAD + c * 16) =
            u32x4{pk2(sum[r][0], sum[r][1]), pk2(sum[r][2], sum[r][3]), pk2(sum[r][4], sum[r][5]), pk2(sum[r][6], sum[r][7])};
    }
    lds_arrive(lds, SY_KVIMG, lane);
  }

  {
    lds_wait(lds, SY_QDONE);
    const rsrc_t rwo = mk_rsrc(ISB ? a.woB : a.woA, 576 * XROW);
    const unsigned lbase = (unsigned)(size_t)(lds + L_WQ);
#pragma unroll
    for (int n = 0; n < 23; ++n) {
      const int v = w + 4 * n;
      if (v < 90) {
        const int ks = v / 5, nn = (v - 5 * ks) * 64 + lane, rho = nn / 10, cp = nn - 10 * rho;
        dma16(rwo, krow(rho) * XROW + 144 * g + 16 * min(cp, 8), ks * 32 * XROW, lbase + v * 1024);
      }
    }
    wait_vm<0>();
    lds_arrive(lds, SY_WO, lane);
    lds_wait(lds, SY_WO);
  }

  {
    const rsrc_t ro = mk_rsrc(ws + obuf, O_STRIDE);
    const unsigned poff = lane < 8 ? ((2 * w + 1) * 72 + 9 * lane + 8) * 256 + 15 * 16 + 12 : 0x7FFF0000u;
    int sp;
    for (sp = 0; sp < SPIN_G; ++sp) {
      asm volatile("" ::: "memory");
      const unsigned f = __builtin_amdgcn_raw_buffer_load_b32(ro, poff, 0, 16);
      if (__all(f != SENT)) break;
    }
    timed_out |= sp == SPIN_G;
    f32x4 oc[5][2];
    const int wa2 = L_WQ + (4 * gg + (i >> 2)) * 160 + (i & 3) * 8;
    for (sp = 0; sp < 64; ++sp) {
      asm volatile("" ::: "memory");
      u32x4 of[18][2];
#pragma unroll
      for (int ks = 0; ks < 18; ++ks)
#pragma unroll
        for (int m = 0; m < 2; ++m) of[ks][m] = ld_sc1(ro, lane * 16, ((2 * w + m) * 72 + 4 * ks) * 256);
#pragma unroll
      for (int n = 0; n < 5; ++n) { oc[n][0] = f32x4{0.f, 0.f, 0.f, 0.f}; oc[n][1] = f32x4{0.f, 0.f, 0.f, 0.f}; }
      unsigned mx = 0u;
#pragma unroll
      for (int ks = 0; ks < 18; ++ks) {
#pragma unroll
        for (int m = 0; m < 2; ++m) { mx = umax3(mx, of[ks][m][0], of[ks][m][1]); mx = umax3(mx, of[ks][m][2], of[ks][m][3]); }
#pragma unroll
        for (int n = 0; n < 5; ++n) {
          const bf16x8 wf = lds_tr(lds, wa2 + ks * 5120 + n * 32, 16 * 160);
          oc[n][0] = mfma(wf, __builtin_bit_cast(bf16x8, of[ks][0]), oc[n][0]);
          oc[n][1] = mfma(wf, __builtin_bit_cast(bf16x8, of[ks][1]), oc[n][1]);
        }
      }
      if (__all(mx != SENT)) break;
    }
    timed_out |= sp == 64;
    if (t == 0 && g == 0 && tid == 0) __hip_atomic_store((GAS unsigned*)(ws + WS_E + prob * 256), T, RLX_AGENT);
    char* orow = (char*)a.out + (size_t)(t * 128 + 32 * w + i) * OUTROW + prob * 1152 + 144 * g;
#pragma unroll
    for (int m = 0; m < 2; ++m) {
#pragma unroll
      for (int n = 0; n < 4; n += 2) {
        unsigned x0 = pk2(oc[n][m][0], oc[n][m][1]), x1 = pk2(oc[n][m][2], oc[n][m][3]);
        unsigned y0 = pk2(oc[n + 1][m][0], oc[n + 1][m][1]), y1 = pk2(oc[n + 1][m][2], oc[n + 1][m][3]);
        swap16(x0, y0); swap16(x1, y1);
        *(u32x4*)(orow + m * 16 * OUTROW + (2 * (n + (gg & 1)) + (gg >> 1)) * 16) = u32x4{x0, x1, y0, y1};
      }
    }
    unsigned x0 = pk2(oc[4][0][0], oc[4][0][1]), x1 = pk2(oc[4][0][2], oc[4][0][3]);
    unsigned y0 = pk2(oc[4][1][0], oc[4][1][1]), y1 = pk2(oc[4][1][2], oc[4][1][3]);
    swap16(x0, y0); swap16(x1, y1);
    if (gg < 2) *(u32x4*)(orow + gg * 16 * OUTROW + 128) = u32x4{x0, x1, y0, y1};
  }
  (void)timed_out;
}

template <bool ISB> DI void h_path(const Args& a, LAS char* lds, const int w, const int lane, const int t, const int g) {
  constexpr int prob = ISB ? 1 : 0;
  constexpr int C5 = ISB ? 5 : 3;
  constexpr int KVROW = C5 * 16, KVHEAD = 19 * KVROW;
  const int i = lane & 15, gg = lane >> 4;
  const rsrc_t rx = mk_rsrc((const char*)(ISB ? a.xB : a.xA) + (size_t)t * 128 * XROW, 128 * XROW);
  const rsrc_t rw = mk_rsrc(ISB ? a.wB : a.wA, 576 * WROW);
  const unsigned lb = (unsigned)(size_t)lds;
  unsigned vx[2], vw[5], vq[8];
#pragma unroll
  for (int par = 0; par < 2; ++par) vx[par] = (lane >> 3) * XROW + (((lane & 7) ^ ((4 * par + (lane >> 4)) & 7)) * 16);
#pragma unroll
  for (int n = 0; n < 5; ++n) {
    const int nn = (w + 4 * n) * 64 + lane, r64 = nn / 18, cp = nn - 18 * r64;
    const int co = cp < 8 ? 1152 + 16 * cp : cp < 16 ? 2304 + 16 * (cp - 8) : cp == 16 ? 1152 + 128 : 2304 + 128;
    vw[n] = ((r64 & 32) + krow(r64 & 31)) * WROW + 144 * g + co;
  }
  auto issue_kv = [&](int c) {
    const unsigned sb = lb + ((c + 3) & 3) * SLOT;
#pragma unroll
    for (int jj = 0; jj < 4; ++jj) dma16(rx, vx[jj & 1], (4 * w + jj) * 8 * XROW + c * 128, sb + (4 * w + jj) * 1024);
#pragma unroll
    for (int n = 0; n < 5; ++n)
      if (n < 4 || w < 2) dma16(rw, vw[n], c * 64 * WROW, sb + WPART + (w + 4 * n) * 1024);
  };
  issue_kv(0); issue_kv(1); issue_kv(2);
#pragma unroll
  for (int n = 0; n < 8; ++n) {
    const int v = w + 4 * n, ks = v / 5, nn = (v - 5 * ks) * 64 + lane, rho = nn / 10, cp = nn - 10 * rho;
    vq[n] = (ks * 32 + krow(rho)) * WROW + 144 * g + 16 * min(cp, 8);
  }
  auto issue_wq = [&](int grp) {
#pragma unroll
    for (int n = 0; n < 8; ++n)
      if (n < 7 || w < 2) dma16(rw, vq[n], grp * 192 * WROW, lb + L_WQ + grp * 30720 + (w + 4 * n) * 1024);
  };
  if (w < 2) wait_vm<18>(); else wait_vm<16>();
  barrier();
  bf16x8 X[18][2];
  const int xa0 = (32 * w + i) * 128 + ((gg ^ ((i >> 1) & 7)) * 16), xa1 = xa0 ^ 64;
#pragma unroll
  for (int c = 0; c < 9; ++c) {
    const int base = ((c + 3) & 3) * SLOT;
    X[2 * c][0] = lds128(lds, base + xa0); X[2 * c][1] = lds128(lds, base + xa0 + 2048);
    X[2 * c + 1][0] = lds128(lds, base + xa1); X[2 * c + 1][1] = lds128(lds, base + xa1 + 2048);
    if (c <= 5) issue_kv(c + 3); else issue_wq(c - 6);
    if (c <= 5) { if (w < 2) wait_vm<18>(); else wait_vm<16>(); }
    if (c == 6) { if (w < 2) wait_vm<17>(); else wait_vm<15>(); }
    if (c == 7) { if (w < 2) wait_vm<16>(); else wait_vm<14>(); }
    barrier();
  }
  f32x4 qa[5][2];
#pragma unroll
  for (int n = 0; n < 5; ++n) { qa[n][0] = f32x4{0.f, 0.f, 0.f, 0.f}; qa[n][1] = f32x4{0.f, 0.f, 0.f, 0.f}; }
  const int wa2 = L_WQ + (4 * gg + (i >> 2)) * 160 + (i & 3) * 8;
#pragma unroll
  for (int grp = 0; grp < 3; ++grp) {
    if (grp == 0) { if (w < 2) wait_vm<16>(); else wait_vm<14>(); }
    if (grp == 1) { if (w < 2) wait_vm<8>(); else wait_vm<7>(); }
    if (grp == 2) wait_vm<0>();
    lds_arrive(lds, SY_WQ0 + grp, lane);
    lds_wait(lds, SY_WQ0 + grp);
#pragma unroll
    for (int k6 = 0; k6 < 6; ++k6) {
      const int ks = grp * 6 + k6;
#pragma unroll
      for (int n = 0; n < 5; ++n) {
        const bf16x8 wf = lds_tr(lds, wa2 + ks * 5120 + n * 32, 16 * 160);
        qa[n][0] = mfma(wf, X[ks][0], qa[n][0]);
        qa[n][1] = mfma(wf, X[ks][1], qa[n][1]);
      }
    }
  }
  lds_arrive(lds, SY_QDONE, lane);
  unsigned ph[2][ISB ? 18 : 10];
#pragma unroll
  for (int m = 0; m < 2; ++m) {
    f32x4 P[5];
    logits(lds, lane, qa[0][m], qa[1][m], qa[2][m], qa[3][m], qa[4][m], P);
    feature_map<ISB>(P, ph[m]);
  }
  lds_wait(lds, SY_KVIMG);
  f32x4 O[5][2];
  {
    const bool own = (i >> 2) == gg;
#pragma unroll
    for (int n = 0; n < 5; ++n) {
      const int e = 4 * n + (i & 3);
      const int ad = (own && e < 19) ? L_PHI + gg * KVHEAD + e * KVROW : L_ZERO;
      f32x4 c0 = {0.f, 0.f, 0.f, 0.f}, c1 = c0;
#pragma unroll
      for (int s = 0; s < C5; ++s) {
        const bf16x8 af = lds128(lds, ad + s * 16);
        unsigned b0[4], b1[4];
#pragma unroll
        for (int j = 0; j < 4; ++j) {
          b0[j] = 4 * s + j < (ISB ? 18 : 10) ? ph[0][4 * s + j < (ISB ? 18 : 10) ? 4 * s + j : 0] : 0u;
          b1[j] = 4 * s + j < (ISB ? 18 : 10) ? ph[1][4 * s + j < (ISB ? 18 : 10) ? 4 * s + j : 0] : 0u;
        }
        c0 = mfma(af, frag_of(b0[0], b0[1], b0[2], b0[3]), c0);
        c1 = mfma(af, frag_of(b1[0], b1[1], b1[2], b1[3]), c1);
      }
      O[n][0] = c0; O[n][1] = c1;
    }
  }
  LAS char* stg = lds + L_V + w * 4608;
#pragma unroll
  for (int m = 0; m < 2; ++m) {
    const float rc = __builtin_amdgcn_rcpf(O[4][m][2] + 1e-6f);
#pragma unroll
    for (int pp = 0; pp < 9; ++pp) {
      const int e = 2 * pp, cp = 9 * gg + pp;
      const unsigned v = nosent(pk2(O[e >> 2][m][e & 3] * rc, O[(e + 1) >> 2][m][(e + 1) & 3] * rc));
      *(LAS unsigned*)(stg + m * 2304 + (cp >> 2) * 256 + i * 16 + (cp & 3) * 4) = v;
    }
  }
  wait_lgkm();
  const rsrc_t ro = mk_rsrc(a.ws + WS_O + (prob * 16 + t) * O_STRIDE, O_STRIDE);
#pragma unroll
  for (int k = 0; k < 5; ++k) {
    const int n = lane + 64 * k;
    if (n < 288) {
      const u32x4 v = *(LAS u32x4*)(stg + n * 16);
      const int m = n >= 144;
      st_sc1(v, ro, ((2 * w + m) * 72 + 9 * g) * 256 + (n - 144 * m) * 16);
    }
  }
}

__global__ __launch_bounds__(512, 2) void hedgehog_fused(Args a) {
  extern __shared__ __attribute__((aligned(16))) char smem[];
  LAS char* lds = (LAS char*)smem;
  const int b = blockIdx.x, xcd = b & 7, r = b >> 3;
  const int prob = xcd >> 2, t = (xcd & 3) * 4 + (r >> 3), g = r & 7;
  const int wave = __builtin_amdgcn_readfirstlane(threadIdx.x >> 6), lane = threadIdx.x & 63;
  if (wave < 4) {
    if (prob) c_path<true>(a, lds, wave, lane, t, g); else c_path<false>(a, lds, wave, lane, t, g);
  } else {
    if (prob) h_path<true>(a, lds, wave - 4, lane, t, g); else h_path<false>(a, lds, wave - 4, lane, t, g);
  }
}

}

extern "C" void kernel_launch(void* const* d_in, const int* in_sizes, int n_in, void* d_out, int out_size, void* d_ws,
                              size_t ws_size, hipStream_t stream) {
  (void)in_sizes; (void)n_in; (void)out_size; (void)ws_size;
  Args a;
  a.xA = d_in[0]; a.wA = d_in[1]; a.woA = d_in[2]; a.xB = d_in[3]; a.wB = d_in[4]; a.wphi = d_in[5]; a.woB = d_in[6];
  a.out = d_out; a.ws = (char*)d_ws;
  (void)hipFuncSetAttribute((const void*)hedgehog_fused, hipFuncAttributeMaxDynamicSharedMemorySize, LDS_BYTES);
  hedgehog_fused<<<256, 512, LDS_BYTES, stream>>>(a);
}
```

```cpp
#include <hip/hip_runtime.h>
#include <hip/hip_bf16.h>
#include <stdint.h>

typedef __bf16 bf16x8 __attribute__((ext_vector_type(8)));
typedef __bf16 bf16x4 __attribute__((ext_vector_type(4)));
typedef __bf16 bf16x2 __attribute__((ext_vector_type(2)));
typedef float f32x4 __attribute__((ext_vector_type(4)));
typedef float f32x2 __attribute__((ext_vector_type(2)));
typedef unsigned u32x4 __attribute__((ext_vector_type(4)));
typedef unsigned u32x2 __attribute__((ext_vector_type(2)));
#define LAS __attribute__((address_space(3)))
#define GAS __attribute__((address_space(1)))
#define DI __device__ __forceinline__
#define RLX_AGENT __ATOMIC_RELAXED, __HIP_MEMORY_SCOPE_AGENT
#define RLX_WG __ATOMIC_RELAXED, __HIP_MEMORY_SCOPE_WORKGROUP
#define SBAR() __builtin_amdgcn_sched_barrier(0)
typedef __amdgpu_buffer_rsrc_t rsrc_t;

#define DBG 0
#define PROF 0
#define STAMP(n)
#define CSTAMP(n)
#define STAMP_DECL
#define STAMP_OUT(wv)

#define LPROF 0
#define LSTAMP(n)
#define ABL 0
#define ROT 1

namespace {

constexpr int XROW = 1152;
constexpr int WROW = 3456;
constexpr int OUTROW = 2304;
constexpr int SLOT = 34816, WPART = 16384;
constexpr int L_WQ = 0;
constexpr int L_PHI = 92160;
constexpr int L_V = 129024;
constexpr int L_WF = 149504;
constexpr int L_SYNC = 162304;
constexpr int L_ZERO = 162560;
constexpr int LDS_BYTES = 163840;
enum { SY_WQ0 = 0, SY_WQ1, SY_WQ2, SY_QDONE, SY_PHI, SY_KVDONE, SY_KVIMG, SY_WO, SY_PRE };
constexpr int WS_E = 0, WS_F1 = 1024, WS_F2 = WS_F1 + 64, WS_KVP = 65536, KVP_STRIDE = 6144, WS_O = 2097152, O_STRIDE = 147456;
constexpr int WS_USED = WS_O + 32 * O_STRIDE;
constexpr unsigned SENT = 0xFFFFFFFFu;
constexpr unsigned OOB = 0x7FFF0000u;
constexpr int SPIN_G = 1 << 18, SPIN_L = 1 << 22;
constexpr float LOG2E = 1.4426950408889634f;

struct Args {
  const void* xA; const void* wA; const void* woA; const void* xB; const void* wB; const void* wphi; const void* woB;
  void* out; char* ws;
};

DI f32x4 mfma(bf16x8 a, bf16x8 b, f32x4 c) { return __builtin_amdgcn_mfma_f32_16x16x32_bf16(a, b, c, 0, 0, 0); }
typedef short s16x4 __attribute__((ext_vector_type(4)));
DI f32x4 mfma16(u32x2 a, u32x2 b, f32x4 c) {
  return __builtin_amdgcn_mfma_f32_16x16x16bf16_1k(__builtin_bit_cast(s16x4, a), __builtin_bit_cast(s16x4, b), c, 0, 0, 0);
}
DI unsigned pk2(float a, float b) { f32x2 p = {a, b}; return __builtin_bit_cast(unsigned, __builtin_convertvector(p, bf16x2)); }
DI bf16x8 frag_of(unsigned a, unsigned b, unsigned c, unsigned d) { u32x4 v = {a, b, c, d}; return __builtin_bit_cast(bf16x8, v); }
DI bf16x8 pack_pair(f32x4 lo, f32x4 hi) { return frag_of(pk2(lo[0], lo[1]), pk2(lo[2], lo[3]), pk2(hi[0], hi[1]), pk2(hi[2], hi[3])); }
DI bf16x8 lds128(LAS char* l, int off) { return *(LAS bf16x8*)(l + off); }
DI bf16x8 lds_tr(LAS char* l, int off, int second) {
  bf16x4 a = __builtin_amdgcn_ds_read_tr16_b64_v4bf16((LAS bf16x4*)(l + off));
  bf16x4 b = __builtin_amdgcn_ds_read_tr16_b64_v4bf16((LAS bf16x4*)(l + off + second));
  return __builtin_shufflevector(a, b, 0, 1, 2, 3, 4, 5, 6, 7);
}
DI void dma16(rsrc_t r, unsigned voff, unsigned soff, unsigned ldsaddr) {
  asm volatile("s_mov_b32 m0, %0\n\ts_nop 0\n\tbuffer_load_dwordx4 %1, %2, %3 offen lds" ::"s"(ldsaddr), "v"(voff), "s"(r), "s"(soff) : "memory");
}
template <int N> DI void wait_vm() { asm volatile("s_waitcnt vmcnt(%0)" ::"n"(N) : "memory"); }
DI void barrier() { asm volatile("s_waitcnt lgkmcnt(0)\n\ts_barrier" ::: "memory"); }
DI void lds_arrive(LAS char* l, int id, int lane) {
  asm volatile("s_waitcnt lgkmcnt(0)" ::: "memory");
  if (lane == 0) __hip_atomic_fetch_add((LAS unsigned*)(l + L_SYNC) + id, 1u, RLX_WG);
  asm volatile("" ::: "memory");
}
DI unsigned lds_peek(LAS char* l, int id) { return __hip_atomic_load((LAS unsigned*)(l + L_SYNC) + id, RLX_WG); }
DI void lds_wait(LAS char* l, int id, unsigned n = 4u) {
  for (int sp = 0; sp < SPIN_L; ++sp) {
    asm volatile("" ::: "memory");
    unsigned v = lds_peek(l, id);
    if (__builtin_amdgcn_readfirstlane(v) >= n) break;
    __builtin_amdgcn_s_sleep(1);
  }
  asm volatile("" ::: "memory");
}
DI rsrc_t mk_rsrc(const void* p, int bytes) { return __builtin_amdgcn_make_buffer_rsrc((void*)p, 0, bytes, 0x00020000); }
DI void st_sc1(u32x4 v, rsrc_t r, unsigned off) { __builtin_amdgcn_raw_buffer_store_b128(v, r, off, 0, 16); }
DI u32x4 ld_sc1(rsrc_t r, unsigned off, unsigned soff) { return __builtin_amdgcn_raw_buffer_load_b128(r, off, soff, 16); }
DI int opaque(int x) { asm volatile("" : "+v"(x)); return x; }
DI unsigned umax3(unsigned a, unsigned b, unsigned c) { return max(max(a, b), c); }
DI unsigned nosent(unsigned x) { return min(x, 0xFFFFFFFEu); }
DI void swap16(unsigned& a, unsigned& b) { auto r = __builtin_amdgcn_permlane16_swap(a, b, false, false); a = r[0]; b = r[1]; }
DI f32x4 zero4() { return f32x4{0.f, 0.f, 0.f, 0.f}; }

DI int krow(int rho) { return 8 * ((rho >> 2) & 3) + 4 * (rho >> 4) + (rho & 3); }

template <bool ISB> DI void feature_map(const f32x4 (&P)[5], unsigned (&ph)[ISB ? 18 : 10]) {
  float p[18];
#pragma unroll
  for (int f = 0; f < 18; ++f) p[f] = P[f >> 2][f & 3];
  if constexpr (ISB) {
    float mx = p[0], mn = p[0];
#pragma unroll
    for (int f = 1; f < 17; f += 2) { mx = fmaxf(fmaxf(mx, p[f]), p[f + 1]); mn = fminf(fminf(mn, p[f]), p[f + 1]); }
    mx = fmaxf(mx, p[17]); mn = fminf(mn, p[17]);
    const f32x2 cp = {-mx * LOG2E, -mx * LOG2E}, cn = {mn * LOG2E, mn * LOG2E};
    f32x2 lp = {LOG2E, LOG2E}, ln = {-LOG2E, -LOG2E};
    asm volatile("" : "+v"(lp), "+v"(ln));
    float ep[18], en[18];
#pragma unroll
    for (int f = 0; f < 18; f += 2) {
      const f32x2 x = {p[f], p[f + 1]};
      const f32x2 a = __builtin_elementwise_fma(x, lp, cp), b = __builtin_elementwise_fma(x, ln, cn);
      ep[f] = __builtin_amdgcn_exp2f(a[0]); ep[f + 1] = __builtin_amdgcn_exp2f(a[1]);
      en[f] = __builtin_amdgcn_exp2f(b[0]); en[f + 1] = __builtin_amdgcn_exp2f(b[1]);
    }
    f32x2 sp = {ep[0], ep[1]}, sn = {en[0], en[1]};
#pragma unroll
    for (int f = 2; f < 18; f += 2) { sp += f32x2{ep[f], ep[f + 1]}; sn += f32x2{en[f], en[f + 1]}; }
    const float rp = __builtin_amdgcn_rcpf(sp[0] + sp[1]), rn = __builtin_amdgcn_rcpf(sn[0] + sn[1]);
#pragma unroll
    for (int f = 0; f < 18; f += 2) {
      f32x2 a = f32x2{ep[f], ep[f + 1]} * rp, b = f32x2{en[f], en[f + 1]} * rn;
      ph[f >> 1] = pk2(a[0], a[1]);
      ph[9 + (f >> 1)] = pk2(b[0], b[1]);
    }
  } else {
    float y[18];
#pragma unroll
    for (int f = 0; f < 18; ++f)
      y[f] = (fmaxf(p[f], 0.f) + __builtin_amdgcn_exp2f(fminf(p[f], 0.f) * LOG2E)) + 1e-6f;
#pragma unroll
    for (int f = 0; f < 18; f += 2) ph[f >> 1] = pk2(y[f], y[f + 1]);
    ph[9] = 0u;
  }
}

struct WF { bf16x8 a[5][2]; u32x2 h[5]; };
DI void load_wf(LAS char* lds, int lane, WF& f) {
  const int b0 = opaque(L_WF + lane * 16), b1 = opaque(L_WF + 10240 + lane * 8);
#pragma unroll
  for (int t = 0; t < 5; ++t) {
    f.a[t][0] = lds128(lds, b0 + (2 * t) * 1024);
    f.a[t][1] = lds128(lds, b0 + (2 * t + 1) * 1024);
    f.h[t] = *(LAS u32x2*)(lds + b1 + t * 512);
  }
}
DI void logits2(const WF& f, const f32x4 (&t0)[2], const f32x4 (&t1)[2], const f32x4 (&t2)[2], const f32x4 (&t3)[2],
                const f32x4 (&t4)[2], f32x4 (&P)[2][5]) {
  bf16x8 b[2][2];
  u32x2 b2[2];
#pragma unroll
  for (int m = 0; m < 2; ++m) {
    b[m][0] = pack_pair(t0[m], t1[m]);
    b[m][1] = pack_pair(t2[m], t3[m]);
    b2[m] = u32x2{pk2(t4[m][0], t4[m][1]), pk2(t4[m][2], t4[m][3])};
  }
#pragma unroll
  for (int m = 0; m < 2; ++m) {
#pragma unroll
    for (int t = 0; t < 5; ++t) P[m][t] = mfma(f.a[t][0], b[m][0], zero4());
#pragma unroll
    for (int t = 0; t < 5; ++t) P[m][t] = mfma(f.a[t][1], b[m][1], P[m][t]);
#pragma unroll
    for (int t = 0; t < 5; ++t) P[m][t] = mfma16(f.h[t], b2[m], P[m][t]);
  }
}
DI int wf_offset(int h, int d, int f) {
  const int col = 18 * h + d, s = col >> 5, ln = 16 * ((col >> 2) & 3) + 4 * h + (f & 3), tt = f >> 2;
  return s < 2 ? (tt * 2 + s) * 1024 + ln * 16 + (((col >> 4) & 1) * 4 + (col & 3)) * 2 : 10240 + tt * 512 + ln * 8 + (col & 3) * 2;
}

template <bool ISB> DI void c_path(const Args& a, LAS char* lds, const int w, const int lane, const int t, const int g) {
  constexpr int prob = ISB ? 1 : 0;
  constexpr int RP = ISB ? 288 : 160;
  constexpr int C5 = ISB ? 5 : 3;
  constexpr int KVROW = C5 * 16, KVHEAD = 19 * KVROW, NCH = 19 * C5;
  const int tid = w * 64 + lane, i = lane & 15, gg = lane >> 4;
  char* ws = a.ws;
  const rsrc_t rws = mk_rsrc(ws, WS_USED);
  const unsigned kvp_grp = WS_KVP + (prob * 8 + g) * 16 * KVP_STRIDE;
  const unsigned obuf = WS_O + (prob * 16 + t) * O_STRIDE;
  bool timed_out = false;

  STAMP_DECL
  STAMP(0);
  const unsigned T = __hip_atomic_load((GAS unsigned*)(ws + WS_E + prob * 256), RLX_AGENT) + 1u;
  unsigned short wel[6];
  if constexpr (ISB) {
    const unsigned short* wp = (const unsigned short*)a.wphi + g * 4 * 324;
#pragma unroll
    for (int k = 0; k < 6; ++k) { const int idx = tid + 256 * k; wel[k] = (ABL & 8) ? (unsigned short)idx : wp[idx < 1296 ? idx : 0]; }
  }
  if (!(ABL & 4)) {
    const u32x4 s = {SENT, SENT, SENT, SENT};
#pragma unroll
    for (int k = 0; k < (4 * NCH + 255) / 256; ++k) {
      const int n = tid + 256 * k;
      if (n < 4 * NCH) st_sc1(s, rws, kvp_grp + t * KVP_STRIDE + n * 16);
    }
  }
  if (tid < 64) *(LAS unsigned*)(lds + L_SYNC + tid * 4) = 0u;
  if (tid < 32) *(LAS unsigned*)(lds + L_ZERO + tid * 4) = 0u;

  f32x4 acc[9][2];
#pragma unroll
  for (int n = 0; n < 9; ++n) { acc[n][0] = zero4(); acc[n][1] = zero4(); }
  const int xa0 = (32 * w + i) * 128 + ((gg ^ ((i >> 1) & 7)) * 16), xa1 = xa0 ^ 64;
  const int wa = WPART + (4 * gg + (i >> 2)) * 288 + (i & 3) * 8;
  const int wm = wa + ((i & 2) ? 256 : 128);
  bf16x8 wb[2][9], xb[2][2];
  WF wf;
  STAMP(1);
  barrier();
  STAMP(2); CSTAMP(44);
  {
    const int xo = opaque(xa0 + 3 * SLOT), wo = opaque(wa + 3 * SLOT), wmo = opaque(wm + 3 * SLOT);
    xb[0][0] = lds128(lds, xo); xb[0][1] = lds128(lds, xo + 2048);
#pragma unroll
    for (int n = 0; n < 9; ++n) wb[0][n] = lds_tr(lds, n < 8 ? wo + n * 32 + (n >= 4 ? 16 : 0) : wmo, 4608);
  }
#pragma unroll
  for (int c = 0; c < 9; ++c) {
    {
      const int base = ((c + 3) & 3) * SLOT;
      const int xo = opaque(xa1 + base), wo = opaque(wa + base + 9216), wmo = opaque(wm + base + 9216);
      xb[1][0] = lds128(lds, xo); xb[1][1] = lds128(lds, xo + 2048);
      SBAR();
#pragma unroll
      for (int n = 0; n < 9; ++n) {
        if (!(ABL & 2)) wb[1][n] = lds_tr(lds, n < 8 ? wo + n * 32 + (n >= 4 ? 16 : 0) : wmo, 4608); else { wb[1][n] = wb[0][n]; asm volatile("" : "+v"(wb[1][n])); }
        if (!(ABL & 1)) {
          acc[n][0] = mfma(wb[0][n], xb[0][0], acc[n][0]);
          acc[n][1] = mfma(wb[0][n], xb[0][1], acc[n][1]);
        } else asm volatile("" :: "v"(wb[0][n]), "v"(xb[0][0]), "v"(xb[0][1]));
        SBAR();
      }
    }
    if (c == 0) {
      const u32x4 z = {0u, 0u, 0u, 0u};
#pragma unroll
      for (int k = 0; k < 4; ++k) { const int n = tid + 256 * k; if (n < 800) *(LAS u32x4*)(lds + L_WF + n * 16) = z; }
    }
    if (c == 1) {
      if constexpr (ISB) {
#pragma unroll
        for (int k = 0; k < 6; ++k) {
          const int idx = tid + 256 * k;
          if (idx < 1296) {
            const int h = idx / 324, rem = idx - h * 324, d = rem / 18, f = rem - d * 18;
            *(LAS unsigned short*)(lds + L_WF + wf_offset(h, d, f)) = wel[k];
          }
        }
      } else if (tid < 72) {
        const int h = tid / 18, f = tid - h * 18;
        *(LAS unsigned short*)(lds + L_WF + wf_offset(h, f, f)) = (unsigned short)0x3F80;
      }
    }
    if (c == 3) wait_vm<0>();
    if (c == 4 && tid == 0) __hip_atomic_store((GAS unsigned*)(ws + WS_F1 + (prob * 128 + t * 8 + g) * 128), T, RLX_AGENT);
    barrier();
    if (c < 8) {
      const int base = ((c + 4) & 3) * SLOT;
      const int xo = opaque(xa0 + base), wo = opaque(wa + base), wmo = opaque(wm + base);
      xb[0][0] = lds128(lds, xo); xb[0][1] = lds128(lds, xo + 2048);
      SBAR();
#pragma unroll
      for (int n = 0; n < 9; ++n) {
        if (!(ABL & 2)) wb[0][n] = lds_tr(lds, n < 8 ? wo + n * 32 + (n >= 4 ? 16 : 0) : wmo, 4608); else { wb[0][n] = wb[1][n]; asm volatile("" : "+v"(wb[0][n])); }
        if (!(ABL & 1)) {
          acc[n][0] = mfma(wb[1][n], xb[1][0], acc[n][0]);
          acc[n][1] = mfma(wb[1][n], xb[1][1], acc[n][1]);
        } else asm volatile("" :: "v"(wb[1][n]), "v"(xb[1][0]), "v"(xb[1][1]));
        SBAR();
      }
    } else {
      load_wf(lds, lane, wf);
      SBAR();
#pragma unroll
      for (int n = 0; n < 9; ++n) {
        acc[n][0] = mfma(wb[1][n], xb[1][0], acc[n][0]);
        acc[n][1] = mfma(wb[1][n], xb[1][1], acc[n][1]);
      }
    }
  }
  const int lane_ = opaque(lane);
  {
  const int lane = lane_, tid = w * 64 + lane, i = lane & 15, gg = lane >> 4;
  const int xs = ((i >> 2) & 3) << 3;
  GAS unsigned* fp = (GAS unsigned*)(ws + WS_F1 + (prob * 128 + (lane & 15) * 8 + g) * 128);
  const unsigned f1_early = __hip_atomic_load(fp, RLX_AGENT);
  __builtin_amdgcn_s_setprio(2);

  STAMP(3); CSTAMP(45);
  {
    int g8[4];
#pragma unroll
    for (int k = 0; k < 4; ++k) g8[k] = 8 * gg + (k && gg >= k ? 4 : 0);
    f32x4 P[2][5];
    logits2(wf, acc[0], acc[1], acc[2], acc[3], acc[8], P);
#pragma unroll
    for (int m = 0; m < 2; ++m) {
      const int vrow = L_V + (32 * w + 16 * m + i) * 160;
#pragma unroll
      for (int n = 0; n < 5; ++n) {
        const f32x4 v = n < 4 ? acc[4 + n][m] : acc[8][m];
#pragma unroll
        for (int pp = 0; pp < 2; ++pp) {
          const int c0 = (n < 4 ? 16 * n : 56) + 2 * pp, h0 = c0 / 18;
          const int thr = (18 * (h0 + 1) - c0 + 3) / 4;
          const int off = 2 * c0 + 4 * h0 + g8[thr < 4 ? thr : 0];
          if (n < 4 || gg >= 2) *(LAS unsigned*)(lds + vrow + (off ^ xs)) = pk2(v[2 * pp], v[2 * pp + 1]);
        }
      }
      *(LAS unsigned*)(lds + vrow + ((40 * gg + 36) ^ xs)) = 0x00003F80u;
    }
#pragma unroll
    for (int m = 0; m < 2; ++m) {
      unsigned ph[ISB ? 18 : 10];
      feature_map<ISB>(P[m], ph);
      constexpr int NU = ISB ? 9 : 5, UM = NU - 1;
      LAS char* prow = lds + opaque(L_PHI + (32 * w + 16 * m + i) * RP);
      const int pm = (UM * gg * 8) ^ xs, pl = ((4 * UM + gg) * 8) ^ xs;
#pragma unroll
      for (int j = 0; j < NU; ++j)
        *(LAS u32x2*)(prow + (j < UM ? pm ^ (j * 8) : pl)) = u32x2{ph[2 * j], ph[2 * j + 1]};
    }
  }
  STAMP(4);
  lds_arrive(lds, SY_PHI, lane);
  lds_wait(lds, SY_PHI);
  STAMP(5);

  bool f1ok;
  {
    constexpr int UM = ISB ? 8 : 4;
    const int q = i >> 2, p = i & 3;
    const int pa = L_PHI + (4 * gg + q) * RP, va = L_V + (4 * gg + q) * 160;
    const int a_b0 = opaque(pa + ((UM * w + p) ^ gg) * 8), a_b1 = opaque(pa + ((UM * w + 4 + p) ^ gg) * 8);
    const int a_mu = opaque(pa + ((4 * UM + p) ^ gg) * 8);
    const int a_al = opaque(va + ((5 * w + p) ^ gg) * 8), a_la = opaque(va + ((5 * p + 4) ^ gg) * 8);
    bf16x8 al[4], la[4], b0[4], mu[4], b1[4];
#pragma unroll
    for (int ks = 0; ks < 4; ++ks) {
      al[ks] = lds_tr(lds, a_al + ks * 32 * 160, 16 * 160);
      b0[ks] = lds_tr(lds, a_b0 + ks * 32 * RP, 16 * RP);
      la[ks] = lds_tr(lds, a_la + ks * 32 * 160, 16 * 160);
      mu[ks] = lds_tr(lds, a_mu + ks * 32 * RP, 16 * RP);
      if constexpr (ISB) b1[ks] = lds_tr(lds, a_b1 + ks * 32 * RP, 16 * RP);
    }
    SBAR();
    f32x4 d0 = zero4(), d1 = zero4(), d2 = zero4(), d3 = zero4(), d4 = zero4(), d5 = zero4();
#pragma unroll
    for (int ks = 0; ks < 4; ++ks) {
      d0 = mfma(b0[ks], al[ks], d0); d2 = mfma(b0[ks], la[ks], d2);
      d4 = mfma(mu[ks], al[ks], d4); d5 = mfma(mu[ks], la[ks], d5);
      if constexpr (ISB) { d1 = mfma(b1[ks], al[ks], d1); d3 = mfma(b1[ks], la[ks], d3); }
    }
    lds_arrive(lds, SY_KVDONE, lane);
    f1ok = __all(f1_early == T);
    const unsigned mine = kvp_grp + t * KVP_STRIDE + w * KVHEAD;
    const bool lam = (i >> 2) == w && (i & 3) < 3;
    const int el = 16 + (i & 3);
    if constexpr (ISB) {
      unsigned x0 = pk2(d0[0], d0[1]), x1 = pk2(d0[2], d0[3]), y0 = pk2(d1[0], d1[1]), y1 = pk2(d1[2], d1[3]);
      swap16(x0, y0); swap16(x1, y1);
      const int ch = ((gg & 1) << 1) | (gg >> 1);
      st_sc1(u32x4{nosent(x0), nosent(x1), nosent(y0), nosent(y1)}, rws, mine + i * KVROW + ch * 16);
      x0 = pk2(d2[0], d2[1]); x1 = pk2(d2[2], d2[3]); y0 = pk2(d3[0], d3[1]); y1 = pk2(d3[2], d3[3]);
      swap16(x0, y0); swap16(x1, y1);
      if (lam) st_sc1(u32x4{nosent(x0), nosent(x1), nosent(y0), nosent(y1)}, rws, mine + el * KVROW + ch * 16);
    } else {
      unsigned x0 = pk2(d0[0], d0[1]), x1 = pk2(d0[2], d0[3]), y0 = pk2(d2[0], d2[1]), y1 = pk2(d2[2], d2[3]);
      swap16(x0, y0); swap16(x1, y1);
      const bool isl = gg & 1;
      if (!isl || lam) st_sc1(u32x4{nosent(x0), nosent(x1), nosent(y0), nosent(y1)}, rws, mine + (isl ? el : i) * KVROW + (gg >> 1) * 16);
    }
    if (gg == w) st_sc1(u32x4{nosent(pk2(d4[0], d4[1])), nosent(pk2(d4[2], d4[3])), 0u, 0u}, rws, mine + i * KVROW + (C5 - 1) * 16);
    if (gg == w && lam) st_sc1(u32x4{nosent(pk2(d5[0], d5[1])), nosent(pk2(d5[2], d5[3])), 0u, 0u}, rws, mine + el * KVROW + (C5 - 1) * 16);
  }

  STAMP(6); CSTAMP(46);
  {
    {
      const u32x4 s = {SENT, SENT, SENT, SENT};
#pragma unroll
      for (int k = 0; k < 5; ++k) {
        const int n = tid + 256 * k, tile = n / 144, rem = n - 144 * tile;
        if (n < 1152) st_sc1(s, rws, obuf + (tile * 72 + 9 * g) * 256 + rem * 16);
      }
    }
    int sp = 0;
    if (!f1ok) {
      for (; sp < SPIN_G; ++sp) {
        const unsigned f = __hip_atomic_load(fp, RLX_AGENT);
        if (__all(f == T)) break;
      }
    }
    timed_out |= sp == SPIN_G;
    STAMP(7);
    const rsrc_t rkv = mk_rsrc(ws + kvp_grp, 16 * KVP_STRIDE);
    const unsigned poff = lane < 16 ? lane * KVP_STRIDE + w * KVHEAD + 18 * KVROW + (C5 - 1) * 16 : OOB;
    for (sp = 0; sp < SPIN_G; ++sp) {
      asm volatile("" ::: "memory");
      const unsigned f = __builtin_amdgcn_raw_buffer_load_b32(rkv, poff, 0, 16);
      if (__all(f != SENT)) break;
    }
    timed_out |= sp == SPIN_G;
    lds_arrive(lds, SY_PRE, lane);
    STAMP(8);
    constexpr int NR = (NCH + 63) / 64;
    float sum[NR][8];
    unsigned lo1u = 0x00003F80u, hi1u = 0x3F800000u;
    asm volatile("" : "+v"(lo1u), "+v"(hi1u));
    const bf16x2 lo1 = __builtin_bit_cast(bf16x2, lo1u), hi1 = __builtin_bit_cast(bf16x2, hi1u);
    for (sp = 0; sp < 64; ++sp) {
      asm volatile("" ::: "memory");
      u32x4 v[NR][16];
#pragma unroll
      for (int r = 0; r < NR; ++r) {
        const int c = lane + 64 * r;
        const unsigned off = c < NCH ? w * KVHEAD + c * 16 : OOB;
#pragma unroll
        for (int s = 0; s < 16; ++s) { v[r][s] = ld_sc1(rkv, off, s * KVP_STRIDE); SBAR(); }
      }
      SBAR();
      unsigned mx = 0u;
#pragma unroll
      for (int r = 0; r < NR; ++r) {
#pragma unroll
        for (int j = 0; j < 8; ++j) sum[r][j] = 0.f;
#pragma unroll
        for (int s = 0; s < 16; ++s) {
          mx = umax3(mx, v[r][s][0], v[r][s][1]); mx = umax3(mx, v[r][s][2], v[r][s][3]);
#pragma unroll
          for (int j = 0; j < 4; ++j) {
            const unsigned xu = v[r][s][j];
            const bf16x2 x = __builtin_bit_cast(bf16x2, xu);
            sum[r][2 * j] = __builtin_amdgcn_fdot2_f32_bf16(x, lo1, sum[r][2 * j], false);
            sum[r][2 * j + 1] = __builtin_amdgcn_fdot2_f32_bf16(x, hi1, sum[r][2 * j + 1], false);
          }
          asm volatile("" : "+v"(sum[r][0]), "+v"(sum[r][1]), "+v"(sum[r][2]), "+v"(sum[r][3]), "+v"(sum[r][4]), "+v"(sum[r][5]),
                       "+v"(sum[r][6]), "+v"(sum[r][7]));
        }
      }
      if (__all(mx != SENT)) break;
    }
    timed_out |= sp == 64;
    lds_wait(lds, SY_KVDONE);
#pragma unroll
    for (int r = 0; r < NR; ++r) {
      const int c = lane + 64 * r;
      if (c < NCH)
        *(LAS u32x4*)(lds + L_PHI + w * KVHEAD + c * 16) =
            u32x4{pk2(sum[r][0], sum[r][1]), pk2(sum[r][2], sum[r][3]), pk2(sum[r][4], sum[r][5]), pk2(sum[r][6], sum[r][7])};
    }
    lds_arrive(lds, SY_KVIMG, lane);
  }
  STAMP(9);
  if (w == 0) {
    lds_wait(lds, SY_PRE);
    if (lane == 0) __hip_atomic_store((GAS unsigned*)(ws + WS_F2 + (prob * 128 + t * 8 + g) * 128), T, RLX_AGENT);
  }
  __builtin_amdgcn_s_setprio(0);
  {
    const rsrc_t rwo = mk_rsrc(ISB ? a.woB : a.woA, 576 * XROW);
    const unsigned lb = (unsigned)(size_t)lds;
    unsigned vo[5];
#pragma unroll
    for (int n = 0; n < 5; ++n) {
      const int nn = n * 64 + lane, rho = nn / 10, cp = nn - 10 * rho;
      vo[n] = krow(rho) * XROW + 144 * g + 16 * min(cp, 8);
    }
    lds_wait(lds, SY_QDONE);
#pragma unroll
    for (int kk = 0; kk < 5; ++kk) {
      const int ks = w + 4 * kk;
      if (kk < 4 || w < 2) {
#pragma unroll
        for (int n = 0; n < 5; ++n) dma16(rwo, vo[n], ks * 32 * XROW, lb + L_WQ + ks * 5120 + n * 1024);
      }
    }
    wait_vm<0>();
    lds_arrive(lds, SY_WO, lane);
  }
  STAMP(10);

  {
    const rsrc_t ro = mk_rsrc(ws + obuf, O_STRIDE);
    const unsigned poff = lane < 8 ? obuf + ((2 * w + 1) * 72 + 9 * lane + 8) * 256 + 15 * 16 + 12
                                   : lane < 16 ? WS_F2 + (prob * 128 + t * 8 + (lane & 7)) * 128 : OOB;
    int sp;
    bool f2seen = false;
    for (sp = 0; sp < SPIN_G; ++sp) {
      asm volatile("" ::: "memory");
      const unsigned f = __builtin_amdgcn_raw_buffer_load_b32(rws, poff, 0, 16);
      if (f2seen && __all(lane >= 8 || f != SENT)) break;
      f2seen = __all((lane & 56) != 8 || f == T);
    }
    timed_out |= sp == SPIN_G;
    lds_wait(lds, SY_WO);
    __builtin_amdgcn_s_setprio(2);
    STAMP(11);
    f32x4 oc[5][2];
    const int wa2 = opaque(L_WQ + (4 * gg + (i >> 2)) * 160 + (i & 3) * 8), wa3 = opaque(wa2 + 12 * 5120);
    for (sp = 0; sp < 64; ++sp) {
      asm volatile("" ::: "memory");
      u32x4 of[18][2];
#pragma unroll
      for (int ks = 0; ks < 18; ++ks) {
#pragma unroll
        for (int m = 0; m < 2; ++m) { of[ks][m] = ld_sc1(ro, lane * 16, ((2 * w + m) * 72 + 4 * ks) * 256); SBAR(); }
      }
      bf16x8 wq[2][5];
#pragma unroll
      for (int n = 0; n < 5; ++n) { wq[0][n] = lds_tr(lds, wa2 + n * 32, 16 * 160); oc[n][0] = zero4(); oc[n][1] = zero4(); }
      unsigned mx = 0u;
      SBAR();
#pragma unroll
      for (int ks = 0; ks < 18; ++ks) {
#pragma unroll
        for (int n = 0; n < 5; ++n) {
          if (ks < 17) wq[(ks + 1) & 1][n] = lds_tr(lds, (ks + 1 < 12 ? wa2 + (ks + 1) * 5120 : wa3 + (ks + 1 - 12) * 5120) + n * 32, 16 * 160);
          oc[n][0] = mfma(wq[ks & 1][n], __builtin_bit_cast(bf16x8, of[ks][0]), oc[n][0]);
          oc[n][1] = mfma(wq[ks & 1][n], __builtin_bit_cast(bf16x8, of[ks][1]), oc[n][1]);
          if (n < 2) { mx = umax3(mx, of[ks][n][0], of[ks][n][1]); mx = umax3(mx, of[ks][n][2], of[ks][n][3]); }
          SBAR();
        }
      }
      if (__all(mx != SENT)) break;
    }
    timed_out |= sp == 64;
    STAMP(12);
    if (t == 0 && g == 0 && tid == 0) __hip_atomic_store((GAS unsigned*)(ws + WS_E + prob * 256), T, RLX_AGENT);
    char* orow = (char*)a.out + (size_t)(t * 128 + 32 * w + i) * OUTROW + prob * 1152 + 144 * g;
#pragma unroll
    for (int m = 0; m < 2; ++m) {
#pragma unroll
      for (int n = 0; n < 4; n += 2) {
        unsigned x0 = pk2(oc[n][m][0], oc[n][m][1]), x1 = pk2(oc[n][m][2], oc[n][m][3]);
        unsigned y0 = pk2(oc[n + 1][m][0], oc[n + 1][m][1]), y1 = pk2(oc[n + 1][m][2], oc[n + 1][m][3]);
        swap16(x0, y0); swap16(x1, y1);
        *(u32x4*)(orow + m * 16 * OUTROW + (2 * (n + (gg & 1)) + (gg >> 1)) * 16) = u32x4{x0, x1, y0, y1};
      }
    }
    unsigned x0 = pk2(oc[4][0][0], oc[4][0][1]), x1 = pk2(oc[4][0][2], oc[4][0][3]);
    unsigned y0 = pk2(oc[4][1][0], oc[4][1][1]), y1 = pk2(oc[4][1][2], oc[4][1][3]);
    swap16(x0, y0); swap16(x1, y1);
    if (gg < 2) *(u32x4*)(orow + gg * 16 * OUTROW + 128) = u32x4{x0, x1, y0, y1};
  }
  (void)timed_out;
  STAMP(13); CSTAMP(47);
  STAMP_OUT(w);
  }
}

template <bool ISB> DI void h_path(const Args& a, LAS char* lds, const int w, const int lane, const int t, const int g) {
  constexpr int prob = ISB ? 1 : 0;
  constexpr int C5 = ISB ? 5 : 3;
  constexpr int KVROW = C5 * 16, KVHEAD = 19 * KVROW;
  const int i = lane & 15, gg = lane >> 4;
  const rsrc_t rx = mk_rsrc((const char*)(ISB ? a.xB : a.xA) + (size_t)t * 128 * XROW, 128 * XROW);
  const rsrc_t rw = mk_rsrc(ISB ? a.wB : a.wA, 576 * WROW);
  const unsigned lb = (unsigned)(size_t)lds;
  STAMP_DECL
  STAMP(0);
  unsigned vx[2], vw[5], vq[8];
#pragma unroll
  for (int par = 0; par < 2; ++par) vx[par] = (lane >> 3) * XROW + (((lane & 7) ^ ((4 * par + (lane >> 4)) & 7)) * 16);
#pragma unroll
  for (int n = 0; n < 5; ++n) {
    const int nn = (w + 4 * n) * 64 + lane, r64 = nn / 18, cp = nn - 18 * r64;
    const int co = cp < 9 ? 1152 + 16 * cp : 2304 + 16 * (cp - 9);
    vw[n] = ((r64 & 32) + krow(r64 & 31)) * WROW + 144 * g + co;
  }
  const int rot = ROT ? (g + 8 * (t & 3)) % 9 : 0;
  auto kchunk = [&](int c) { const int k = c + rot; return k >= 9 ? k - 9 : k; };
  auto issue_kv = [&](int c) {
    const unsigned sb = lb + ((c + 3) & 3) * SLOT;
    const int kc = kchunk(c);
#pragma unroll
    for (int jj = 0; jj < 4; ++jj) dma16(rx, vx[jj & 1], (4 * w + jj) * 8 * XROW + kc * 128, sb + (4 * w + jj) * 1024);
#pragma unroll
    for (int n = 0; n < 5; ++n)
      if (n < 4 || w < 2) dma16(rw, vw[n], kc * 64 * WROW, sb + WPART + (w + 4 * n) * 1024);
  };
  issue_kv(0); issue_kv(1); issue_kv(2); issue_kv(3);
  STAMP(1);
  if (w < 2) wait_vm<27>(); else wait_vm<24>();
  STAMP(2);
  barrier();
#pragma unroll
  for (int n = 0; n < 8; ++n) {
    const int v = w + 4 * n, ks = v / 5, nn = (v - 5 * ks) * 64 + lane, rho = nn / 10, cp = nn - 10 * rho;
    vq[n] = ((ks & 1) * 32 + krow(rho)) * WROW + 144 * g + 16 * min(cp, 8);
  }
  auto issue_wq = [&](int grp) {
#pragma unroll
    for (int n = 0; n < 8; ++n)
      if (n < 7 || w < 2)
        dma16(rw, vq[n], kchunk(3 * grp + ((w + 4 * n) / 5 >> 1)) * 64 * WROW, lb + L_WQ + grp * 30720 + (w + 4 * n) * 1024);
  };
  bf16x8 X[18][2];
  const int xa0 = (32 * w + i) * 128 + ((gg ^ ((i >> 1) & 7)) * 16), xa1 = xa0 ^ 64;
#pragma unroll
  for (int c = 0; c < 9; ++c) {
    const int base = ((c + 3) & 3) * SLOT;
    const int xo0 = opaque(xa0 + base), xo1 = opaque(xa1 + base);
    X[2 * c][0] = lds128(lds, xo0); X[2 * c][1] = lds128(lds, xo0 + 2048);
    X[2 * c + 1][0] = lds128(lds, xo1); X[2 * c + 1][1] = lds128(lds, xo1 + 2048);
    if (c >= 1 && c <= 5) issue_kv(c + 3);
    if (c >= 6) issue_wq(c - 6);
    LSTAMP(16 + c);
    if (c <= 5) { if (w < 2) wait_vm<18>(); else wait_vm<16>(); }
    if (c == 6) { if (w < 2) wait_vm<17>(); else wait_vm<15>(); }
    if (c == 7) { if (w < 2) wait_vm<16>(); else wait_vm<14>(); }
    LSTAMP(25 + c);
    barrier();
    LSTAMP(34 + c);
  }
  STAMP(3);
  f32x4 qa[5][2];
#pragma unroll
  for (int n = 0; n < 5; ++n) { qa[n][0] = zero4(); qa[n][1] = zero4(); }
  const int wa2 = opaque(L_WQ + (4 * gg + (i >> 2)) * 160 + (i & 3) * 8), wa3 = opaque(wa2 + 12 * 5120);
  bf16x8 wq[2][5];
  if (w < 2) wait_vm<16>(); else wait_vm<14>();
  lds_arrive(lds, SY_WQ0, lane);
  lds_wait(lds, SY_WQ0);
  STAMP(4);
#pragma unroll
  for (int n = 0; n < 5; ++n) wq[0][n] = lds_tr(lds, wa2 + n * 32, 16 * 160);
#pragma unroll
  for (int ks = 0; ks < 18; ++ks) {
    if (ks == 5) { if (w < 2) wait_vm<8>(); else wait_vm<7>(); lds_arrive(lds, SY_WQ1, lane); lds_wait(lds, SY_WQ1); STAMP(5); }
    if (ks == 11) { wait_vm<0>(); lds_arrive(lds, SY_WQ2, lane); lds_wait(lds, SY_WQ2); STAMP(6); }
    SBAR();
#pragma unroll
    for (int n = 0; n < 5; ++n) {
      if (ks < 17) wq[(ks + 1) & 1][n] = lds_tr(lds, (ks + 1 < 12 ? wa2 + (ks + 1) * 5120 : wa3 + (ks + 1 - 12) * 5120) + n * 32, 16 * 160);
      qa[n][0] = mfma(wq[ks & 1][n], X[ks][0], qa[n][0]);
      qa[n][1] = mfma(wq[ks & 1][n], X[ks][1], qa[n][1]);
      SBAR();
    }
  }
  lds_arrive(lds, SY_QDONE, lane);
  WF wf;
  load_wf(lds, lane, wf);
  STAMP(7);
  unsigned ph[2][ISB ? 18 : 10];
  {
    f32x4 P[2][5];
    logits2(wf, qa[0], qa[1], qa[2], qa[3], qa[4], P);
    feature_map<ISB>(P[0], ph[0]);
    feature_map<ISB>(P[1], ph[1]);
  }
  STAMP(8);
  lds_wait(lds, SY_KVIMG);
  STAMP(9);
  __builtin_amdgcn_s_setprio(3);
  f32x4 O[5][2];
  {
    const bool own = (i >> 2) == gg;
    bf16x8 af[5][C5];
#pragma unroll
    for (int n = 0; n < 5; ++n) {
      const int e = 4 * n + (i & 3);
      const int ad = opaque((own && e < 19) ? L_PHI + gg * KVHEAD + e * KVROW : L_ZERO);
#pragma unroll
      for (int s = 0; s < C5; ++s) af[n][s] = lds128(lds, ad + s * 16);
    }
    SBAR();
    constexpr int NP = ISB ? 18 : 10;
#pragma unroll
    for (int s = 0; s < C5; ++s) {
      bf16x8 bq[2];
#pragma unroll
      for (int m = 0; m < 2; ++m) {
        unsigned u[4];
#pragma unroll
        for (int j = 0; j < 4; ++j) u[j] = 4 * s + j < NP ? ph[m][4 * s + j < NP ? 4 * s + j : 0] : 0u;
        bq[m] = frag_of(u[0], u[1], u[2], u[3]);
      }
#pragma unroll
      for (int n = 0; n < 5; ++n) {
#pragma unroll
        for (int m = 0; m < 2; ++m) O[n][m] = mfma(af[n][s], bq[m], s == 0 ? zero4() : O[n][m]);
      }
    }
  }
  STAMP(10);
  LAS char* stg = lds + L_V + w * 4608;
#pragma unroll
  for (int m = 0; m < 2; ++m) {
    const float rc = __builtin_amdgcn_rcpf(O[4][m][2] + 1e-6f);
#pragma unroll
    for (int pp = 0; pp < 9; ++pp) {
      const int e = 2 * pp, cp = 9 * gg + pp;
      const unsigned v = nosent(pk2(O[e >> 2][m][e & 3] * rc, O[(e + 1) >> 2][m][(e + 1) & 3] * rc));
      *(LAS unsigned*)(stg + m * 2304 + (cp >> 2) * 256 + i * 16 + (cp & 3) * 4) = v;
    }
  }
  asm volatile("s_waitcnt lgkmcnt(0)" ::: "memory");
  const rsrc_t ro = mk_rsrc(a.ws + WS_O + (prob * 16 + t) * O_STRIDE, O_STRIDE);
#pragma unroll
  for (int k = 0; k < 5; ++k) {
    const int n = lane + 64 * k;
    if (n < 288) {
      const u32x4 v = *(LAS u32x4*)(stg + n * 16);
      const int m = n >= 144;
      st_sc1(v, ro, ((2 * w + m) * 72 + 9 * g) * 256 + (n - 144 * m) * 16);
    }
  }
  STAMP(11);
  STAMP_OUT(4 + w);
}

__global__ __launch_bounds__(512, 2) void hedgehog_fused(Args a) {
  extern __shared__ __attribute__((aligned(16))) char smem[];
  LAS char* lds = (LAS char*)smem;
  const int b = blockIdx.x, xcd = b & 7, r = b >> 3;
  const int prob = xcd >> 2, t = (xcd & 3) * 4 + (r >> 3), g = r & 7;
  const int wave = __builtin_amdgcn_readfirstlane(threadIdx.x >> 6), lane = threadIdx.x & 63;
  if (wave < 4) {
    if (prob) c_path<true>(a, lds, wave, lane, t, g); else c_path<false>(a, lds, wave, lane, t, g);
  } else {
    if (prob) h_path<true>(a, lds, wave - 4, lane, t, g); else h_path<false>(a, lds, wave - 4, lane, t, g);
  }
}

}

extern "C" void kernel_launch(void* const* d_in, const int* in_sizes, int n_in, void* d_out, int out_size, void* d_ws,
                              size_t ws_size, hipStream_t stream) {
  (void)in_sizes; (void)n_in; (void)out_size; (void)ws_size;
  Args a;
  a.xA = d_in[0]; a.wA = d_in[1]; a.woA = d_in[2]; a.xB = d_in[3]; a.wB = d_in[4]; a.wphi = d_in[5]; a.woB = d_in[6];
  a.out = d_out; a.ws = (char*)d_ws;
  (void)hipFuncSetAttribute((const void*)hedgehog_fused, hipFuncAttributeMaxDynamicSharedMemorySize, LDS_BYTES);
  hedgehog_fused<<<256, 512, LDS_BYTES, stream>>>(a);
}
```

```cpp
#include <hip/hip_runtime.h>
#include <hip/hip_bf16.h>
#include <stdint.h>

typedef __bf16 bf16x8 __attribute__((ext_vector_type(8)));
typedef __bf16 bf16x4 __attribute__((ext_vector_type(4)));
typedef __bf16 bf16x2 __attribute__((ext_vector_type(2)));
typedef float f32x4 __attribute__((ext_vector_type(4)));
typedef float f32x2 __attribute__((ext_vector_type(2)));
typedef unsigned u32x4 __attribute__((ext_vector_type(4)));
typedef unsigned u32x2 __attribute__((ext_vector_type(2)));
#define LAS __attribute__((address_space(3)))
#define GAS __attribute__((address_space(1)))
#define DI __device__ __forceinline__
#define RLX_AGENT __ATOMIC_RELAXED, __HIP_MEMORY_SCOPE_AGENT
#define RLX_WG __ATOMIC_RELAXED, __HIP_MEMORY_SCOPE_WORKGROUP
#define SBAR() __builtin_amdgcn_sched_barrier(0)
typedef __amdgpu_buffer_rsrc_t rsrc_t;

#define DBG 0
#define PROF 0
#define STAMP(n)
#define CSTAMP(n)
#define STAMP_DECL
#define STAMP_OUT(wv)

#define LPROF 0
#define LSTAMP(n)
#define ABL 0
#define ROT 1
#define DELAY 0
#define SWAP 0

namespace {

constexpr int XROW = 1152;
constexpr int WROW = 3456;
constexpr int OUTROW = 2304;
constexpr int SLOT = 34816, WPART = 16384;
constexpr int L_WQ = 0;
constexpr int L_PHI = 92160;
constexpr int L_V = 129024;
constexpr int L_WF = 149504;
constexpr int L_SYNC = 162304;
constexpr int L_ZERO = 162560;
constexpr int LDS_BYTES = 163840;
enum { SY_WQ0 = 0, SY_WQ1, SY_WQ2, SY_QDONE, SY_PHI, SY_KVDONE, SY_KVIMG, SY_WO, SY_PRE };
constexpr int WS_E = 0, WS_F1 = 1024, WS_F2 = WS_F1 + 64, WS_KVP = 65536, KVP_STRIDE = 6144, WS_O = 2097152, O_STRIDE = 147456;
constexpr int WS_USED = WS_O + 32 * O_STRIDE;
constexpr unsigned SENT = 0xFFFFFFFFu;
constexpr unsigned OOB = 0x7FFF0000u;
constexpr int SPIN_G = 1 << 18, SPIN_L = 1 << 22;
constexpr float LOG2E = 1.4426950408889634f;

struct Args {
  const void* xA; const void* wA; const void* woA; const void* xB; const void* wB; const void* wphi; const void* woB;
  void* out; char* ws;
};

DI f32x4 mfma(bf16x8 a, bf16x8 b, f32x4 c) { return __builtin_amdgcn_mfma_f32_16x16x32_bf16(a, b, c, 0, 0, 0); }
DI void mfma_acc(f32x4& c, bf16x8 a, bf16x8 b) { asm volatile("v_mfma_f32_16x16x32_bf16 %0, %1, %2, %0" : "+v"(c) : "v"(a), "v"(b)); }
DI void mfma_fence() { asm volatile("s_nop 7\n\ts_nop 3" ::: "memory"); }
typedef short s16x4 __attribute__((ext_vector_type(4)));
DI f32x4 mfma16(u32x2 a, u32x2 b, f32x4 c) {
  return __builtin_amdgcn_mfma_f32_16x16x16bf16_1k(__builtin_bit_cast(s16x4, a), __builtin_bit_cast(s16x4, b), c, 0, 0, 0);
}
DI unsigned pk2(float a, float b) { f32x2 p = {a, b}; return __builtin_bit_cast(unsigned, __builtin_convertvector(p, bf16x2)); }
DI bf16x8 frag_of(unsigned a, unsigned b, unsigned c, unsigned d) { u32x4 v = {a, b, c, d}; return __builtin_bit_cast(bf16x8, v); }
DI bf16x8 pack_pair(f32x4 lo, f32x4 hi) { return frag_of(pk2(lo[0], lo[1]), pk2(lo[2], lo[3]), pk2(hi[0], hi[1]), pk2(hi[2], hi[3])); }
DI bf16x8 lds128(LAS char* l, int off) { return *(LAS bf16x8*)(l + off); }
DI bf16x8 lds_tr(LAS char* l, int off, int second) {
  bf16x4 a = __builtin_amdgcn_ds_read_tr16_b64_v4bf16((LAS bf16x4*)(l + off));
  bf16x4 b = __builtin_amdgcn_ds_read_tr16_b64_v4bf16((LAS bf16x4*)(l + off + second));
  return __builtin_shufflevector(a, b, 0, 1, 2, 3, 4, 5, 6, 7);
}
DI void dma16(rsrc_t r, unsigned voff, unsigned soff, unsigned ldsaddr) {
  asm volatile("s_mov_b32 m0, %0\n\ts_nop 0\n\tbuffer_load_dwordx4 %1, %2, %3 offen lds" ::"s"(ldsaddr), "v"(voff), "s"(r), "s"(soff) : "memory");
}
template <int N> DI void wait_vm() { asm volatile("s_waitcnt vmcnt(%0)" ::"n"(N) : "memory"); }
DI void barrier() { asm volatile("s_waitcnt lgkmcnt(0)\n\ts_barrier" ::: "memory"); }
DI void lds_arrive(LAS char* l, int id, int) {
  const unsigned addr = (unsigned)(size_t)l + L_SYNC + id * 4, one = 1u;
  unsigned long long save;
  asm volatile("s_waitcnt lgkmcnt(0)\n\ts_mov_b64 %0, exec\n\ts_mov_b64 exec, 1\n\tds_add_u32 %1, %2\n\ts_mov_b64 exec, %0"
               : "=&s"(save) : "v"(addr), "v"(one) : "memory");
}
DI void lds_wait(LAS char* l, int id, unsigned n = 4u) {
  const unsigned addr = (unsigned)(size_t)l + L_SYNC + id * 4;
#pragma unroll 1
  for (int sp = 0; sp < SPIN_L; ++sp) {
    unsigned v;
    asm volatile("ds_read_b32 %0, %1\n\ts_waitcnt lgkmcnt(0)" : "=v"(v) : "v"(addr) : "memory");
    if ((unsigned)__builtin_amdgcn_readfirstlane(v) >= n) break;
    __builtin_amdgcn_s_sleep(1);
  }
}
DI rsrc_t mk_rsrc(const void* p, int bytes) { return __builtin_amdgcn_make_buffer_rsrc((void*)p, 0, bytes, 0x00020000); }
DI void st_sc1(u32x4 v, rsrc_t r, unsigned off) { __builtin_amdgcn_raw_buffer_store_b128(v, r, off, 0, 16); }
DI u32x4 ld_sc1(rsrc_t r, unsigned off, unsigned soff) { return __builtin_amdgcn_raw_buffer_load_b128(r, off, soff, 16); }
DI int opaque(int x) { asm volatile("" : "+v"(x)); return x; }
DI unsigned umax3(unsigned a, unsigned b, unsigned c) { return max(max(a, b), c); }
DI unsigned nosent(unsigned x) { return min(x, 0xFFFFFFFEu); }
DI void swap16(unsigned& a, unsigned& b) { auto r = __builtin_amdgcn_permlane16_swap(a, b, false, false); a = r[0]; b = r[1]; }
DI f32x4 zero4() { return f32x4{0.f, 0.f, 0.f, 0.f}; }

DI int krow(int rho) { return 8 * ((rho >> 2) & 3) + 4 * (rho >> 4) + (rho & 3); }

template <bool ISB> DI void feature_map(const f32x4 (&P)[5], unsigned (&ph)[ISB ? 18 : 10]) {
  float p[18];
#pragma unroll
  for (int f = 0; f < 18; ++f) p[f] = P[f >> 2][f & 3];
  if constexpr (ISB) {
    float mx = p[0], mn = p[0];
#pragma unroll
    for (int f = 1; f < 17; f += 2) { mx = fmaxf(fmaxf(mx, p[f]), p[f + 1]); mn = fminf(fminf(mn, p[f]), p[f + 1]); }
    mx = fmaxf(mx, p[17]); mn = fminf(mn, p[17]);
    const f32x2 cp = {-mx * LOG2E, -mx * LOG2E}, cn = {mn * LOG2E, mn * LOG2E};
    f32x2 lp = {LOG2E, LOG2E}, ln = {-LOG2E, -LOG2E};
    asm volatile("" : "+v"(lp), "+v"(ln));
    float ep[18], en[18];
#pragma unroll
    for (int f = 0; f < 18; f += 2) {
      const f32x2 x = {p[f], p[f + 1]};
      const f32x2 a = __builtin_elementwise_fma(x, lp, cp), b = __builtin_elementwise_fma(x, ln, cn);
      ep[f] = __builtin_amdgcn_exp2f(a[0]); ep[f + 1] = __builtin_amdgcn_exp2f(a[1]);
      en[f] = __builtin_amdgcn_exp2f(b[0]); en[f + 1] = __builtin_amdgcn_exp2f(b[1]);
    }
    f32x2 sp = {ep[0], ep[1]}, sn = {en[0], en[1]};
#pragma unroll
    for (int f = 2; f < 18; f += 2) { sp += f32x2{ep[f], ep[f + 1]}; sn += f32x2{en[f], en[f + 1]}; }
    const float rp = __builtin_amdgcn_rcpf(sp[0] + sp[1]), rn = __builtin_amdgcn_rcpf(sn[0] + sn[1]);
#pragma unroll
    for (int f = 0; f < 18; f += 2) {
      f32x2 a = f32x2{ep[f], ep[f + 1]} * rp, b = f32x2{en[f], en[f + 1]} * rn;
      ph[f >> 1] = pk2(a[0], a[1]);
      ph[9 + (f >> 1)] = pk2(b[0], b[1]);
    }
  } else {
    float y[18];
#pragma unroll
    for (int f = 0; f < 18; ++f)
      y[f] = (fmaxf(p[f], 0.f) + __builtin_amdgcn_exp2f(fminf(p[f], 0.f) * LOG2E)) + 1e-6f;
#pragma unroll
    for (int f = 0; f < 18; f += 2) ph[f >> 1] = pk2(y[f], y[f + 1]);
    ph[9] = 0u;
  }
}

struct WF { bf16x8 a[5][2]; u32x2 h[5]; };
DI void load_wf(LAS char* lds, int lane, WF& f) {
  const int b0 = opaque(L_WF + lane * 16), b1 = opaque(L_WF + 10240 + lane * 8);
#pragma unroll
  for (int t = 0; t < 5; ++t) {
    f.a[t][0] = lds128(lds, b0 + (2 * t) * 1024);
    f.a[t][1] = lds128(lds, b0 + (2 * t + 1) * 1024);
    f.h[t] = *(LAS u32x2*)(lds + b1 + t * 512);
  }
}
DI void logits2(const WF& f, const f32x4 (&t0)[2], const f32x4 (&t1)[2], const f32x4 (&t2)[2], const f32x4 (&t3)[2],
                const f32x4 (&t4)[2], f32x4 (&P)[2][5]) {
  bf16x8 b[2][2];
  u32x2 b2[2];
#pragma unroll
  for (int m = 0; m < 2; ++m) {
    b[m][0] = pack_pair(t0[m], t1[m]);
    b[m][1] = pack_pair(t2[m], t3[m]);
    b2[m] = u32x2{pk2(t4[m][0], t4[m][1]), pk2(t4[m][2], t4[m][3])};
  }
#pragma unroll
  for (int m = 0; m < 2; ++m) {
#pragma unroll
    for (int t = 0; t < 5; ++t) P[m][t] = mfma(f.a[t][0], b[m][0], zero4());
#pragma unroll
    for (int t = 0; t < 5; ++t) P[m][t] = mfma(f.a[t][1], b[m][1], P[m][t]);
#pragma unroll
    for (int t = 0; t < 5; ++t) P[m][t] = mfma16(f.h[t], b2[m], P[m][t]);
  }
}
DI int wf_offset(int h, int d, int f) {
  const int col = 18 * h + d, s = col >> 5, ln = 16 * ((col >> 2) & 3) + 4 * h + (f & 3), tt = f >> 2;
  return s < 2 ? (tt * 2 + s) * 1024 + ln * 16 + (((col >> 4) & 1) * 4 + (col & 3)) * 2 : 10240 + tt * 512 + ln * 8 + (col & 3) * 2;
}

template <bool ISB> DI void c_path(const Args& a, LAS char* lds, const int w, const int lane, const int t, const int g) {
  constexpr int prob = ISB ? 1 : 0;
  constexpr int RP = ISB ? 288 : 160;
  constexpr int C5 = ISB ? 5 : 3;
  constexpr int KVROW = C5 * 16, KVHEAD = 19 * KVROW, NCH = 19 * C5;
  const int tid = w * 64 + lane, i = lane & 15, gg = lane >> 4;
  char* ws = a.ws;
  const rsrc_t rws = mk_rsrc(ws, WS_USED);
  const unsigned kvp_grp = WS_KVP + (prob * 8 + g) * 16 * KVP_STRIDE;
  const unsigned obuf = WS_O + (prob * 16 + t) * O_STRIDE;
  bool timed_out = false;

  STAMP_DECL
  STAMP(0); CSTAMP(43);
  const unsigned T = __hip_atomic_load((GAS unsigned*)(ws + WS_E + prob * 256), RLX_AGENT) + 1u;
  unsigned short wel[6];
  if constexpr (ISB) {
    const unsigned short* wp = (const unsigned short*)a.wphi + g * 4 * 324;
#pragma unroll
    for (int k = 0; k < 6; ++k) { const int idx = tid + 256 * k; wel[k] = (ABL & 8) ? (unsigned short)idx : wp[idx < 1296 ? idx : 0]; }
  }
  if (!(ABL & 4)) {
    const u32x4 s = {SENT, SENT, SENT, SENT};
#pragma unroll
    for (int k = 0; k < (4 * NCH + 255) / 256; ++k) {
      const int n = tid + 256 * k;
      if (n < 4 * NCH) st_sc1(s, rws, kvp_grp + t * KVP_STRIDE + n * 16);
    }
  }
  if (tid < 64) *(LAS unsigned*)(lds + L_SYNC + tid * 4) = 0u;
  if (tid < 32) *(LAS unsigned*)(lds + L_ZERO + tid * 4) = 0u;

  f32x4 acc[9][2];
#pragma unroll
  for (int n = 0; n < 9; ++n) { acc[n][0] = zero4(); acc[n][1] = zero4(); asm volatile("" : "+v"(acc[n][0]), "+v"(acc[n][1])); }
  const int xa0 = (32 * w + i) * 128 + ((gg ^ ((i >> 1) & 7)) * 16), xa1 = xa0 ^ 64;
  const int wa = WPART + (4 * gg + (i >> 2)) * 288 + (i & 3) * 8;
  const int wm = wa + ((i & 2) ? 256 : 128);
  bf16x8 wb[2][9], xb[2][2];
  WF wf;
  STAMP(1);
  barrier();
  STAMP(2); CSTAMP(44);
  {
    const int xo = opaque(xa0 + 3 * SLOT), wo = opaque(wa + 3 * SLOT), wmo = opaque(wm + 3 * SLOT);
    xb[0][0] = lds128(lds, xo); xb[0][1] = lds128(lds, xo + 2048);
#pragma unroll
    for (int n = 0; n < 9; ++n) wb[0][n] = lds_tr(lds, n < 8 ? wo + n * 32 + (n >= 4 ? 16 : 0) : wmo, 4608);
  }
#pragma unroll
  for (int c = 0; c < 9; ++c) {
    {
      const int base = ((c + 3) & 3) * SLOT;
      const int xo = opaque(xa1 + base), wo = opaque(wa + base + 9216), wmo = opaque(wm + base + 9216);
      if (!(ABL & 16)) { xb[1][0] = lds128(lds, xo); xb[1][1] = lds128(lds, xo + 2048); } else { xb[1][0] = xb[0][0]; xb[1][1] = xb[0][1]; }
      SBAR();
#pragma unroll
      for (int n = 0; n < 9; ++n) {
        if (!(ABL & 2)) wb[1][n] = lds_tr(lds, n < 8 ? wo + n * 32 + (n >= 4 ? 16 : 0) : wmo, 4608); else { wb[1][n] = wb[0][n]; asm volatile("" : "+v"(wb[1][n])); }
        if (!(ABL & 1)) {
          mfma_acc(acc[n][0], wb[0][n], xb[0][0]);
          mfma_acc(acc[n][1], wb[0][n], xb[0][1]);
        } else asm volatile("" :: "v"(wb[0][n]), "v"(xb[0][0]), "v"(xb[0][1]));
        SBAR();
      }
    }
    if (c == 0) {
      const u32x4 z = {0u, 0u, 0u, 0u};
#pragma unroll
      for (int k = 0; k < 4; ++k) { const int n = tid + 256 * k; if (n < 800) *(LAS u32x4*)(lds + L_WF + n * 16) = z; }
    }
    if (c == 1) {
      if constexpr (ISB) {
#pragma unroll
        for (int k = 0; k < 6; ++k) {
          const int idx = tid + 256 * k;
          if (idx < 1296) {
            const int h = idx / 324, rem = idx - h * 324, d = rem / 18, f = rem - d * 18;
            *(LAS unsigned short*)(lds + L_WF + wf_offset(h, d, f)) = wel[k];
          }
        }
      } else if (tid < 72) {
        const int h = tid / 18, f = tid - h * 18;
        *(LAS unsigned short*)(lds + L_WF + wf_offset(h, f, f)) = (unsigned short)0x3F80;
      }
    }
    if (c == 3) wait_vm<0>();
    if (c == 4 && tid == 0) __hip_atomic_store((GAS unsigned*)(ws + WS_F1 + (prob * 128 + t * 8 + g) * 128), T, RLX_AGENT);
    barrier();
    if (c < 8) {
      const int base = ((c + 4) & 3) * SLOT;
      const int xo = opaque(xa0 + base), wo = opaque(wa + base), wmo = opaque(wm + base);
      if (!(ABL & 16)) { xb[0][0] = lds128(lds, xo); xb[0][1] = lds128(lds, xo + 2048); } else { xb[0][0] = xb[1][0]; xb[0][1] = xb[1][1]; }
      SBAR();
#pragma unroll
      for (int n = 0; n < 9; ++n) {
        if (!(ABL & 2)) wb[0][n] = lds_tr(lds, n < 8 ? wo + n * 32 + (n >= 4 ? 16 : 0) : wmo, 4608); else { wb[0][n] = wb[1][n]; asm volatile("" : "+v"(wb[0][n])); }
        if (!(ABL & 1)) {
          mfma_acc(acc[n][0], wb[1][n], xb[1][0]);
          mfma_acc(acc[n][1], wb[1][n], xb[1][1]);
        } else asm volatile("" :: "v"(wb[1][n]), "v"(xb[1][0]), "v"(xb[1][1]));
        SBAR();
      }
    } else {
      load_wf(lds, lane, wf);
      SBAR();
#pragma unroll
      for (int n = 0; n < 9; ++n) {
        mfma_acc(acc[n][0], wb[1][n], xb[1][0]);
        mfma_acc(acc[n][1], wb[1][n], xb[1][1]);
      }
      mfma_fence();
    }
  }
  const int lane_ = opaque(lane);
  {
  const int lane = lane_, tid = w * 64 + lane, i = lane & 15, gg = lane >> 4;
  const int xs = ((i >> 2) & 3) << 3;
  GAS unsigned* fp = (GAS unsigned*)(ws + WS_F1 + (prob * 128 + (lane & 15) * 8 + g) * 128);
  const unsigned f1_early = __hip_atomic_load(fp, RLX_AGENT);
  __builtin_amdgcn_s_setprio(2);

  STAMP(3); CSTAMP(45);
  {
    int g8[4];
#pragma unroll
    for (int k = 0; k < 4; ++k) g8[k] = 8 * gg + (k && gg >= k ? 4 : 0);
    f32x4 P[2][5];
    logits2(wf, acc[0], acc[1], acc[2], acc[3], acc[8], P);
#pragma unroll
    for (int m = 0; m < 2; ++m) {
      const int vrow = L_V + (32 * w + 16 * m + i) * 160;
#pragma unroll
      for (int n = 0; n < 5; ++n) {
        const f32x4 v = n < 4 ? acc[4 + n][m] : acc[8][m];
#pragma unroll
        for (int pp = 0; pp < 2; ++pp) {
          const int c0 = (n < 4 ? 16 * n : 56) + 2 * pp, h0 = c0 / 18;
          const int thr = (18 * (h0 + 1) - c0 + 3) / 4;
          const int off = 2 * c0 + 4 * h0 + g8[thr < 4 ? thr : 0];
          if (n < 4 || gg >= 2) *(LAS unsigned*)(lds + vrow + (off ^ xs)) = pk2(v[2 * pp], v[2 * pp + 1]);
        }
      }
      *(LAS unsigned*)(lds + vrow + ((40 * gg + 36) ^ xs)) = 0x00003F80u;
    }
#pragma unroll
    for (int m = 0; m < 2; ++m) {
      unsigned ph[ISB ? 18 : 10];
      feature_map<ISB>(P[m], ph);
      constexpr int NU = ISB ? 9 : 5, UM = NU - 1;
      LAS char* prow = lds + opaque(L_PHI + (32 * w + 16 * m + i) * RP);
      const int pm = (UM * gg * 8) ^ xs, pl = ((4 * UM + gg) * 8) ^ xs;
#pragma unroll
      for (int j = 0; j < NU; ++j)
        *(LAS u32x2*)(prow + (j < UM ? pm ^ (j * 8) : pl)) = u32x2{ph[2 * j], ph[2 * j + 1]};
    }
  }
  STAMP(4);
  lds_arrive(lds, SY_PHI, lane);
  lds_wait(lds, SY_PHI);
  STAMP(5);

  bool f1ok;
  {
    constexpr int UM = ISB ? 8 : 4;
    const int q = i >> 2, p = i & 3;
    const int pa = L_PHI + (4 * gg + q) * RP, va = L_V + (4 * gg + q) * 160;
    const int a_b0 = opaque(pa + ((UM * w + p) ^ gg) * 8), a_b1 = opaque(pa + ((UM * w + 4 + p) ^ gg) * 8);
    const int a_mu = opaque(pa + ((4 * UM + p) ^ gg) * 8);
    const int a_al = opaque(va + ((5 * w + p) ^ gg) * 8), a_la = opaque(va + ((5 * p + 4) ^ gg) * 8);
    bf16x8 al[4], la[4], b0[4], mu[4], b1[4];
#pragma unroll
    for (int ks = 0; ks < 4; ++ks) {
      al[ks] = lds_tr(lds, a_al + ks * 32 * 160, 16 * 160);
      b0[ks] = lds_tr(lds, a_b0 + ks * 32 * RP, 16 * RP);
      la[ks] = lds_tr(lds, a_la + ks * 32 * 160, 16 * 160);
      mu[ks] = lds_tr(lds, a_mu + ks * 32 * RP, 16 * RP);
      if constexpr (ISB) b1[ks] = lds_tr(lds, a_b1 + ks * 32 * RP, 16 * RP);
    }
    SBAR();
    f32x4 d0 = zero4(), d1 = zero4(), d2 = zero4(), d3 = zero4(), d4 = zero4(), d5 = zero4();
#pragma unroll
    for (int ks = 0; ks < 4; ++ks) {
      d0 = mfma(b0[ks], al[ks], d0); d2 = mfma(b0[ks], la[ks], d2);
      d4 = mfma(mu[ks], al[ks], d4); d5 = mfma(mu[ks], la[ks], d5);
      if constexpr (ISB) { d1 = mfma(b1[ks], al[ks], d1); d3 = mfma(b1[ks], la[ks], d3); }
    }
    lds_arrive(lds, SY_KVDONE, lane);
    f1ok = __all(f1_early == T);
    const unsigned mine = kvp_grp + t * KVP_STRIDE + w * KVHEAD;
    const bool lam = (i >> 2) == w && (i & 3) < 3;
    const int el = 16 + (i & 3);
    if constexpr (ISB) {
      unsigned x0 = pk2(d0[0], d0[1]), x1 = pk2(d0[2], d0[3]), y0 = pk2(d1[0], d1[1]), y1 = pk2(d1[2], d1[3]);
      swap16(x0, y0); swap16(x1, y1);
      const int ch = ((gg & 1) << 1) | (gg >> 1);
      st_sc1(u32x4{nosent(x0), nosent(x1), nosent(y0), nosent(y1)}, rws, mine + i * KVROW + ch * 16);
      x0 = pk2(d2[0], d2[1]); x1 = pk2(d2[2], d2[3]); y0 = pk2(d3[0], d3[1]); y1 = pk2(d3[2], d3[3]);
      swap16(x0, y0); swap16(x1, y1);
      if (lam) st_sc1(u32x4{nosent(x0), nosent(x1), nosent(y0), nosent(y1)}, rws, mine + el * KVROW + ch * 16);
    } else {
      unsigned x0 = pk2(d0[0], d0[1]), x1 = pk2(d0[2], d0[3]), y0 = pk2(d2[0], d2[1]), y1 = pk2(d2[2], d2[3]);
      swap16(x0, y0); swap16(x1, y1);
      const bool isl = gg & 1;
      if (!isl || lam) st_sc1(u32x4{nosent(x0), nosent(x1), nosent(y0), nosent(y1)}, rws, mine + (isl ? el : i) * KVROW + (gg >> 1) * 16);
    }
    if (gg == w) st_sc1(u32x4{nosent(pk2(d4[0], d4[1])), nosent(pk2(d4[2], d4[3])), 0u, 0u}, rws, mine + i * KVROW + (C5 - 1) * 16);
    if (gg == w && lam) st_sc1(u32x4{nosent(pk2(d5[0], d5[1])), nosent(pk2(d5[2], d5[3])), 0u, 0u}, rws, mine + el * KVROW + (C5 - 1) * 16);
  }

  STAMP(6); CSTAMP(46);
  {
    {
      const u32x4 s = {SENT, SENT, SENT, SENT};
#pragma unroll
      for (int k = 0; k < 5; ++k) {
        const int n = tid + 256 * k, tile = n / 144, rem = n - 144 * tile;
        if (n < 1152) st_sc1(s, rws, obuf + (tile * 72 + 9 * g) * 256 + rem * 16);
      }
    }
    int sp = 0;
    if (!f1ok) {
      for (; sp < SPIN_G; ++sp) {
        const unsigned f = __hip_atomic_load(fp, RLX_AGENT);
        if (__all(f == T)) break;
      }
    }
    timed_out |= sp == SPIN_G;
    STAMP(7);
    const rsrc_t rkv = mk_rsrc(ws + kvp_grp, 16 * KVP_STRIDE);
    const unsigned poff = lane < 16 ? lane * KVP_STRIDE + w * KVHEAD + 18 * KVROW + (C5 - 1) * 16 : OOB;
    for (sp = 0; sp < SPIN_G; ++sp) {
      asm volatile("" ::: "memory");
      const unsigned f = __builtin_amdgcn_raw_buffer_load_b32(rkv, poff, 0, 16);
      if (__all(f != SENT)) break;
    }
    timed_out |= sp == SPIN_G;
    lds_arrive(lds, SY_PRE, lane);
    STAMP(8);
    constexpr int NG = (NCH + 15) / 16;
    const bf16x8 sel = frag_of(i == 0 ? 0x3F80u : i == 1 ? 0x3F800000u : 0u, i == 2 ? 0x3F80u : i == 3 ? 0x3F800000u : 0u,
                               i == 4 ? 0x3F80u : i == 5 ? 0x3F800000u : 0u, i == 6 ? 0x3F80u : i == 7 ? 0x3F800000u : 0u);
    f32x4 sum[NG];
    for (sp = 0; sp < 64; ++sp) {
      asm volatile("" ::: "memory");
      u32x4 v[NG][4];
#pragma unroll
      for (int cg = 0; cg < NG; ++cg) {
        const unsigned off = 16 * cg + i < NCH ? gg * KVP_STRIDE + w * KVHEAD + (16 * cg + i) * 16 : OOB;
#pragma unroll
        for (int u = 0; u < 4; ++u) { v[cg][u] = ld_sc1(rkv, off, 4 * u * KVP_STRIDE); SBAR(); }
      }
      f32x2 chk = {0.f, 0.f};
#pragma unroll
      for (int cg = 0; cg < NG; ++cg) {
        f32x4 d = zero4();
#pragma unroll
        for (int u = 0; u < 4; ++u) d = mfma(sel, __builtin_bit_cast(bf16x8, v[cg][u]), d);
        sum[cg] = d;
        chk += f32x2{d[0], d[1]} * 0.f + f32x2{d[2], d[3]} * 0.f;
      }
      const float ck = chk[0] + chk[1];
      if (__all(ck == ck)) break;
      unsigned mx = 0u;
#pragma unroll
      for (int cg = 0; cg < NG; ++cg)
#pragma unroll
        for (int u = 0; u < 4; ++u) { mx = umax3(mx, v[cg][u][0], v[cg][u][1]); mx = umax3(mx, v[cg][u][2], v[cg][u][3]); }
      if (__all(mx != SENT)) break;
    }
    timed_out |= sp == 64;
    lds_wait(lds, SY_KVDONE);
#pragma unroll
    for (int cg = 0; cg < NG; ++cg)
      if (gg < 2 && 16 * cg + i < NCH)
        *(LAS u32x2*)(lds + L_PHI + w * KVHEAD + (16 * cg + i) * 16 + gg * 8) = u32x2{pk2(sum[cg][0], sum[cg][1]), pk2(sum[cg][2], sum[cg][3])};
    lds_arrive(lds, SY_KVIMG, lane);
  }
  STAMP(9);
  if (w == 0) {
    lds_wait(lds, SY_PRE);
    if (lane == 0) __hip_atomic_store((GAS unsigned*)(ws + WS_F2 + (prob * 128 + t * 8 + g) * 128), T, RLX_AGENT);
  }
  __builtin_amdgcn_s_setprio(0);
  {
    const rsrc_t rwo = mk_rsrc(ISB ? a.woB : a.woA, 576 * XROW);
    const unsigned lb = (unsigned)(size_t)lds;
    unsigned vo[5];
#pragma unroll
    for (int n = 0; n < 5; ++n) {
      const int nn = n * 64 + lane, rho = nn / 10, cp = nn - 10 * rho;
      vo[n] = krow(rho) * XROW + 144 * g + 16 * min(cp, 8);
    }
    lds_wait(lds, SY_QDONE);
#pragma unroll
    for (int kk = 0; kk < 5; ++kk) {
      const int ks = w + 4 * kk;
      if (kk < 4 || w < 2) {
#pragma unroll
        for (int n = 0; n < 5; ++n) dma16(rwo, vo[n], ks * 32 * XROW, lb + L_WQ + ks * 5120 + n * 1024);
      }
    }
    wait_vm<0>();
    lds_arrive(lds, SY_WO, lane);
  }
  STAMP(10);

  {
    const rsrc_t ro = mk_rsrc(ws + obuf, O_STRIDE);
    const unsigned poff = lane < 8 ? obuf + ((2 * w + 1) * 72 + 9 * lane + 8) * 256 + 15 * 16 + 12
                                   : lane < 16 ? WS_F2 + (prob * 128 + t * 8 + (lane & 7)) * 128 : OOB;
    int sp;
    bool f2seen = false;
    for (sp = 0; sp < SPIN_G; ++sp) {
      asm volatile("" ::: "memory");
      const unsigned f = __builtin_amdgcn_raw_buffer_load_b32(rws, poff, 0, 16);
      if (f2seen && __all(lane >= 8 || f != SENT)) break;
      f2seen = __all((lane & 56) != 8 || f == T);
    }
    timed_out |= sp == SPIN_G;
    lds_wait(lds, SY_WO);
    __builtin_amdgcn_s_setprio(2);
    STAMP(11);
    f32x4 oc[5][2];
    const int wa2 = opaque(L_WQ + (4 * gg + (i >> 2)) * 160 + (i & 3) * 8), wa3 = opaque(wa2 + 12 * 5120);
    for (sp = 0; sp < 64; ++sp) {
      asm volatile("" ::: "memory");
      u32x4 of[18][2];
#pragma unroll
      for (int ks = 0; ks < 18; ++ks) {
#pragma unroll
        for (int m = 0; m < 2; ++m) { of[ks][m] = ld_sc1(ro, lane * 16, ((2 * w + m) * 72 + 4 * ks) * 256); SBAR(); }
      }
      bf16x8 wq[2][5];
#pragma unroll
      for (int n = 0; n < 5; ++n) { wq[0][n] = lds_tr(lds, wa2 + n * 32, 16 * 160); oc[n][0] = zero4(); oc[n][1] = zero4(); }
      unsigned mx = 0u;
#pragma unroll
      for (int n = 0; n < 5; ++n) asm volatile("" : "+v"(oc[n][0]), "+v"(oc[n][1]));
      asm volatile("s_nop 1");
      SBAR();
#pragma unroll
      for (int ks = 0; ks < 18; ++ks) {
#pragma unroll
        for (int n = 0; n < 5; ++n) {
          if (ks < 17) wq[(ks + 1) & 1][n] = lds_tr(lds, (ks + 1 < 12 ? wa2 + (ks + 1) * 5120 : wa3 + (ks + 1 - 12) * 5120) + n * 32, 16 * 160);
          mfma_acc(oc[n][0], wq[ks & 1][n], __builtin_bit_cast(bf16x8, of[ks][0]));
          mfma_acc(oc[n][1], wq[ks & 1][n], __builtin_bit_cast(bf16x8, of[ks][1]));
          if (n < 2) { mx = umax3(mx, of[ks][n][0], of[ks][n][1]); mx = umax3(mx, of[ks][n][2], of[ks][n][3]); }
          SBAR();
        }
      }
      mfma_fence();
      if (__all(mx != SENT)) break;
    }
    timed_out |= sp == 64;
    STAMP(12);
    if (t == 0 && g == 0 && tid == 0) __hip_atomic_store((GAS unsigned*)(ws + WS_E + prob * 256), T, RLX_AGENT);
    char* orow = (char*)a.out + (size_t)(t * 128 + 32 * w + i) * OUTROW + prob * 1152 + 144 * g;
#pragma unroll
    for (int m = 0; m < 2; ++m) {
#pragma unroll
      for (int n = 0; n < 4; n += 2) {
        unsigned x0 = pk2(oc[n][m][0], oc[n][m][1]), x1 = pk2(oc[n][m][2], oc[n][m][3]);
        unsigned y0 = pk2(oc[n + 1][m][0], oc[n + 1][m][1]), y1 = pk2(oc[n + 1][m][2], oc[n + 1][m][3]);
        swap16(x0, y0); swap16(x1, y1);
        *(u32x4*)(orow + m * 16 * OUTROW + (2 * (n + (gg & 1)) + (gg >> 1)) * 16) = u32x4{x0, x1, y0, y1};
      }
    }
    unsigned x0 = pk2(oc[4][0][0], oc[4][0][1]), x1 = pk2(oc[4][0][2], oc[4][0][3]);
    unsigned y0 = pk2(oc[4][1][0], oc[4][1][1]), y1 = pk2(oc[4][1][2], oc[4][1][3]);
    swap16(x0, y0); swap16(x1, y1);
    if (gg < 2) *(u32x4*)(orow + gg * 16 * OUTROW + 128) = u32x4{x0, x1, y0, y1};
  }
  (void)timed_out;
  STAMP(13); CSTAMP(47);
  STAMP_OUT(w);
  }
}

template <bool ISB> DI void h_path(const Args& a, LAS char* lds, const int w, const int lane, const int t, const int g) {
  constexpr int prob = ISB ? 1 : 0;
  constexpr int C5 = ISB ? 5 : 3;
  constexpr int KVROW = C5 * 16, KVHEAD = 19 * KVROW;
  const int i = lane & 15, gg = lane >> 4;
  const rsrc_t rx = mk_rsrc((const char*)(ISB ? a.xB : a.xA) + (size_t)t * 128 * XROW, 128 * XROW);
  const rsrc_t rw = mk_rsrc(ISB ? a.wB : a.wA, 576 * WROW);
  const unsigned lb = (unsigned)(size_t)lds;
  STAMP_DECL
  STAMP(0);
  unsigned vx[2], vw[5], vq[8];
#pragma unroll
  for (int par = 0; par < 2; ++par) vx[par] = (lane >> 3) * XROW + (((lane & 7) ^ ((4 * par + (lane >> 4)) & 7)) * 16);
#pragma unroll
  for (int n = 0; n < 5; ++n) {
    const int nn = (w + 4 * n) * 64 + lane, r64 = nn / 18, cp = nn - 18 * r64;
    const int co = cp < 9 ? 1152 + 16 * cp : 2304 + 16 * (cp - 9);
    vw[n] = ((r64 & 32) + krow(r64 & 31)) * WROW + 144 * g + co;
  }
  const int rot = ROT ? (g + 8 * (t & 3)) % 9 : 0;
  auto kchunk = [&](int c) { const int k = c + rot; return k >= 9 ? k - 9 : k; };
  auto issue_kv = [&](int c) {
    const unsigned sb = lb + ((c + 3) & 3) * SLOT;
    const int kc = kchunk(c);
#pragma unroll
    for (int jj = 0; jj < 4; ++jj) dma16(rx, vx[jj & 1], (4 * w + jj) * 8 * XROW + kc * 128, sb + (4 * w + jj) * 1024);
#pragma unroll
    for (int n = 0; n < 5; ++n)
      if (n < 4 || w < 2) dma16(rw, vw[n], kc * 64 * WROW, sb + WPART + (w + 4 * n) * 1024);
  };
  LSTAMP(12);
  issue_kv(0); LSTAMP(13); issue_kv(1); LSTAMP(14);
  STAMP(1);
  if (w < 2) wait_vm<9>(); else wait_vm<8>();
  STAMP(2);
  barrier();
  issue_kv(2); LSTAMP(15);
#pragma unroll
  for (int n = 0; n < 8; ++n) {
    const int v = w + 4 * n, ks = v / 5, nn = (v - 5 * ks) * 64 + lane, rho = nn / 10, cp = nn - 10 * rho;
    vq[n] = ((ks & 1) * 32 + krow(rho)) * WROW + 144 * g + 16 * min(cp, 8);
  }
  auto issue_wq = [&](int grp) {
#pragma unroll
    for (int n = 0; n < 8; ++n)
      if (n < 7 || w < 2)
        dma16(rw, vq[n], kchunk(3 * grp + ((w + 4 * n) / 5 >> 1)) * 64 * WROW, lb + L_WQ + grp * 30720 + (w + 4 * n) * 1024);
  };
  bf16x8 X[18][2];
  const int xa0 = (32 * w + i) * 128 + ((gg ^ ((i >> 1) & 7)) * 16), xa1 = xa0 ^ 64;
#pragma unroll
  for (int c = 0; c < 9; ++c) {
    const int base = ((c + 3) & 3) * SLOT;
    const int xo0 = opaque(xa0 + base), xo1 = opaque(xa1 + base);
    if (!(ABL & 32) || c == 0) {
    X[2 * c][0] = lds128(lds, xo0); X[2 * c][1] = lds128(lds, xo0 + 2048);
    X[2 * c + 1][0] = lds128(lds, xo1); X[2 * c + 1][1] = lds128(lds, xo1 + 2048);
    } else { X[2 * c][0] = X[0][0]; X[2 * c][1] = X[0][1]; X[2 * c + 1][0] = X[1][0]; X[2 * c + 1][1] = X[1][1]; }
    if (c <= 5) issue_kv(c + 3);
    LSTAMP(16 + c);
    if (c <= 5) { if (w < 2) wait_vm<18>(); else wait_vm<16>(); }
    if (c == 6) { if (w < 2) wait_vm<9>(); else wait_vm<8>(); }
    if (c == 7) wait_vm<0>();
    LSTAMP(25 + c);
    barrier();
    LSTAMP(34 + c);
  }
  issue_wq(0); issue_wq(1); issue_wq(2);
  STAMP(3);
  f32x4 qa[5][2];
#pragma unroll
  for (int n = 0; n < 5; ++n) { qa[n][0] = zero4(); qa[n][1] = zero4(); asm volatile("" : "+v"(qa[n][0]), "+v"(qa[n][1])); }
  const int wa2 = opaque(L_WQ + (4 * gg + (i >> 2)) * 160 + (i & 3) * 8), wa3 = opaque(wa2 + 12 * 5120);
  bf16x8 wq[2][5];
  if (w < 2) wait_vm<16>(); else wait_vm<14>();
  lds_arrive(lds, SY_WQ0, lane);
  lds_wait(lds, SY_WQ0);
  STAMP(4);
#pragma unroll
  for (int n = 0; n < 5; ++n) wq[0][n] = lds_tr(lds, wa2 + n * 32, 16 * 160);
#pragma unroll
  for (int ks = 0; ks < 18; ++ks) {
    if (ks == 5) { if (w < 2) wait_vm<8>(); else wait_vm<7>(); lds_arrive(lds, SY_WQ1, lane); lds_wait(lds, SY_WQ1); STAMP(5); }
    if (ks == 11) { wait_vm<0>(); lds_arrive(lds, SY_WQ2, lane); lds_wait(lds, SY_WQ2); STAMP(6); }
    SBAR();
#pragma unroll
    for (int n = 0; n < 5; ++n) {
      if (ks < 17) wq[(ks + 1) & 1][n] = lds_tr(lds, (ks + 1 < 12 ? wa2 + (ks + 1) * 5120 : wa3 + (ks + 1 - 12) * 5120) + n * 32, 16 * 160);
      mfma_acc(qa[n][0], wq[ks & 1][n], X[ks][0]);
      mfma_acc(qa[n][1], wq[ks & 1][n], X[ks][1]);
      SBAR();
    }
  }
  mfma_fence();
  lds_arrive(lds, SY_QDONE, lane);
  WF wf;
  load_wf(lds, lane, wf);
  STAMP(7);
  unsigned ph[2][ISB ? 18 : 10];
  {
    f32x4 P[2][5];
    logits2(wf, qa[0], qa[1], qa[2], qa[3], qa[4], P);
    feature_map<ISB>(P[0], ph[0]);
    feature_map<ISB>(P[1], ph[1]);
  }
  STAMP(8);
  lds_wait(lds, SY_KVIMG);
  STAMP(9);
  __builtin_amdgcn_s_setprio(3);
  f32x4 O[5][2];
  {
    const bool own = (i >> 2) == gg;
    bf16x8 af[5][C5];
#pragma unroll
    for (int n = 0; n < 5; ++n) {
      const int e = 4 * n + (i & 3);
      const int ad = opaque((own && e < 19) ? L_PHI + gg * KVHEAD + e * KVROW : L_ZERO);
#pragma unroll
      for (int s = 0; s < C5; ++s) af[n][s] = lds128(lds, ad + s * 16);
    }
    SBAR();
    constexpr int NP = ISB ? 18 : 10;
#pragma unroll
    for (int s = 0; s < C5; ++s) {
      bf16x8 bq[2];
#pragma unroll
      for (int m = 0; m < 2; ++m) {
        unsigned u[4];
#pragma unroll
        for (int j = 0; j < 4; ++j) u[j] = 4 * s + j < NP ? ph[m][4 * s + j < NP ? 4 * s + j : 0] : 0u;
        bq[m] = frag_of(u[0], u[1], u[2], u[3]);
      }
#pragma unroll
      for (int n = 0; n < 5; ++n) {
#pragma unroll
        for (int m = 0; m < 2; ++m) O[n][m] = mfma(af[n][s], bq[m], s == 0 ? zero4() : O[n][m]);
      }
    }
  }
  STAMP(10);
  LAS char* stg = lds + L_V + w * 4608;
#pragma unroll
  for (int m = 0; m < 2; ++m) {
    const float rc = __builtin_amdgcn_rcpf(O[4][m][2] + 1e-6f);
#pragma unroll
    for (int pp = 0; pp < 9; ++pp) {
      const int e = 2 * pp, cp = 9 * gg + pp;
      const unsigned v = nosent(pk2(O[e >> 2][m][e & 3] * rc, O[(e + 1) >> 2][m][(e + 1) & 3] * rc));
      *(LAS unsigned*)(stg + m * 2304 + (cp >> 2) * 256 + i * 16 + (cp & 3) * 4) = v;
    }
  }
  asm volatile("s_waitcnt lgkmcnt(0)" ::: "memory");
  const rsrc_t ro = mk_rsrc(a.ws + WS_O + (prob * 16 + t) * O_STRIDE, O_STRIDE);
#pragma unroll
  for (int k = 0; k < 5; ++k) {
    const int n = lane + 64 * k;
    if (n < 288) {
      const u32x4 v = *(LAS u32x4*)(stg + n * 16);
      const int m = n >= 144;
      st_sc1(v, ro, ((2 * w + m) * 72 + 9 * g) * 256 + (n - 144 * m) * 16);
    }
  }
  STAMP(11);
  STAMP_OUT(4 + w);
}

__global__ __launch_bounds__(512, 2) void hedgehog_fused(Args a) {
  extern __shared__ __attribute__((aligned(16))) char smem[];
  LAS char* lds = (LAS char*)smem;
  const int b = blockIdx.x, xcd = b & 7, r = b >> 3;
  const int prob = (xcd >> 2) ^ SWAP, t = (xcd & 3) * 4 + (r >> 3), g = r & 7;
  const int wave = __builtin_amdgcn_readfirstlane(threadIdx.x >> 6), lane = threadIdx.x & 63;
  if (wave < 4) {
    if (prob) c_path<true>(a, lds, wave, lane, t, g); else c_path<false>(a, lds, wave, lane, t, g);
  } else {
    if (prob) h_path<true>(a, lds, wave - 4, lane, t, g); else h_path<false>(a, lds, wave - 4, lane, t, g);
  }
}

}

extern "C" void kernel_launch(void* const* d_in, const int* in_sizes, int n_in, void* d_out, int out_size, void* d_ws,
                              size_t ws_size, hipStream_t stream) {
  (void)in_sizes; (void)n_in; (void)out_size; (void)ws_size;
  Args a;
  a.xA = d_in[0]; a.wA = d_in[1]; a.woA = d_in[2]; a.xB = d_in[3]; a.wB = d_in[4]; a.wphi = d_in[5]; a.woB = d_in[6];
  a.out = d_out; a.ws = (char*)d_ws;
  (void)hipFuncSetAttribute((const void*)hedgehog_fused, hipFuncAttributeMaxDynamicSharedMemorySize, LDS_BYTES);
  hedgehog_fused<<<256, 512, LDS_BYTES, stream>>>(a);
}
```

```cpp
#include <hip/hip_runtime.h>
#include <hip/hip_bf16.h>
#include <stdint.h>

typedef __bf16 bf16x8 __attribute__((ext_vector_type(8)));
typedef __bf16 bf16x4 __attribute__((ext_vector_type(4)));
typedef __bf16 bf16x2 __attribute__((ext_vector_type(2)));
typedef float f32x4 __attribute__((ext_vector_type(4)));
typedef float f32x2 __attribute__((ext_vector_type(2)));
typedef unsigned u32x4 __attribute__((ext_vector_type(4)));
typedef unsigned u32x2 __attribute__((ext_vector_type(2)));
#define LAS __attribute__((address_space(3)))
#define GAS __attribute__((address_space(1)))
#define DI __device__ __forceinline__
#define RLX_AGENT __ATOMIC_RELAXED, __HIP_MEMORY_SCOPE_AGENT
#define RLX_WG __ATOMIC_RELAXED, __HIP_MEMORY_SCOPE_WORKGROUP
#define SBAR() __builtin_amdgcn_sched_barrier(0)
typedef __amdgpu_buffer_rsrc_t rsrc_t;

#define DBG 0
#define PROF 0
#define STAMP(n)
#define CSTAMP(n)
#define STAMP_DECL
#define STAMP_OUT(wv)

#define LPROF 0
#define LSTAMP(n)
#define ABL 0
#define ROT 1
#define DELAY 0
#define OUT_AUX 0
#define SWAP 0

namespace {

constexpr int XROW = 1152;
constexpr int WROW = 3456;
constexpr int OUTROW = 2304;
constexpr int SLOT = 34816, WPART = 16384;
constexpr int L_WQ = 0;
constexpr int L_PHI = 92160;
constexpr int L_V = 129024;
constexpr int L_WF = 149504;
constexpr int L_SYNC = 162304;
constexpr int L_ZERO = 162560;
constexpr int LDS_BYTES = 163840;
enum { SY_WQ0 = 0, SY_WQ1, SY_WQ2, SY_QDONE, SY_PHI, SY_KVDONE, SY_KVIMG, SY_WO, SY_PRE };
constexpr int WS_E = 0, WS_F1 = 1024, WS_F2 = WS_F1 + 64, WS_KVP = 65536, KVP_STRIDE = 6144, WS_O = 2097152, O_STRIDE = 147456;
constexpr int WS_USED = WS_O + 32 * O_STRIDE;
constexpr unsigned SENT = 0xFFFFFFFFu;
constexpr unsigned OOB = 0x7FFF0000u;
constexpr int SPIN_G = 1 << 18, SPIN_L = 1 << 22;
constexpr float LOG2E = 1.4426950408889634f;

struct Args {
  const void* xA; const void* wA; const void* woA; const void* xB; const void* wB; const void* wphi; const void* woB;
  void* out; char* ws;
};

DI f32x4 mfma(bf16x8 a, bf16x8 b, f32x4 c) { return __builtin_amdgcn_mfma_f32_16x16x32_bf16(a, b, c, 0, 0, 0); }
DI void mfma_acc(f32x4& c, bf16x8 a, bf16x8 b) { asm volatile("v_mfma_f32_16x16x32_bf16 %0, %1, %2, %0" : "+v"(c) : "v"(a), "v"(b)); }
DI void mfma_fence() { asm volatile("s_nop 7\n\ts_nop 3" ::: "memory"); }
typedef short s16x4 __attribute__((ext_vector_type(4)));
DI f32x4 mfma16(u32x2 a, u32x2 b, f32x4 c) {
  return __builtin_amdgcn_mfma_f32_16x16x16bf16_1k(__builtin_bit_cast(s16x4, a), __builtin_bit_cast(s16x4, b), c, 0, 0, 0);
}
DI unsigned pk2(float a, float b) { f32x2 p = {a, b}; return __builtin_bit_cast(unsigned, __builtin_convertvector(p, bf16x2)); }
DI bf16x8 frag_of(unsigned a, unsigned b, unsigned c, unsigned d) { u32x4 v = {a, b, c, d}; return __builtin_bit_cast(bf16x8, v); }
DI bf16x8 pack_pair(f32x4 lo, f32x4 hi) { return frag_of(pk2(lo[0], lo[1]), pk2(lo[2], lo[3]), pk2(hi[0], hi[1]), pk2(hi[2], hi[3])); }
DI bf16x8 lds128(LAS char* l, int off) { return *(LAS bf16x8*)(l + off); }
DI bf16x8 lds_tr(LAS char* l, int off, int second) {
  bf16x4 a = __builtin_amdgcn_ds_read_tr16_b64_v4bf16((LAS bf16x4*)(l + off));
  bf16x4 b = __builtin_amdgcn_ds_read_tr16_b64_v4bf16((LAS bf16x4*)(l + off + second));
  return __builtin_shufflevector(a, b, 0, 1, 2, 3, 4, 5, 6, 7);
}
DI void dma16(rsrc_t r, unsigned voff, unsigned soff, unsigned ldsaddr) {
  asm volatile("s_mov_b32 m0, %0\n\ts_nop 0\n\tbuffer_load_dwordx4 %1, %2, %3 offen lds" ::"s"(ldsaddr), "v"(voff), "s"(r), "s"(soff) : "memory");
}
template <int N> DI void wait_vm() { asm volatile("s_waitcnt vmcnt(%0)" ::"n"(N) : "memory"); }
DI void barrier() { asm volatile("s_waitcnt lgkmcnt(0)\n\ts_barrier" ::: "memory"); }
DI void lds_arrive(LAS char* l, int id, int) {
  const unsigned addr = (unsigned)(size_t)l + L_SYNC + id * 4, one = 1u;
  unsigned long long save;
  asm volatile("s_waitcnt lgkmcnt(0)\n\ts_mov_b64 %0, exec\n\ts_mov_b64 exec, 1\n\tds_add_u32 %1, %2\n\ts_mov_b64 exec, %0"
               : "=&s"(save) : "v"(addr), "v"(one) : "memory");
}
DI void lds_wait(LAS char* l, int id, unsigned n = 4u) {
  const unsigned addr = (unsigned)(size_t)l + L_SYNC + id * 4;
#pragma unroll 1
  for (int sp = 0; sp < SPIN_L; ++sp) {
    unsigned v;
    asm volatile("ds_read_b32 %0, %1\n\ts_waitcnt lgkmcnt(0)" : "=v"(v) : "v"(addr) : "memory");
    if ((unsigned)__builtin_amdgcn_readfirstlane(v) >= n) break;
    __builtin_amdgcn_s_sleep(1);
  }
}
DI rsrc_t mk_rsrc(const void* p, int bytes) { return __builtin_amdgcn_make_buffer_rsrc((void*)p, 0, bytes, 0x00020000); }
DI void st_sc1(u32x4 v, rsrc_t r, unsigned off) { __builtin_amdgcn_raw_buffer_store_b128(v, r, off, 0, 16); }
DI u32x4 ld_sc1(rsrc_t r, unsigned off, unsigned soff) { return __builtin_amdgcn_raw_buffer_load_b128(r, off, soff, 16); }
DI int opaque(int x) { asm volatile("" : "+v"(x)); return x; }
DI unsigned umax3(unsigned a, unsigned b, unsigned c) { return max(max(a, b), c); }
DI unsigned nosent(unsigned x) { return min(x, 0xFFFFFFFEu); }
DI void swap16(unsigned& a, unsigned& b) { auto r = __builtin_amdgcn_permlane16_swap(a, b, false, false); a = r[0]; b = r[1]; }
DI f32x4 zero4() { return f32x4{0.f, 0.f, 0.f, 0.f}; }

DI int krow(int rho) { return 8 * ((rho >> 2) & 3) + 4 * (rho >> 4) + (rho & 3); }

template <bool ISB> DI void feature_map(const f32x4 (&P)[5], unsigned (&ph)[ISB ? 18 : 10]) {
  float p[18];
#pragma unroll
  for (int f = 0; f < 18; ++f) p[f] = P[f >> 2][f & 3];
  if constexpr (ISB) {
    float mx = p[0], mn = p[0];
#pragma unroll
    for (int f = 1; f < 17; f += 2) { mx = fmaxf(fmaxf(mx, p[f]), p[f + 1]); mn = fminf(fminf(mn, p[f]), p[f + 1]); }
    mx = fmaxf(mx, p[17]); mn = fminf(mn, p[17]);
    const f32x2 cp = {-mx * LOG2E, -mx * LOG2E}, cn = {mn * LOG2E, mn * LOG2E};
    f32x2 lp = {LOG2E, LOG2E}, ln = {-LOG2E, -LOG2E};
    asm volatile("" : "+v"(lp), "+v"(ln));
    float ep[18], en[18];
#pragma unroll
    for (int f = 0; f < 18; f += 2) {
      const f32x2 x = {p[f], p[f + 1]};
      const f32x2 a = __builtin_elementwise_fma(x, lp, cp), b = __builtin_elementwise_fma(x, ln, cn);
      ep[f] = __builtin_amdgcn_exp2f(a[0]); ep[f + 1] = __builtin_amdgcn_exp2f(a[1]);
      en[f] = __builtin_amdgcn_exp2f(b[0]); en[f + 1] = __builtin_amdgcn_exp2f(b[1]);
    }
    f32x2 sp = {ep[0], ep[1]}, sn = {en[0], en[1]};
#pragma unroll
    for (int f = 2; f < 18; f += 2) { sp += f32x2{ep[f], ep[f + 1]}; sn += f32x2{en[f], en[f + 1]}; }
    const float rp = __builtin_amdgcn_rcpf(sp[0] + sp[1]), rn = __builtin_amdgcn_rcpf(sn[0] + sn[1]);
#pragma unroll
    for (int f = 0; f < 18; f += 2) {
      f32x2 a = f32x2{ep[f], ep[f + 1]} * rp, b = f32x2{en[f], en[f + 1]} * rn;
      ph[f >> 1] = pk2(a[0], a[1]);
      ph[9 + (f >> 1)] = pk2(b[0], b[1]);
    }
  } else {
    float y[18];
#pragma unroll
    for (int f = 0; f < 18; ++f)
      y[f] = (fmaxf(p[f], 0.f) + __builtin_amdgcn_exp2f(fminf(p[f], 0.f) * LOG2E)) + 1e-6f;
#pragma unroll
    for (int f = 0; f < 18; f += 2) ph[f >> 1] = pk2(y[f], y[f + 1]);
    ph[9] = 0u;
  }
}

struct WF { bf16x8 a[5][2]; u32x2 h[5]; };
DI void load_wf(LAS char* lds, int lane, WF& f) {
  const int b0 = opaque(L_WF + lane * 16), b1 = opaque(L_WF + 10240 + lane * 8);
#pragma unroll
  for (int t = 0; t < 5; ++t) {
    f.a[t][0] = lds128(lds, b0 + (2 * t) * 1024);
    f.a[t][1] = lds128(lds, b0 + (2 * t + 1) * 1024);
    f.h[t] = *(LAS u32x2*)(lds + b1 + t * 512);
  }
}
DI void logits2(const WF& f, const f32x4 (&t0)[2], const f32x4 (&t1)[2], const f32x4 (&t2)[2], const f32x4 (&t3)[2],
                const f32x4 (&t4)[2], f32x4 (&P)[2][5]) {
  bf16x8 b[2][2];
  u32x2 b2[2];
#pragma unroll
  for (int m = 0; m < 2; ++m) {
    b[m][0] = pack_pair(t0[m], t1[m]);
    b[m][1] = pack_pair(t2[m], t3[m]);
    b2[m] = u32x2{pk2(t4[m][0], t4[m][1]), pk2(t4[m][2], t4[m][3])};
  }
#pragma unroll
  for (int m = 0; m < 2; ++m) {
#pragma unroll
    for (int t = 0; t < 5; ++t) P[m][t] = mfma(f.a[t][0], b[m][0], zero4());
#pragma unroll
    for (int t = 0; t < 5; ++t) P[m][t] = mfma(f.a[t][1], b[m][1], P[m][t]);
#pragma unroll
    for (int t = 0; t < 5; ++t) P[m][t] = mfma16(f.h[t], b2[m], P[m][t]);
  }
}
DI int wf_offset(int h, int d, int f) {
  const int col = 18 * h + d, s = col >> 5, ln = 16 * ((col >> 2) & 3) + 4 * h + (f & 3), tt = f >> 2;
  return s < 2 ? (tt * 2 + s) * 1024 + ln * 16 + (((col >> 4) & 1) * 4 + (col & 3)) * 2 : 10240 + tt * 512 + ln * 8 + (col & 3) * 2;
}

template <bool ISB> DI void c_path(const Args& a, LAS char* lds, const int w, const int lane, const int t, const int g) {
  constexpr int prob = ISB ? 1 : 0;
  constexpr int RP = ISB ? 288 : 160;
  constexpr int C5 = ISB ? 5 : 3;
  constexpr int KVROW = C5 * 16, KVHEAD = 19 * KVROW, NCH = 19 * C5;
  const int tid = w * 64 + lane, i = lane & 15, gg = lane >> 4;
  char* ws = a.ws;
  const rsrc_t rws = mk_rsrc(ws, WS_USED);
  const unsigned kvp_grp = WS_KVP + (prob * 8 + g) * 16 * KVP_STRIDE;
  const unsigned obuf = WS_O + (prob * 16 + t) * O_STRIDE;
  bool timed_out = false;

  STAMP_DECL
  STAMP(0); CSTAMP(43);
  const unsigned T = __hip_atomic_load((GAS unsigned*)(ws + WS_E + prob * 256), RLX_AGENT) + 1u;
  unsigned short wel[6];
  if constexpr (ISB) {
    const unsigned short* wp = (const unsigned short*)a.wphi + g * 4 * 324;
#pragma unroll
    for (int k = 0; k < 6; ++k) { const int idx = tid + 256 * k; wel[k] = (ABL & 8) ? (unsigned short)idx : wp[idx < 1296 ? idx : 0]; }
  }
  if (!(ABL & 4)) {
    const u32x4 s = {SENT, SENT, SENT, SENT};
#pragma unroll
    for (int k = 0; k < (4 * NCH + 255) / 256; ++k) {
      const int n = tid + 256 * k;
      if (n < 4 * NCH) st_sc1(s, rws, kvp_grp + t * KVP_STRIDE + n * 16);
    }
  }
  if (tid < 64) *(LAS unsigned*)(lds + L_SYNC + tid * 4) = 0u;
  if (tid < 32) *(LAS unsigned*)(lds + L_ZERO + tid * 4) = 0u;

  f32x4 acc[9][2];
#pragma unroll
  for (int n = 0; n < 9; ++n) { acc[n][0] = zero4(); acc[n][1] = zero4(); asm volatile("" : "+v"(acc[n][0]), "+v"(acc[n][1])); }
  const int xa0 = (32 * w + i) * 128 + ((gg ^ ((i >> 1) & 7)) * 16), xa1 = xa0 ^ 64;
  const int wa = WPART + (4 * gg + (i >> 2)) * 288 + (i & 3) * 8;
  const int wm = wa + ((i & 2) ? 256 : 128);
  bf16x8 wb[2][9], xb[2][2];
  WF wf;
  STAMP(1);
  barrier();
  STAMP(2); CSTAMP(44);
  {
    const int xo = opaque(xa0 + 3 * SLOT), wo = opaque(wa + 3 * SLOT), wmo = opaque(wm + 3 * SLOT);
    xb[0][0] = lds128(lds, xo); xb[0][1] = lds128(lds, xo + 2048);
#pragma unroll
    for (int n = 0; n < 9; ++n) wb[0][n] = lds_tr(lds, n < 8 ? wo + n * 32 + (n >= 4 ? 16 : 0) : wmo, 4608);
  }
#pragma unroll
  for (int c = 0; c < 9; ++c) {
    {
      const int base = ((c + 3) & 3) * SLOT;
      const int xo = opaque(xa1 + base), wo = opaque(wa + base + 9216), wmo = opaque(wm + base + 9216);
      if (!(ABL & 16)) { xb[1][0] = lds128(lds, xo); xb[1][1] = lds128(lds, xo + 2048); } else { xb[1][0] = xb[0][0]; xb[1][1] = xb[0][1]; }
      SBAR();
#pragma unroll
      for (int n = 0; n < 9; ++n) {
        if (!(ABL & 2)) wb[1][n] = lds_tr(lds, n < 8 ? wo + n * 32 + (n >= 4 ? 16 : 0) : wmo, 4608); else { wb[1][n] = wb[0][n]; asm volatile("" : "+v"(wb[1][n])); }
        if (!(ABL & 1)) {
          mfma_acc(acc[n][0], wb[0][n], xb[0][0]);
          mfma_acc(acc[n][1], wb[0][n], xb[0][1]);
        } else asm volatile("" :: "v"(wb[0][n]), "v"(xb[0][0]), "v"(xb[0][1]));
        SBAR();
      }
    }
    if (c == 0) {
      const u32x4 z = {0u, 0u, 0u, 0u};
#pragma unroll
      for (int k = 0; k < 4; ++k) { const int n = tid + 256 * k; if (n < 800) *(LAS u32x4*)(lds + L_WF + n * 16) = z; }
    }
    if (c == 1) {
      if constexpr (ISB) {
#pragma unroll
        for (int k = 0; k < 6; ++k) {
          const int idx = tid + 256 * k;
          if (idx < 1296) {
            const int h = idx / 324, rem = idx - h * 324, d = rem / 18, f = rem - d * 18;
            *(LAS unsigned short*)(lds + L_WF + wf_offset(h, d, f)) = wel[k];
          }
        }
      } else if (tid < 72) {
        const int h = tid / 18, f = tid - h * 18;
        *(LAS unsigned short*)(lds + L_WF + wf_offset(h, f, f)) = (unsigned short)0x3F80;
      }
    }
    if (c == 3) wait_vm<0>();
    if (c == 4 && tid == 0) __hip_atomic_store((GAS unsigned*)(ws + WS_F1 + (prob * 128 + t * 8 + g) * 128), T, RLX_AGENT);
    barrier();
    if (c < 8) {
      const int base = ((c + 4) & 3) * SLOT;
      const int xo = opaque(xa0 + base), wo = opaque(wa + base), wmo = opaque(wm + base);
      if (!(ABL & 16)) { xb[0][0] = lds128(lds, xo); xb[0][1] = lds128(lds, xo + 2048); } else { xb[0][0] = xb[1][0]; xb[0][1] = xb[1][1]; }
      SBAR();
#pragma unroll
      for (int n = 0; n < 9; ++n) {
        if (!(ABL & 2)) wb[0][n] = lds_tr(lds, n < 8 ? wo + n * 32 + (n >= 4 ? 16 : 0) : wmo, 4608); else { wb[0][n] = wb[1][n]; asm volatile("" : "+v"(wb[0][n])); }
        if (!(ABL & 1)) {
          mfma_acc(acc[n][0], wb[1][n], xb[1][0]);
          mfma_acc(acc[n][1], wb[1][n], xb[1][1]);
        } else asm volatile("" :: "v"(wb[1][n]), "v"(xb[1][0]), "v"(xb[1][1]));
        SBAR();
      }
    } else {
      load_wf(lds, lane, wf);
      SBAR();
#pragma unroll
      for (int n = 0; n < 9; ++n) {
        mfma_acc(acc[n][0], wb[1][n], xb[1][0]);
        mfma_acc(acc[n][1], wb[1][n], xb[1][1]);
      }
      mfma_fence();
    }
  }
  const int lane_ = opaque(lane);
  {
  const int lane = lane_, tid = w * 64 + lane, i = lane & 15, gg = lane >> 4;
  const int xs = ((i >> 2) & 3) << 3;
  GAS unsigned* fp = (GAS unsigned*)(ws + WS_F1 + (prob * 128 + (lane & 15) * 8 + g) * 128);
  const unsigned f1_early = __hip_atomic_load(fp, RLX_AGENT);
  __builtin_amdgcn_s_setprio(2);

  STAMP(3); CSTAMP(45);
  {
    int g8[4];
#pragma unroll
    for (int k = 0; k < 4; ++k) g8[k] = 8 * gg + (k && gg >= k ? 4 : 0);
    f32x4 P[2][5];
    logits2(wf, acc[0], acc[1], acc[2], acc[3], acc[8], P);
#pragma unroll
    for (int m = 0; m < 2; ++m) {
      const int vrow = L_V + (32 * w + 16 * m + i) * 160;
#pragma unroll
      for (int n = 0; n < 5; ++n) {
        const f32x4 v = n < 4 ? acc[4 + n][m] : acc[8][m];
#pragma unroll
        for (int pp = 0; pp < 2; ++pp) {
          const int c0 = (n < 4 ? 16 * n : 56) + 2 * pp, h0 = c0 / 18;
          const int thr = (18 * (h0 + 1) - c0 + 3) / 4;
          const int off = 2 * c0 + 4 * h0 + g8[thr < 4 ? thr : 0];
          if (n < 4 || gg >= 2) *(LAS unsigned*)(lds + vrow + (off ^ xs)) = pk2(v[2 * pp], v[2 * pp + 1]);
        }
      }
      *(LAS unsigned*)(lds + vrow + ((40 * gg + 36) ^ xs)) = 0x00003F80u;
    }
#pragma unroll
    for (int m = 0; m < 2; ++m) {
      unsigned ph[ISB ? 18 : 10];
      feature_map<ISB>(P[m], ph);
      constexpr int NU = ISB ? 9 : 5, UM = NU - 1;
      LAS char* prow = lds + opaque(L_PHI + (32 * w + 16 * m + i) * RP);
      const int pm = (UM * gg * 8) ^ xs, pl = ((4 * UM + gg) * 8) ^ xs;
#pragma unroll
      for (int j = 0; j < NU; ++j)
        *(LAS u32x2*)(prow + (j < UM ? pm ^ (j * 8) : pl)) = u32x2{ph[2 * j], ph[2 * j + 1]};
    }
  }
  STAMP(4);
  lds_arrive(lds, SY_PHI, lane);
  lds_wait(lds, SY_PHI);
  STAMP(5);

  bool f1ok;
  {
    constexpr int UM = ISB ? 8 : 4;
    const int q = i >> 2, p = i & 3;
    const int pa = L_PHI + (4 * gg + q) * RP, va = L_V + (4 * gg + q) * 160;
    const int a_b0 = opaque(pa + ((UM * w + p) ^ gg) * 8), a_b1 = opaque(pa + ((UM * w + 4 + p) ^ gg) * 8);
    const int a_mu = opaque(pa + ((4 * UM + p) ^ gg) * 8);
    const int a_al = opaque(va + ((5 * w + p) ^ gg) * 8), a_la = opaque(va + ((5 * p + 4) ^ gg) * 8);
    bf16x8 al[4], la[4], b0[4], mu[4], b1[4];
#pragma unroll
    for (int ks = 0; ks < 4; ++ks) {
      al[ks] = lds_tr(lds, a_al + ks * 32 * 160, 16 * 160);
      b0[ks] = lds_tr(lds, a_b0 + ks * 32 * RP, 16 * RP);
      la[ks] = lds_tr(lds, a_la + ks * 32 * 160, 16 * 160);
      mu[ks] = lds_tr(lds, a_mu + ks * 32 * RP, 16 * RP);
      if constexpr (ISB) b1[ks] = lds_tr(lds, a_b1 + ks * 32 * RP, 16 * RP);
    }
    SBAR();
    f32x4 d0 = zero4(), d1 = zero4(), d2 = zero4(), d3 = zero4(), d4 = zero4(), d5 = zero4();
#pragma unroll
    for (int ks = 0; ks < 4; ++ks) {
      d0 = mfma(b0[ks], al[ks], d0); d2 = mfma(b0[ks], la[ks], d2);
      d4 = mfma(mu[ks], al[ks], d4); d5 = mfma(mu[ks], la[ks], d5);
      if constexpr (ISB) { d1 = mfma(b1[ks], al[ks], d1); d3 = mfma(b1[ks], la[ks], d3); }
    }
    lds_arrive(lds, SY_KVDONE, lane);
    f1ok = __all(f1_early == T);
    const unsigned mine = kvp_grp + t * KVP_STRIDE + w * KVHEAD;
    const bool lam = (i >> 2) == w && (i & 3) < 3;
    const int el = 16 + (i & 3);
    if constexpr (ISB) {
      unsigned x0 = pk2(d0[0], d0[1]), x1 = pk2(d0[2], d0[3]), y0 = pk2(d1[0], d1[1]), y1 = pk2(d1[2], d1[3]);
      swap16(x0, y0); swap16(x1, y1);
      const int ch = ((gg & 1) << 1) | (gg >> 1);
      st_sc1(u32x4{nosent(x0), nosent(x1), nosent(y0), nosent(y1)}, rws, mine + i * KVROW + ch * 16);
      x0 = pk2(d2[0], d2[1]); x1 = pk2(d2[2], d2[3]); y0 = pk2(d3[0], d3[1]); y1 = pk2(d3[2], d3[3]);
      swap16(x0, y0); swap16(x1, y1);
      if (lam) st_sc1(u32x4{nosent(x0), nosent(x1), nosent(y0), nosent(y1)}, rws, mine + el * KVROW + ch * 16);
    } else {
      unsigned x0 = pk2(d0[0], d0[1]), x1 = pk2(d0[2], d0[3]), y0 = pk2(d2[0], d2[1]), y1 = pk2(d2[2], d2[3]);
      swap16(x0, y0); swap16(x1, y1);
      const bool isl = gg & 1;
      if (!isl || lam) st_sc1(u32x4{nosent(x0), nosent(x1), nosent(y0), nosent(y1)}, rws, mine + (isl ? el : i) * KVROW + (gg >> 1) * 16);
    }
    if (gg == w) st_sc1(u32x4{nosent(pk2(d4[0], d4[1])), nosent(pk2(d4[2], d4[3])), 0u, 0u}, rws, mine + i * KVROW + (C5 - 1) * 16);
    if (gg == w && lam) st_sc1(u32x4{nosent(pk2(d5[0], d5[1])), nosent(pk2(d5[2], d5[3])), 0u, 0u}, rws, mine + el * KVROW + (C5 - 1) * 16);
  }

  STAMP(6); CSTAMP(46);
  {
    {
      const u32x4 s = {SENT, SENT, SENT, SENT};
#pragma unroll
      for (int k = 0; k < 5; ++k) {
        const int n = tid + 256 * k, tile = n / 144, rem = n - 144 * tile;
        if (n < 1152) st_sc1(s, rws, obuf + (tile * 72 + 9 * g) * 256 + rem * 16);
      }
    }
    int sp = 0;
    if (!f1ok) {
      for (; sp < SPIN_G; ++sp) {
        const unsigned f = __hip_atomic_load(fp, RLX_AGENT);
        if (__all(f == T)) break;
      }
    }
    timed_out |= sp == SPIN_G;
    STAMP(7);
    const rsrc_t rkv = mk_rsrc(ws + kvp_grp, 16 * KVP_STRIDE);
    const unsigned poff = lane < 16 ? lane * KVP_STRIDE + w * KVHEAD + 18 * KVROW + (C5 - 1) * 16 : OOB;
    for (sp = 0; sp < SPIN_G; ++sp) {
      asm volatile("" ::: "memory");
      const unsigned f = __builtin_amdgcn_raw_buffer_load_b32(rkv, poff, 0, 16);
      if (__all(f != SENT)) break;
    }
    timed_out |= sp == SPIN_G;
    lds_arrive(lds, SY_PRE, lane);
    STAMP(8);
    constexpr int NG = (NCH + 15) / 16;
    const bf16x8 sel = frag_of(i == 0 ? 0x3F80u : i == 1 ? 0x3F800000u : 0u, i == 2 ? 0x3F80u : i == 3 ? 0x3F800000u : 0u,
                               i == 4 ? 0x3F80u : i == 5 ? 0x3F800000u : 0u, i == 6 ? 0x3F80u : i == 7 ? 0x3F800000u : 0u);
    f32x4 sum[NG];
    for (sp = 0; sp < 64; ++sp) {
      asm volatile("" ::: "memory");
      u32x4 v[NG][4];
#pragma unroll
      for (int cg = 0; cg < NG; ++cg) {
        const unsigned off = 16 * cg + i < NCH ? gg * KVP_STRIDE + w * KVHEAD + (16 * cg + i) * 16 : OOB;
#pragma unroll
        for (int u = 0; u < 4; ++u) { v[cg][u] = ld_sc1(rkv, off, 4 * u * KVP_STRIDE); SBAR(); }
      }
      f32x2 chk = {0.f, 0.f};
#pragma unroll
      for (int cg = 0; cg < NG; ++cg) {
        f32x4 d = zero4();
#pragma unroll
        for (int u = 0; u < 4; ++u) d = mfma(sel, __builtin_bit_cast(bf16x8, v[cg][u]), d);
        sum[cg] = d;
        chk += f32x2{d[0], d[1]} * 0.f + f32x2{d[2], d[3]} * 0.f;
      }
      const float ck = chk[0] + chk[1];
      if (__all(ck == ck)) break;
      unsigned mx = 0u;
#pragma unroll
      for (int cg = 0; cg < NG; ++cg)
#pragma unroll
        for (int u = 0; u < 4; ++u) { mx = umax3(mx, v[cg][u][0], v[cg][u][1]); mx = umax3(mx, v[cg][u][2], v[cg][u][3]); }
      if (__all(mx != SENT)) break;
    }
    timed_out |= sp == 64;
    lds_wait(lds, SY_KVDONE);
#pragma unroll
    for (int cg = 0; cg < NG; ++cg)
      if (gg < 2 && 16 * cg + i < NCH)
        *(LAS u32x2*)(lds + L_PHI + w * KVHEAD + (16 * cg + i) * 16 + gg * 8) = u32x2{pk2(sum[cg][0], sum[cg][1]), pk2(sum[cg][2], sum[cg][3])};
    lds_arrive(lds, SY_KVIMG, lane);
  }
  STAMP(9);
  if (w == 0) {
    lds_wait(lds, SY_PRE);
    if (lane == 0) __hip_atomic_store((GAS unsigned*)(ws + WS_F2 + (prob * 128 + t * 8 + g) * 128), T, RLX_AGENT);
  }
  __builtin_amdgcn_s_setprio(0);
  {
    const rsrc_t rwo = mk_rsrc(ISB ? a.woB : a.woA, 576 * XROW);
    const unsigned lb = (unsigned)(size_t)lds;
    unsigned vo[5];
#pragma unroll
    for (int n = 0; n < 5; ++n) {
      const int nn = n * 64 + lane, rho = nn / 10, cp = nn - 10 * rho;
      vo[n] = krow(rho) * XROW + 144 * g + 16 * min(cp, 8);
    }
    lds_wait(lds, SY_QDONE);
#pragma unroll
    for (int kk = 0; kk < 5; ++kk) {
      const int ks = w + 4 * kk;
      if (kk < 4 || w < 2) {
#pragma unroll
        for (int n = 0; n < 5; ++n) dma16(rwo, vo[n], ks * 32 * XROW, lb + L_WQ + ks * 5120 + n * 1024);
      }
    }
    wait_vm<0>();
    lds_arrive(lds, SY_WO, lane);
  }
  STAMP(10);

  {
    const rsrc_t ro = mk_rsrc(ws + obuf, O_STRIDE);
    const unsigned poff = lane < 8 ? obuf + ((2 * w + 1) * 72 + 9 * lane + 8) * 256 + 15 * 16 + 12
                                   : lane < 16 ? WS_F2 + (prob * 128 + t * 8 + (lane & 7)) * 128 : OOB;
    int sp;
    bool f2seen = false;
    for (sp = 0; sp < SPIN_G; ++sp) {
      asm volatile("" ::: "memory");
      const unsigned f = __builtin_amdgcn_raw_buffer_load_b32(rws, poff, 0, 16);
      if (f2seen && __all(lane >= 8 || f != SENT)) break;
      f2seen = __all((lane & 56) != 8 || f == T);
    }
    timed_out |= sp == SPIN_G;
    lds_wait(lds, SY_WO);
    __builtin_amdgcn_s_setprio(2);
    STAMP(11);
    f32x4 oc[5][2];
    const int wa2 = opaque(L_WQ + (4 * gg + (i >> 2)) * 160 + (i & 3) * 8), wa3 = opaque(wa2 + 12 * 5120);
    for (sp = 0; sp < 64; ++sp) {
      asm volatile("" ::: "memory");
      u32x4 of[18][2];
#pragma unroll
      for (int ks = 0; ks < 18; ++ks) {
#pragma unroll
        for (int m = 0; m < 2; ++m) { of[ks][m] = ld_sc1(ro, lane * 16, ((2 * w + m) * 72 + 4 * ks) * 256); SBAR(); }
      }
      bf16x8 wq[2][5];
#pragma unroll
      for (int n = 0; n < 5; ++n) { wq[0][n] = lds_tr(lds, wa2 + n * 32, 16 * 160); oc[n][0] = zero4(); oc[n][1] = zero4(); }
      unsigned mx = 0u;
#pragma unroll
      for (int n = 0; n < 5; ++n) asm volatile("" : "+v"(oc[n][0]), "+v"(oc[n][1]));
      asm volatile("s_nop 1");
      SBAR();
#pragma unroll
      for (int ks = 0; ks < 18; ++ks) {
#pragma unroll
        for (int n = 0; n < 5; ++n) {
          if (ks < 17) wq[(ks + 1) & 1][n] = lds_tr(lds, (ks + 1 < 12 ? wa2 + (ks + 1) * 5120 : wa3 + (ks + 1 - 12) * 5120) + n * 32, 16 * 160);
          mfma_acc(oc[n][0], wq[ks & 1][n], __builtin_bit_cast(bf16x8, of[ks][0]));
          mfma_acc(oc[n][1], wq[ks & 1][n], __builtin_bit_cast(bf16x8, of[ks][1]));
          if (n < 2) { mx = umax3(mx, of[ks][n][0], of[ks][n][1]); mx = umax3(mx, of[ks][n][2], of[ks][n][3]); }
          SBAR();
        }
      }
      mfma_fence();
      if (__all(mx != SENT)) break;
    }
    timed_out |= sp == 64;
    STAMP(12);
    if (t == 0 && g == 0 && tid == 0) __hip_atomic_store((GAS unsigned*)(ws + WS_E + prob * 256), T, RLX_AGENT);
    const rsrc_t rout = mk_rsrc(a.out, 2048 * OUTROW);
    const unsigned orow = (t * 128 + 32 * w + i) * OUTROW + prob * 1152 + 144 * g;
#pragma unroll
    for (int m = 0; m < 2; ++m) {
#pragma unroll
      for (int n = 0; n < 4; n += 2) {
        unsigned x0 = pk2(oc[n][m][0], oc[n][m][1]), x1 = pk2(oc[n][m][2], oc[n][m][3]);
        unsigned y0 = pk2(oc[n + 1][m][0], oc[n + 1][m][1]), y1 = pk2(oc[n + 1][m][2], oc[n + 1][m][3]);
        swap16(x0, y0); swap16(x1, y1);
        __builtin_amdgcn_raw_buffer_store_b128(u32x4{x0, x1, y0, y1}, rout, orow + m * 16 * OUTROW + (2 * (n + (gg & 1)) + (gg >> 1)) * 16, 0, OUT_AUX);
      }
    }
    unsigned x0 = pk2(oc[4][0][0], oc[4][0][1]), x1 = pk2(oc[4][0][2], oc[4][0][3]);
    unsigned y0 = pk2(oc[4][1][0], oc[4][1][1]), y1 = pk2(oc[4][1][2], oc[4][1][3]);
    swap16(x0, y0); swap16(x1, y1);
    if (gg < 2) __builtin_amdgcn_raw_buffer_store_b128(u32x4{x0, x1, y0, y1}, rout, orow + gg * 16 * OUTROW + 128, 0, OUT_AUX);
  }
  (void)timed_out;
  STAMP(13); CSTAMP(47);
  STAMP_OUT(w);
  }
}

template <bool ISB> DI void h_path(const Args& a, LAS char* lds, const int w, const int lane, const int t, const int g) {
  constexpr int prob = ISB ? 1 : 0;
  constexpr int C5 = ISB ? 5 : 3;
  constexpr int KVROW = C5 * 16, KVHEAD = 19 * KVROW;
  const int i = lane & 15, gg = lane >> 4;
  const rsrc_t rx = mk_rsrc((const char*)(ISB ? a.xB : a.xA) + (size_t)t * 128 * XROW, 128 * XROW);
  const rsrc_t rw = mk_rsrc(ISB ? a.wB : a.wA, 576 * WROW);
  const unsigned lb = (unsigned)(size_t)lds;
  STAMP_DECL
  STAMP(0);
  unsigned vx[2], vw[5], vq[8];
#pragma unroll
  for (int par = 0; par < 2; ++par) vx[par] = (lane >> 3) * XROW + (((lane & 7) ^ ((4 * par + (lane >> 4)) & 7)) * 16);
#pragma unroll
  for (int n = 0; n < 5; ++n) {
    const int nn = (w + 4 * n) * 64 + lane, r64 = nn / 18, cp = nn - 18 * r64;
    const int co = cp < 9 ? 1152 + 16 * cp : 2304 + 16 * (cp - 9);
    vw[n] = ((r64 & 32) + krow(r64 & 31)) * WROW + 144 * g + co;
  }
  const int rot = ROT ? (g + 8 * (t & 3)) % 9 : 0;
  auto kchunk = [&](int c) { const int k = c + rot; return k >= 9 ? k - 9 : k; };
  auto issue_kv = [&](int c) {
    const unsigned sb = lb + ((c + 3) & 3) * SLOT;
    const int kc = kchunk(c);
#pragma unroll
    for (int jj = 0; jj < 4; ++jj) dma16(rx, vx[jj & 1], (4 * w + jj) * 8 * XROW + kc * 128, sb + (4 * w + jj) * 1024);
#pragma unroll
    for (int n = 0; n < 5; ++n)
      if (n < 4 || w < 2) dma16(rw, vw[n], kc * 64 * WROW, sb + WPART + (w + 4 * n) * 1024);
  };
  LSTAMP(12);
  issue_kv(0); LSTAMP(13); issue_kv(1); LSTAMP(14);
  STAMP(1);
  if (w < 2) wait_vm<9>(); else wait_vm<8>();
  STAMP(2);
  barrier();
  issue_kv(2); LSTAMP(15);
#pragma unroll
  for (int n = 0; n < 8; ++n) {
    const int v = w + 4 * n, ks = v / 5, nn = (v - 5 * ks) * 64 + lane, rho = nn / 10, cp = nn - 10 * rho;
    vq[n] = ((ks & 1) * 32 + krow(rho)) * WROW + 144 * g + 16 * min(cp, 8);
  }
  auto issue_wq = [&](int grp) {
#pragma unroll
    for (int n = 0; n < 8; ++n)
      if (n < 7 || w < 2)
        dma16(rw, vq[n], kchunk(3 * grp + ((w + 4 * n) / 5 >> 1)) * 64 * WROW, lb + L_WQ + grp * 30720 + (w + 4 * n) * 1024);
  };
  bf16x8 X[18][2];
  const int xa0 = (32 * w + i) * 128 + ((gg ^ ((i >> 1) & 7)) * 16), xa1 = xa0 ^ 64;
#pragma unroll
  for (int c = 0; c < 9; ++c) {
    const int base = ((c + 3) & 3) * SLOT;
    const int xo0 = opaque(xa0 + base), xo1 = opaque(xa1 + base);
    if (!(ABL & 32) || c == 0) {
    X[2 * c][0] = lds128(lds, xo0); X[2 * c][1] = lds128(lds, xo0 + 2048);
    X[2 * c + 1][0] = lds128(lds, xo1); X[2 * c + 1][1] = lds128(lds, xo1 + 2048);
    } else { X[2 * c][0] = X[0][0]; X[2 * c][1] = X[0][1]; X[2 * c + 1][0] = X[1][0]; X[2 * c + 1][1] = X[1][1]; }
    if (c <= 5) issue_kv(c + 3);
    LSTAMP(16 + c);
    if (c <= 5) { if (w < 2) wait_vm<18>(); else wait_vm<16>(); }
    if (c == 6) { if (w < 2) wait_vm<9>(); else wait_vm<8>(); }
    if (c == 7) wait_vm<0>();
    LSTAMP(25 + c);
    barrier();
    LSTAMP(34 + c);
  }
  issue_wq(0); issue_wq(1); issue_wq(2);
  STAMP(3);
  f32x4 qa[5][2];
#pragma unroll
  for (int n = 0; n < 5; ++n) { qa[n][0] = zero4(); qa[n][1] = zero4(); asm volatile("" : "+v"(qa[n][0]), "+v"(qa[n][1])); }
  const int wa2 = opaque(L_WQ + (4 * gg + (i >> 2)) * 160 + (i & 3) * 8), wa3 = opaque(wa2 + 12 * 5120);
  bf16x8 wq[2][5];
  if (w < 2) wait_vm<16>(); else wait_vm<14>();
  lds_arrive(lds, SY_WQ0, lane);
  lds_wait(lds, SY_WQ0);
  STAMP(4);
#pragma unroll
  for (int n = 0; n < 5; ++n) wq[0][n] = lds_tr(lds, wa2 + n * 32, 16 * 160);
#pragma unroll
  for (int ks = 0; ks < 18; ++ks) {
    if (ks == 5) { if (w < 2) wait_vm<8>(); else wait_vm<7>(); lds_arrive(lds, SY_WQ1, lane); lds_wait(lds, SY_WQ1); STAMP(5); }
    if (ks == 11) { wait_vm<0>(); lds_arrive(lds, SY_WQ2, lane); lds_wait(lds, SY_WQ2); STAMP(6); }
    SBAR();
#pragma unroll
    for (int n = 0; n < 5; ++n) {
      if (ks < 17) wq[(ks + 1) & 1][n] = lds_tr(lds, (ks + 1 < 12 ? wa2 + (ks + 1) * 5120 : wa3 + (ks + 1 - 12) * 5120) + n * 32, 16 * 160);
      mfma_acc(qa[n][0], wq[ks & 1][n], X[ks][0]);
      mfma_acc(qa[n][1], wq[ks & 1][n], X[ks][1]);
      SBAR();
    }
  }
  mfma_fence();
  lds_arrive(lds, SY_QDONE, lane);
  WF wf;
  load_wf(lds, lane, wf);
  STAMP(7);
  unsigned ph[2][ISB ? 18 : 10];
  {
    f32x4 P[2][5];
    logits2(wf, qa[0], qa[1], qa[2], qa[3], qa[4], P);
    feature_map<ISB>(P[0], ph[0]);
    feature_map<ISB>(P[1], ph[1]);
  }
  STAMP(8);
  lds_wait(lds, SY_KVIMG);
  STAMP(9);
  __builtin_amdgcn_s_setprio(3);
  f32x4 O[5][2];
  {
    const bool own = (i >> 2) == gg;
    bf16x8 af[5][C5];
#pragma unroll
    for (int n = 0; n < 5; ++n) {
      const int e = 4 * n + (i & 3);
      const int ad = opaque((own && e < 19) ? L_PHI + gg * KVHEAD + e * KVROW : L_ZERO);
#pragma unroll
      for (int s = 0; s < C5; ++s) af[n][s] = lds128(lds, ad + s * 16);
    }
    SBAR();
    constexpr int NP = ISB ? 18 : 10;
#pragma unroll
    for (int s = 0; s < C5; ++s) {
      bf16x8 bq[2];
#pragma unroll
      for (int m = 0; m < 2; ++m) {
        unsigned u[4];
#pragma unroll
        for (int j = 0; j < 4; ++j) u[j] = 4 * s + j < NP ? ph[m][4 * s + j < NP ? 4 * s + j : 0] : 0u;
        bq[m] = frag_of(u[0], u[1], u[2], u[3]);
      }
#pragma unroll
      for (int n = 0; n < 5; ++n) {
#pragma unroll
        for (int m = 0; m < 2; ++m) O[n][m] = mfma(af[n][s], bq[m], s == 0 ? zero4() : O[n][m]);
      }
    }
  }
  STAMP(10);
  LAS char* stg = lds + L_V + w * 4608;
#pragma unroll
  for (int m = 0; m < 2; ++m) {
    const float rc = __builtin_amdgcn_rcpf(O[4][m][2] + 1e-6f);
#pragma unroll
    for (int pp = 0; pp < 9; ++pp) {
      const int e = 2 * pp, cp = 9 * gg + pp;
      const unsigned v = nosent(pk2(O[e >> 2][m][e & 3] * rc, O[(e + 1) >> 2][m][(e + 1) & 3] * rc));
      *(LAS unsigned*)(stg + m * 2304 + (cp >> 2) * 256 + i * 16 + (cp & 3) * 4) = v;
    }
  }
  asm volatile("s_waitcnt lgkmcnt(0)" ::: "memory");
  const rsrc_t ro = mk_rsrc(a.ws + WS_O + (prob * 16 + t) * O_STRIDE, O_STRIDE);
#pragma unroll
  for (int k = 0; k < 5; ++k) {
    const int n = lane + 64 * k;
    if (n < 288) {
      const u32x4 v = *(LAS u32x4*)(stg + n * 16);
      const int m = n >= 144;
      st_sc1(v, ro, ((2 * w + m) * 72 + 9 * g) * 256 + (n - 144 * m) * 16);
    }
  }
  STAMP(11);
  STAMP_OUT(4 + w);
}

__global__ __launch_bounds__(512, 2) void hedgehog_fused(Args a) {
  extern __shared__ __attribute__((aligned(16))) char smem[];
  LAS char* lds = (LAS char*)smem;
  const int b = blockIdx.x, xcd = b & 7, r = b >> 3;
  const int prob = (xcd >> 2) ^ SWAP, t = (xcd & 3) * 4 + (r >> 3), g = r & 7;
  const int wave = __builtin_amdgcn_readfirstlane(threadIdx.x >> 6), lane = threadIdx.x & 63;
  if (wave < 4) {
    if (prob) c_path<true>(a, lds, wave, lane, t, g); else c_path<false>(a, lds, wave, lane, t, g);
  } else {
    if (prob) h_path<true>(a, lds, wave - 4, lane, t, g); else h_path<false>(a, lds, wave - 4, lane, t, g);
  }
}

}

extern "C" void kernel_launch(void* const* d_in, const int* in_sizes, int n_in, void* d_out, int out_size, void* d_ws,
                              size_t ws_size, hipStream_t stream) {
  (void)in_sizes; (void)n_in; (void)out_size; (void)ws_size;
  Args a;
  a.xA = d_in[0]; a.wA = d_in[1]; a.woA = d_in[2]; a.xB = d_in[3]; a.wB = d_in[4]; a.wphi = d_in[5]; a.woB = d_in[6];
  a.out = d_out; a.ws = (char*)d_ws;
  (void)hipFuncSetAttribute((const void*)hedgehog_fused, hipFuncAttributeMaxDynamicSharedMemorySize, LDS_BYTES);
  hedgehog_fused<<<256, 512, LDS_BYTES, stream>>>(a);
}
```

```cpp
#include <hip/hip_runtime.h>
#include <hip/hip_bf16.h>
#include <stdint.h>

typedef __bf16 bf16x8 __attribute__((ext_vector_type(8)));
typedef __bf16 bf16x4 __attribute__((ext_vector_type(4)));
typedef __bf16 bf16x2 __attribute__((ext_vector_type(2)));
typedef float f32x4 __attribute__((ext_vector_type(4)));
typedef float f32x2 __attribute__((ext_vector_type(2)));
typedef unsigned u32x4 __attribute__((ext_vector_type(4)));
typedef unsigned u32x2 __attribute__((ext_vector_type(2)));
#define LAS __attribute__((address_space(3)))
#define GAS __attribute__((address_space(1)))
#define DI __device__ __forceinline__
#define RLX_AGENT __ATOMIC_RELAXED, __HIP_MEMORY_SCOPE_AGENT
#define RLX_WG __ATOMIC_RELAXED, __HIP_MEMORY_SCOPE_WORKGROUP
#define SBAR() __builtin_amdgcn_sched_barrier(0)
typedef __amdgpu_buffer_rsrc_t rsrc_t;

#define DBG 0
#define PROF 0
#define STAMP(n)
#define CSTAMP(n)
#define STAMP_DECL
#define STAMP_OUT(wv)

#define LPROF 0
#define LSTAMP(n)
#define ABL 0
#define ROT 1
#define DELAY 0
#define OUT_AUX 0
#define CPF 0
#define SWAP 0

namespace {

constexpr int XROW = 1152;
constexpr int WROW = 3456;
constexpr int OUTROW = 2304;
constexpr int SLOT = 34816, WPART = 16384;
constexpr int L_WQ = 0;
constexpr int L_PHI = 92160;
constexpr int L_V = 129024;
constexpr int L_WF = 149504;
constexpr int L_SYNC = 162304;
constexpr int L_ZERO = 162560;
constexpr int LDS_BYTES = 163840;
enum { SY_WQ0 = 0, SY_WQ1, SY_WQ2, SY_QDONE, SY_PHI, SY_KVDONE, SY_KVIMG, SY_WO, SY_PRE };
constexpr int WS_E = 0, WS_F1 = 1024, WS_KVP = 65536, KVP_STRIDE = 6144, WS_O = 2097152, O_STRIDE = 147456;
constexpr int WS_USED = WS_O + 32 * O_STRIDE;
constexpr unsigned SENT = 0xFFFFFFFFu;
constexpr unsigned OOB = 0x7FFF0000u;
constexpr int SPIN_G = 1 << 18, SPIN_L = 1 << 22;
constexpr float LOG2E = 1.4426950408889634f;

struct Args {
  const void* xA; const void* wA; const void* woA; const void* xB; const void* wB; const void* wphi; const void* woB;
  void* out; char* ws;
};

DI f32x4 mfma(bf16x8 a, bf16x8 b, f32x4 c) { return __builtin_amdgcn_mfma_f32_16x16x32_bf16(a, b, c, 0, 0, 0); }
DI void mfma_acc(f32x4& c, bf16x8 a, bf16x8 b) { asm volatile("v_mfma_f32_16x16x32_bf16 %0, %1, %2, %0" : "+v"(c) : "v"(a), "v"(b)); }
DI void mfma_fence() { asm volatile("s_nop 7\n\ts_nop 3" ::: "memory"); }
typedef short s16x4 __attribute__((ext_vector_type(4)));
DI f32x4 mfma16(u32x2 a, u32x2 b, f32x4 c) {
  return __builtin_amdgcn_mfma_f32_16x16x16bf16_1k(__builtin_bit_cast(s16x4, a), __builtin_bit_cast(s16x4, b), c, 0, 0, 0);
}
DI unsigned pk2(float a, float b) { f32x2 p = {a, b}; return __builtin_bit_cast(unsigned, __builtin_convertvector(p, bf16x2)); }
DI bf16x8 frag_of(unsigned a, unsigned b, unsigned c, unsigned d) { u32x4 v = {a, b, c, d}; return __builtin_bit_cast(bf16x8, v); }
DI bf16x8 pack_pair(f32x4 lo, f32x4 hi) { return frag_of(pk2(lo[0], lo[1]), pk2(lo[2], lo[3]), pk2(hi[0], hi[1]), pk2(hi[2], hi[3])); }
DI bf16x8 lds128(LAS char* l, int off) { return *(LAS bf16x8*)(l + off); }
DI bf16x8 lds_tr(LAS char* l, int off, int second) {
  bf16x4 a = __builtin_amdgcn_ds_read_tr16_b64_v4bf16((LAS bf16x4*)(l + off));
  bf16x4 b = __builtin_amdgcn_ds_read_tr16_b64_v4bf16((LAS bf16x4*)(l + off + second));
  return __builtin_shufflevector(a, b, 0, 1, 2, 3, 4, 5, 6, 7);
}
DI void dma16(rsrc_t r, unsigned voff, unsigned soff, unsigned ldsaddr) {
  asm volatile("s_mov_b32 m0, %0\n\ts_nop 0\n\tbuffer_load_dwordx4 %1, %2, %3 offen lds" ::"s"(ldsaddr), "v"(voff), "s"(r), "s"(soff) : "memory");
}
template <int N> DI void wait_vm() { asm volatile("s_waitcnt vmcnt(%0)" ::"n"(N) : "memory"); }
DI void barrier() { asm volatile("s_waitcnt lgkmcnt(0)\n\ts_barrier" ::: "memory"); }
DI void lds_arrive(LAS char* l, int id, int) {
  const unsigned addr = (unsigned)(size_t)l + L_SYNC + id * 4, one = 1u;
  unsigned long long save;
  asm volatile("s_waitcnt lgkmcnt(0)\n\ts_mov_b64 %0, exec\n\ts_mov_b64 exec, 1\n\tds_add_u32 %1, %2\n\ts_mov_b64 exec, %0"
               : "=&s"(save) : "v"(addr), "v"(one) : "memory");
}
DI void lds_wait(LAS char* l, int id, unsigned n = 4u) {
  const unsigned addr = (unsigned)(size_t)l + L_SYNC + id * 4;
#pragma unroll 1
  for (int sp = 0; sp < SPIN_L; ++sp) {
    unsigned v;
    asm volatile("ds_read_b32 %0, %1\n\ts_waitcnt lgkmcnt(0)" : "=v"(v) : "v"(addr) : "memory");
    if ((unsigned)__builtin_amdgcn_readfirstlane(v) >= n) break;
    __builtin_amdgcn_s_sleep(1);
  }
}
DI rsrc_t mk_rsrc(const void* p, int bytes) { return __builtin_amdgcn_make_buffer_rsrc((void*)p, 0, bytes, 0x00020000); }
DI void st_sc1(u32x4 v, rsrc_t r, unsigned off) { __builtin_amdgcn_raw_buffer_store_b128(v, r, off, 0, 16); }
DI u32x4 ld_sc1(rsrc_t r, unsigned off, unsigned soff) { return __builtin_amdgcn_raw_buffer_load_b128(r, off, soff, 16); }
DI int opaque(int x) { asm volatile("" : "+v"(x)); return x; }
DI unsigned umax3(unsigned a, unsigned b, unsigned c) { return max(max(a, b), c); }
DI unsigned nosent(unsigned x) { return min(x, 0xFFFFFFFEu); }
DI void swap16(unsigned& a, unsigned& b) { auto r = __builtin_amdgcn_permlane16_swap(a, b, false, false); a = r[0]; b = r[1]; }
DI f32x4 zero4() { return f32x4{0.f, 0.f, 0.f, 0.f}; }

DI int krow(int rho) { return 8 * ((rho >> 2) & 3) + 4 * (rho >> 4) + (rho & 3); }

template <bool ISB> DI void feature_map(const f32x4 (&P)[5], unsigned (&ph)[ISB ? 18 : 10]) {
  float p[18];
#pragma unroll
  for (int f = 0; f < 18; ++f) p[f] = P[f >> 2][f & 3];
  if constexpr (ISB) {
    float mx = p[0], mn = p[0];
#pragma unroll
    for (int f = 1; f < 17; f += 2) { mx = fmaxf(fmaxf(mx, p[f]), p[f + 1]); mn = fminf(fminf(mn, p[f]), p[f + 1]); }
    mx = fmaxf(mx, p[17]); mn = fminf(mn, p[17]);
    const f32x2 cp = {-mx * LOG2E, -mx * LOG2E}, cn = {mn * LOG2E, mn * LOG2E};
    f32x2 lp = {LOG2E, LOG2E}, ln = {-LOG2E, -LOG2E};
    asm volatile("" : "+v"(lp), "+v"(ln));
    float ep[18], en[18];
#pragma unroll
    for (int f = 0; f < 18; f += 2) {
      const f32x2 x = {p[f], p[f + 1]};
      const f32x2 a = __builtin_elementwise_fma(x, lp, cp), b = __builtin_elementwise_fma(x, ln, cn);
      ep[f] = __builtin_amdgcn_exp2f(a[0]); ep[f + 1] = __builtin_amdgcn_exp2f(a[1]);
      en[f] = __builtin_amdgcn_exp2f(b[0]); en[f + 1] = __builtin_amdgcn_exp2f(b[1]);
    }
    f32x2 sp = {ep[0], ep[1]}, sn = {en[0], en[1]};
#pragma unroll
    for (int f = 2; f < 18; f += 2) { sp += f32x2{ep[f], ep[f + 1]}; sn += f32x2{en[f], en[f + 1]}; }
    const float rp = __builtin_amdgcn_rcpf(sp[0] + sp[1]), rn = __builtin_amdgcn_rcpf(sn[0] + sn[1]);
#pragma unroll
    for (int f = 0; f < 18; f += 2) {
      f32x2 a = f32x2{ep[f], ep[f + 1]} * rp, b = f32x2{en[f], en[f + 1]} * rn;
      ph[f >> 1] = pk2(a[0], a[1]);
      ph[9 + (f >> 1)] = pk2(b[0], b[1]);
    }
  } else {
    float y[18];
#pragma unroll
    for (int f = 0; f < 18; ++f)
      y[f] = (fmaxf(p[f], 0.f) + __builtin_amdgcn_exp2f(fminf(p[f], 0.f) * LOG2E)) + 1e-6f;
#pragma unroll
    for (int f = 0; f < 18; f += 2) ph[f >> 1] = pk2(y[f], y[f + 1]);
    ph[9] = 0u;
  }
}

struct WF { bf16x8 a[5][2]; u32x2 h[5]; };
DI void load_wf(LAS char* lds, int lane, WF& f) {
  const int b0 = opaque(L_WF + lane * 16), b1 = opaque(L_WF + 10240 + lane * 8);
#pragma unroll
  for (int t = 0; t < 5; ++t) {
    f.a[t][0] = lds128(lds, b0 + (2 * t) * 1024);
    f.a[t][1] = lds128(lds, b0 + (2 * t + 1) * 1024);
    f.h[t] = *(LAS u32x2*)(lds + b1 + t * 512);
  }
}
DI void logits2(const WF& f, const f32x4 (&t0)[2], const f32x4 (&t1)[2], const f32x4 (&t2)[2], const f32x4 (&t3)[2],
                const f32x4 (&t4)[2], f32x4 (&P)[2][5]) {
  bf16x8 b[2][2];
  u32x2 b2[2];
#pragma unroll
  for (int m = 0; m < 2; ++m) {
    b[m][0] = pack_pair(t0[m], t1[m]);
    b[m][1] = pack_pair(t2[m], t3[m]);
    b2[m] = u32x2{pk2(t4[m][0], t4[m][1]), pk2(t4[m][2], t4[m][3])};
  }
#pragma unroll
  for (int m = 0; m < 2; ++m) {
#pragma unroll
    for (int t = 0; t < 5; ++t) P[m][t] = mfma(f.a[t][0], b[m][0], zero4());
#pragma unroll
    for (int t = 0; t < 5; ++t) P[m][t] = mfma(f.a[t][1], b[m][1], P[m][t]);
#pragma unroll
    for (int t = 0; t < 5; ++t) P[m][t] = mfma16(f.h[t], b2[m], P[m][t]);
  }
}
DI int wf_offset(int h, int d, int f) {
  const int col = 18 * h + d, s = col >> 5, ln = 16 * ((col >> 2) & 3) + 4 * h + (f & 3), tt = f >> 2;
  return s < 2 ? (tt * 2 + s) * 1024 + ln * 16 + (((col >> 4) & 1) * 4 + (col & 3)) * 2 : 10240 + tt * 512 + ln * 8 + (col & 3) * 2;
}

template <bool ISB> DI void c_path(const Args& a, LAS char* lds, const int w, const int lane, const int t, const int g) {
  constexpr int prob = ISB ? 1 : 0;
  constexpr int RP = ISB ? 288 : 160;
  constexpr int C5 = ISB ? 5 : 3;
  constexpr int KVROW = C5 * 16, KVHEAD = 19 * KVROW, NCH = 19 * C5;
  const int tid = w * 64 + lane, i = lane & 15, gg = lane >> 4;
  char* ws = a.ws;
  const rsrc_t rws = mk_rsrc(ws, WS_USED);
  const unsigned kvp_grp = WS_KVP + (prob * 8 + g) * 16 * KVP_STRIDE;
  const unsigned obuf = WS_O + (prob * 16 + t) * O_STRIDE;
  bool timed_out = false;

  STAMP_DECL
  STAMP(0); CSTAMP(43);
  const unsigned T = __hip_atomic_load((GAS unsigned*)(ws + WS_E + prob * 256), RLX_AGENT) + 1u;
  unsigned short wel[6];
  if constexpr (ISB) {
    const unsigned short* wp = (const unsigned short*)a.wphi + g * 4 * 324;
#pragma unroll
    for (int k = 0; k < 6; ++k) { const int idx = tid + 256 * k; wel[k] = (ABL & 8) ? (unsigned short)idx : wp[idx < 1296 ? idx : 0]; }
  }
  if (!(ABL & 4)) {
    const u32x4 s = {SENT, SENT, SENT, SENT};
#pragma unroll
    for (int k = 0; k < (4 * NCH + 255) / 256; ++k) {
      const int n = tid + 256 * k;
      if (n < 4 * NCH) st_sc1(s, rws, kvp_grp + t * KVP_STRIDE + n * 16);
    }
  }
  if (tid < 64) *(LAS unsigned*)(lds + L_SYNC + tid * 4) = 0u;
  if (tid < 32) *(LAS unsigned*)(lds + L_ZERO + tid * 4) = 0u;

  f32x4 acc[9][2];
#pragma unroll
  for (int n = 0; n < 9; ++n) { acc[n][0] = zero4(); acc[n][1] = zero4(); asm volatile("" : "+v"(acc[n][0]), "+v"(acc[n][1])); }
  const int xa0 = (32 * w + i) * 128 + ((gg ^ ((i >> 1) & 7)) * 16), xa1 = xa0 ^ 64;
  const int wa = WPART + (4 * gg + (i >> 2)) * 288 + (i & 3) * 8;
  const int wm = wa + ((i & 2) ? 256 : 128);
  bf16x8 wb[2][9], xb[2][2];
  WF wf;
  STAMP(1);
  barrier();
  STAMP(2); CSTAMP(44);
  {
    const int xo = opaque(xa0 + 3 * SLOT), wo = opaque(wa + 3 * SLOT), wmo = opaque(wm + 3 * SLOT);
    xb[0][0] = lds128(lds, xo); xb[0][1] = lds128(lds, xo + 2048);
#pragma unroll
    for (int n = 0; n < 9; ++n) wb[0][n] = lds_tr(lds, n < 8 ? wo + n * 32 + (n >= 4 ? 16 : 0) : wmo, 4608);
  }
#pragma unroll
  for (int c = 0; c < 9; ++c) {
    {
      const int base = ((c + 3) & 3) * SLOT;
      const int xo = opaque(xa1 + base), wo = opaque(wa + base + 9216), wmo = opaque(wm + base + 9216);
      if (!(ABL & 16)) { xb[1][0] = lds128(lds, xo); xb[1][1] = lds128(lds, xo + 2048); } else { xb[1][0] = xb[0][0]; xb[1][1] = xb[0][1]; }
      SBAR();
#pragma unroll
      for (int n = 0; n < 9; ++n) {
        if (!(ABL & 2)) wb[1][n] = lds_tr(lds, n < 8 ? wo + n * 32 + (n >= 4 ? 16 : 0) : wmo, 4608); else { wb[1][n] = wb[0][n]; asm volatile("" : "+v"(wb[1][n])); }
        if (!(ABL & 1)) {
          mfma_acc(acc[n][0], wb[0][n], xb[0][0]);
          mfma_acc(acc[n][1], wb[0][n], xb[0][1]);
        } else asm volatile("" :: "v"(wb[0][n]), "v"(xb[0][0]), "v"(xb[0][1]));
        SBAR();
      }
    }
    if (c == 0) {
      const u32x4 z = {0u, 0u, 0u, 0u};
#pragma unroll
      for (int k = 0; k < 4; ++k) { const int n = tid + 256 * k; if (n < 800) *(LAS u32x4*)(lds + L_WF + n * 16) = z; }
    }
    if (c == 1) {
      if constexpr (ISB) {
#pragma unroll
        for (int k = 0; k < 6; ++k) {
          const int idx = tid + 256 * k;
          if (idx < 1296) {
            const int h = idx / 324, rem = idx - h * 324, d = rem / 18, f = rem - d * 18;
            *(LAS unsigned short*)(lds + L_WF + wf_offset(h, d, f)) = wel[k];
          }
        }
      } else if (tid < 72) {
        const int h = tid / 18, f = tid - h * 18;
        *(LAS unsigned short*)(lds + L_WF + wf_offset(h, f, f)) = (unsigned short)0x3F80;
      }
    }
    if (c == 2 && !(ABL & 4)) {
      const u32x4 s = {SENT, SENT, SENT, SENT};
#pragma unroll
      for (int k = 0; k < 5; ++k) {
        const int n = tid + 256 * k, tile = n / 144, rem = n - 144 * tile;
        if (n < 1152) st_sc1(s, rws, obuf + (tile * 72 + 9 * g) * 256 + rem * 16);
      }
    }
    if (c == 5) wait_vm<0>();
    if (c == 6 && tid == 0) __hip_atomic_store((GAS unsigned*)(ws + WS_F1 + (prob * 128 + t * 8 + g) * 128), T, RLX_AGENT);
    barrier();
    if (c < 8) {
      const int base = ((c + 4) & 3) * SLOT;
      const int xo = opaque(xa0 + base), wo = opaque(wa + base), wmo = opaque(wm + base);
      if (!(ABL & 16)) { xb[0][0] = lds128(lds, xo); xb[0][1] = lds128(lds, xo + 2048); } else { xb[0][0] = xb[1][0]; xb[0][1] = xb[1][1]; }
      SBAR();
#pragma unroll
      for (int n = 0; n < 9; ++n) {
        if (!(ABL & 2)) wb[0][n] = lds_tr(lds, n < 8 ? wo + n * 32 + (n >= 4 ? 16 : 0) : wmo, 4608); else { wb[0][n] = wb[1][n]; asm volatile("" : "+v"(wb[0][n])); }
        if (!(ABL & 1)) {
          mfma_acc(acc[n][0], wb[1][n], xb[1][0]);
          mfma_acc(acc[n][1], wb[1][n], xb[1][1]);
        } else asm volatile("" :: "v"(wb[1][n]), "v"(xb[1][0]), "v"(xb[1][1]));
        SBAR();
      }
    } else {
      load_wf(lds, lane, wf);
      SBAR();
#pragma unroll
      for (int n = 0; n < 9; ++n) {
        mfma_acc(acc[n][0], wb[1][n], xb[1][0]);
        mfma_acc(acc[n][1], wb[1][n], xb[1][1]);
      }
      mfma_fence();
    }
  }
  const int lane_ = opaque(lane);
  {
  const int lane = lane_, tid = w * 64 + lane, i = lane & 15, gg = lane >> 4;
  const int xs = ((i >> 2) & 3) << 3;
  GAS unsigned* fp = (GAS unsigned*)(ws + WS_F1 + (prob * 128 + ((lane & 16) ? t * 8 + (lane & 7) : (lane & 15) * 8 + g)) * 128);
  const unsigned f1_early = __hip_atomic_load(fp, RLX_AGENT);
  __builtin_amdgcn_s_setprio(2);

  STAMP(3); CSTAMP(45);
  {
    int g8[4];
#pragma unroll
    for (int k = 0; k < 4; ++k) g8[k] = 8 * gg + (k && gg >= k ? 4 : 0);
    f32x4 P[2][5];
    logits2(wf, acc[0], acc[1], acc[2], acc[3], acc[8], P);
#pragma unroll
    for (int m = 0; m < 2; ++m) {
      const int vrow = L_V + (32 * w + 16 * m + i) * 160;
#pragma unroll
      for (int n = 0; n < 5; ++n) {
        const f32x4 v = n < 4 ? acc[4 + n][m] : acc[8][m];
#pragma unroll
        for (int pp = 0; pp < 2; ++pp) {
          const int c0 = (n < 4 ? 16 * n : 56) + 2 * pp, h0 = c0 / 18;
          const int thr = (18 * (h0 + 1) - c0 + 3) / 4;
          const int off = 2 * c0 + 4 * h0 + g8[thr < 4 ? thr : 0];
          if (n < 4 || gg >= 2) *(LAS unsigned*)(lds + vrow + (off ^ xs)) = pk2(v[2 * pp], v[2 * pp + 1]);
        }
      }
      *(LAS unsigned*)(lds + vrow + ((40 * gg + 36) ^ xs)) = 0x00003F80u;
    }
#pragma unroll
    for (int m = 0; m < 2; ++m) {
      unsigned ph[ISB ? 18 : 10];
      feature_map<ISB>(P[m], ph);
      constexpr int NU = ISB ? 9 : 5, UM = NU - 1;
      LAS char* prow = lds + opaque(L_PHI + (32 * w + 16 * m + i) * RP);
      const int pm = (UM * gg * 8) ^ xs, pl = ((4 * UM + gg) * 8) ^ xs;
#pragma unroll
      for (int j = 0; j < NU; ++j)
        *(LAS u32x2*)(prow + (j < UM ? pm ^ (j * 8) : pl)) = u32x2{ph[2 * j], ph[2 * j + 1]};
    }
  }
  STAMP(4);
  lds_arrive(lds, SY_PHI, lane);
  lds_wait(lds, SY_PHI);
  STAMP(5);

  bool f1ok;
  {
    constexpr int UM = ISB ? 8 : 4;
    const int q = i >> 2, p = i & 3;
    const int pa = L_PHI + (4 * gg + q) * RP, va = L_V + (4 * gg + q) * 160;
    const int a_b0 = opaque(pa + ((UM * w + p) ^ gg) * 8), a_b1 = opaque(pa + ((UM * w + 4 + p) ^ gg) * 8);
    const int a_mu = opaque(pa + ((4 * UM + p) ^ gg) * 8);
    const int a_al = opaque(va + ((5 * w + p) ^ gg) * 8), a_la = opaque(va + ((5 * p + 4) ^ gg) * 8);
    bf16x8 al[4], la[4], b0[4], mu[4], b1[4];
#pragma unroll
    for (int ks = 0; ks < 4; ++ks) {
      al[ks] = lds_tr(lds, a_al + ks * 32 * 160, 16 * 160);
      b0[ks] = lds_tr(lds, a_b0 + ks * 32 * RP, 16 * RP);
      la[ks] = lds_tr(lds, a_la + ks * 32 * 160, 16 * 160);
      mu[ks] = lds_tr(lds, a_mu + ks * 32 * RP, 16 * RP);
      if constexpr (ISB) b1[ks] = lds_tr(lds, a_b1 + ks * 32 * RP, 16 * RP);
    }
    SBAR();
    f32x4 d0 = zero4(), d1 = zero4(), d2 = zero4(), d3 = zero4(), d4 = zero4(), d5 = zero4();
#pragma unroll
    for (int ks = 0; ks < 4; ++ks) {
      d0 = mfma(b0[ks], al[ks], d0); d2 = mfma(b0[ks], la[ks], d2);
      d4 = mfma(mu[ks], al[ks], d4); d5 = mfma(mu[ks], la[ks], d5);
      if constexpr (ISB) { d1 = mfma(b1[ks], al[ks], d1); d3 = mfma(b1[ks], la[ks], d3); }
    }
    lds_arrive(lds, SY_KVDONE, lane);
    f1ok = __all(f1_early == T);
    const unsigned mine = kvp_grp + t * KVP_STRIDE + w * KVHEAD;
    const bool lam = (i >> 2) == w && (i & 3) < 3;
    const int el = 16 + (i & 3);
    if constexpr (ISB) {
      unsigned x0 = pk2(d0[0], d0[1]), x1 = pk2(d0[2], d0[3]), y0 = pk2(d1[0], d1[1]), y1 = pk2(d1[2], d1[3]);
      swap16(x0, y0); swap16(x1, y1);
      const int ch = ((gg & 1) << 1) | (gg >> 1);
      st_sc1(u32x4{nosent(x0), nosent(x1), nosent(y0), nosent(y1)}, rws, mine + i * KVROW + ch * 16);
      x0 = pk2(d2[0], d2[1]); x1 = pk2(d2[2], d2[3]); y0 = pk2(d3[0], d3[1]); y1 = pk2(d3[2], d3[3]);
      swap16(x0, y0); swap16(x1, y1);
      if (lam) st_sc1(u32x4{nosent(x0), nosent(x1), nosent(y0), nosent(y1)}, rws, mine + el * KVROW + ch * 16);
    } else {
      unsigned x0 = pk2(d0[0], d0[1]), x1 = pk2(d0[2], d0[3]), y0 = pk2(d2[0], d2[1]), y1 = pk2(d2[2], d2[3]);
      swap16(x0, y0); swap16(x1, y1);
      const bool isl = gg & 1;
      if (!isl || lam) st_sc1(u32x4{nosent(x0), nosent(x1), nosent(y0), nosent(y1)}, rws, mine + (isl ? el : i) * KVROW + (gg >> 1) * 16);
    }
    if (gg == w) st_sc1(u32x4{nosent(pk2(d4[0], d4[1])), nosent(pk2(d4[2], d4[3])), 0u, 0u}, rws, mine + i * KVROW + (C5 - 1) * 16);
    if (gg == w && lam) st_sc1(u32x4{nosent(pk2(d5[0], d5[1])), nosent(pk2(d5[2], d5[3])), 0u, 0u}, rws, mine + el * KVROW + (C5 - 1) * 16);
  }

  STAMP(6); CSTAMP(46);
  {
    int sp = 0;
    if (!f1ok) {
      for (; sp < SPIN_G; ++sp) {
        const unsigned f = __hip_atomic_load(fp, RLX_AGENT);
        if (__all(f == T)) break;
      }
    }
    timed_out |= sp == SPIN_G;
    STAMP(7);
    const rsrc_t rkv = mk_rsrc(ws + kvp_grp, 16 * KVP_STRIDE);
    const unsigned poff = lane < 16 ? lane * KVP_STRIDE + w * KVHEAD + 18 * KVROW + (C5 - 1) * 16 : OOB;
    for (sp = 0; sp < SPIN_G; ++sp) {
      asm volatile("" ::: "memory");
      const unsigned f = __builtin_amdgcn_raw_buffer_load_b32(rkv, poff, 0, 16);
      if (__all(f != SENT)) break;
    }
    timed_out |= sp == SPIN_G;
    STAMP(8);
    constexpr int NG = (NCH + 15) / 16;
    const bf16x8 sel = frag_of(i == 0 ? 0x3F80u : i == 1 ? 0x3F800000u : 0u, i == 2 ? 0x3F80u : i == 3 ? 0x3F800000u : 0u,
                               i == 4 ? 0x3F80u : i == 5 ? 0x3F800000u : 0u, i == 6 ? 0x3F80u : i == 7 ? 0x3F800000u : 0u);
    f32x4 sum[NG];
    for (sp = 0; sp < 64; ++sp) {
      asm volatile("" ::: "memory");
      u32x4 v[NG][4];
#pragma unroll
      for (int cg = 0; cg < NG; ++cg) {
        const unsigned off = 16 * cg + i < NCH ? gg * KVP_STRIDE + w * KVHEAD + (16 * cg + i) * 16 : OOB;
#pragma unroll
        for (int u = 0; u < 4; ++u) { v[cg][u] = ld_sc1(rkv, off, 4 * u * KVP_STRIDE); SBAR(); }
      }
      f32x2 chk = {0.f, 0.f};
#pragma unroll
      for (int cg = 0; cg < NG; ++cg) {
        f32x4 d = zero4();
#pragma unroll
        for (int u = 0; u < 4; ++u) d = mfma(sel, __builtin_bit_cast(bf16x8, v[cg][u]), d);
        sum[cg] = d;
        chk += f32x2{d[0], d[1]} * 0.f + f32x2{d[2], d[3]} * 0.f;
      }
      const float ck = chk[0] + chk[1];
      if (__all(ck == ck)) break;
      unsigned mx = 0u;
#pragma unroll
      for (int cg = 0; cg < NG; ++cg)
#pragma unroll
        for (int u = 0; u < 4; ++u) { mx = umax3(mx, v[cg][u][0], v[cg][u][1]); mx = umax3(mx, v[cg][u][2], v[cg][u][3]); }
      if (__all(mx != SENT)) break;
    }
    timed_out |= sp == 64;
    lds_wait(lds, SY_KVDONE);
#pragma unroll
    for (int cg = 0; cg < NG; ++cg)
      if (gg < 2 && 16 * cg + i < NCH)
        *(LAS u32x2*)(lds + L_PHI + w * KVHEAD + (16 * cg + i) * 16 + gg * 8) = u32x2{pk2(sum[cg][0], sum[cg][1]), pk2(sum[cg][2], sum[cg][3])};
    lds_arrive(lds, SY_KVIMG, lane);
  }
  STAMP(9);
  __builtin_amdgcn_s_setprio(0);
  {
    const rsrc_t rwo = mk_rsrc(ISB ? a.woB : a.woA, 576 * XROW);
    const unsigned lb = (unsigned)(size_t)lds;
    unsigned vo[5];
#pragma unroll
    for (int n = 0; n < 5; ++n) {
      const int nn = n * 64 + lane, rho = nn / 10, cp = nn - 10 * rho;
      vo[n] = krow(rho) * XROW + 144 * g + 16 * min(cp, 8);
    }
    lds_wait(lds, SY_QDONE);
#pragma unroll
    for (int kk = 0; kk < 5; ++kk) {
      const int ks = w + 4 * kk;
      if (kk < 4 || w < 2) {
#pragma unroll
        for (int n = 0; n < 5; ++n) dma16(rwo, vo[n], ks * 32 * XROW, lb + L_WQ + ks * 5120 + n * 1024);
      }
    }
    wait_vm<0>();
    lds_arrive(lds, SY_WO, lane);
  }
  STAMP(10);

  {
    const rsrc_t ro = mk_rsrc(ws + obuf, O_STRIDE);
    const unsigned poff = lane < 8 ? obuf + ((2 * w + 1) * 72 + 9 * lane + 8) * 256 + 15 * 16 + 12 : OOB;
    int sp;
    for (sp = 0; sp < SPIN_G; ++sp) {
      asm volatile("" ::: "memory");
      const unsigned f = __builtin_amdgcn_raw_buffer_load_b32(rws, poff, 0, 16);
      if (__all(f != SENT)) break;
    }
    timed_out |= sp == SPIN_G;
    lds_wait(lds, SY_WO);
    __builtin_amdgcn_s_setprio(2);
    STAMP(11);
    f32x4 oc[5][2];
    const int wa2 = opaque(L_WQ + (4 * gg + (i >> 2)) * 160 + (i & 3) * 8), wa3 = opaque(wa2 + 12 * 5120);
    for (sp = 0; sp < 64; ++sp) {
      asm volatile("" ::: "memory");
      u32x4 of[18][2];
#pragma unroll
      for (int ks = 0; ks < 18; ++ks) {
#pragma unroll
        for (int m = 0; m < 2; ++m) { of[ks][m] = ld_sc1(ro, lane * 16, ((2 * w + m) * 72 + 4 * ks) * 256); SBAR(); }
      }
      bf16x8 wq[2][5];
#pragma unroll
      for (int n = 0; n < 5; ++n) { wq[0][n] = lds_tr(lds, wa2 + n * 32, 16 * 160); oc[n][0] = zero4(); oc[n][1] = zero4(); }
      unsigned mx = 0u;
#pragma unroll
      for (int n = 0; n < 5; ++n) asm volatile("" : "+v"(oc[n][0]), "+v"(oc[n][1]));
      asm volatile("s_nop 1");
      SBAR();
#pragma unroll
      for (int ks = 0; ks < 18; ++ks) {
#pragma unroll
        for (int n = 0; n < 5; ++n) {
          if (ks < 17) wq[(ks + 1) & 1][n] = lds_tr(lds, (ks + 1 < 12 ? wa2 + (ks + 1) * 5120 : wa3 + (ks + 1 - 12) * 5120) + n * 32, 16 * 160);
          mfma_acc(oc[n][0], wq[ks & 1][n], __builtin_bit_cast(bf16x8, of[ks][0]));
          mfma_acc(oc[n][1], wq[ks & 1][n], __builtin_bit_cast(bf16x8, of[ks][1]));
          if (n < 2) { mx = umax3(mx, of[ks][n][0], of[ks][n][1]); mx = umax3(mx, of[ks][n][2], of[ks][n][3]); }
          SBAR();
        }
      }
      mfma_fence();
      if (__all(mx != SENT)) break;
    }
    timed_out |= sp == 64;
    STAMP(12);
    if (t == 0 && g == 0 && tid == 0) __hip_atomic_store((GAS unsigned*)(ws + WS_E + prob * 256), T, RLX_AGENT);
    const rsrc_t rout = mk_rsrc(a.out, 2048 * OUTROW);
    const unsigned orow = (t * 128 + 32 * w + i) * OUTROW + prob * 1152 + 144 * g;
#pragma unroll
    for (int m = 0; m < 2; ++m) {
#pragma unroll
      for (int n = 0; n < 4; n += 2) {
        unsigned x0 = pk2(oc[n][m][0], oc[n][m][1]), x1 = pk2(oc[n][m][2], oc[n][m][3]);
        unsigned y0 = pk2(oc[n + 1][m][0], oc[n + 1][m][1]), y1 = pk2(oc[n + 1][m][2], oc[n + 1][m][3]);
        swap16(x0, y0); swap16(x1, y1);
        __builtin_amdgcn_raw_buffer_store_b128(u32x4{x0, x1, y0, y1}, rout, orow + m * 16 * OUTROW + (2 * (n + (gg & 1)) + (gg >> 1)) * 16, 0, OUT_AUX);
      }
    }
    unsigned x0 = pk2(oc[4][0][0], oc[4][0][1]), x1 = pk2(oc[4][0][2], oc[4][0][3]);
    unsigned y0 = pk2(oc[4][1][0], oc[4][1][1]), y1 = pk2(oc[4][1][2], oc[4][1][3]);
    swap16(x0, y0); swap16(x1, y1);
    if (gg < 2) __builtin_amdgcn_raw_buffer_store_b128(u32x4{x0, x1, y0, y1}, rout, orow + gg * 16 * OUTROW + 128, 0, OUT_AUX);
  }
  (void)timed_out;
  STAMP(13); CSTAMP(47);
  STAMP_OUT(w);
  }
}

template <bool ISB> DI void h_path(const Args& a, LAS char* lds, const int w, const int lane, const int t, const int g) {
  constexpr int prob = ISB ? 1 : 0;
  constexpr int C5 = ISB ? 5 : 3;
  constexpr int KVROW = C5 * 16, KVHEAD = 19 * KVROW;
  const int i = lane & 15, gg = lane >> 4;
  const rsrc_t rx = mk_rsrc((const char*)(ISB ? a.xB : a.xA) + (size_t)t * 128 * XROW, 128 * XROW);
  const rsrc_t rw = mk_rsrc(ISB ? a.wB : a.wA, 576 * WROW);
  const unsigned lb = (unsigned)(size_t)lds;
  STAMP_DECL
  STAMP(0);
  unsigned vx[2], vw[5], vq[8];
#pragma unroll
  for (int par = 0; par < 2; ++par) vx[par] = (lane >> 3) * XROW + (((lane & 7) ^ ((4 * par + (lane >> 4)) & 7)) * 16);
#pragma unroll
  for (int n = 0; n < 5; ++n) {
    const int nn = (w + 4 * n) * 64 + lane, r64 = nn / 18, cp = nn - 18 * r64;
    const int co = cp < 9 ? 1152 + 16 * cp : 2304 + 16 * (cp - 9);
    vw[n] = ((r64 & 32) + krow(r64 & 31)) * WROW + 144 * g + co;
  }
  const int rot = ROT ? (g + 8 * (t & 3)) % 9 : 0;
  auto kchunk = [&](int c) { const int k = c + rot; return k >= 9 ? k - 9 : k; };
  auto issue_kv = [&](int c) {
    const unsigned sb = lb + ((c + 3) & 3) * SLOT;
    const int kc = kchunk(c);
#pragma unroll
    for (int jj = 0; jj < 4; ++jj) dma16(rx, vx[jj & 1], (4 * w + jj) * 8 * XROW + kc * 128, sb + (4 * w + jj) * 1024);
#pragma unroll
    for (int n = 0; n < 5; ++n)
      if (n < 4 || w < 2) dma16(rw, vw[n], kc * 64 * WROW, sb + WPART + (w + 4 * n) * 1024);
  };
  LSTAMP(12);
  issue_kv(0); LSTAMP(13); issue_kv(1); LSTAMP(14);
  STAMP(1);
  if (w < 2) wait_vm<9>(); else wait_vm<8>();
  STAMP(2);
  barrier();
  issue_kv(2); LSTAMP(15);
#pragma unroll
  for (int n = 0; n < 8; ++n) {
    const int v = w + 4 * n, ks = v / 5, nn = (v - 5 * ks) * 64 + lane, rho = nn / 10, cp = nn - 10 * rho;
    vq[n] = ((ks & 1) * 32 + krow(rho)) * WROW + 144 * g + 16 * min(cp, 8);
  }
  auto issue_wq = [&](int grp) {
#pragma unroll
    for (int n = 0; n < 8; ++n)
      if (n < 7 || w < 2)
        dma16(rw, vq[n], kchunk(3 * grp + ((w + 4 * n) / 5 >> 1)) * 64 * WROW, lb + L_WQ + grp * 30720 + (w + 4 * n) * 1024);
  };
  bf16x8 X[18][2];
  const int xa0 = (32 * w + i) * 128 + ((gg ^ ((i >> 1) & 7)) * 16), xa1 = xa0 ^ 64;
#pragma unroll
  for (int c = 0; c < 9; ++c) {
    const int base = ((c + 3) & 3) * SLOT;
    const int xo0 = opaque(xa0 + base), xo1 = opaque(xa1 + base);
    if (!(ABL & 32) || c == 0) {
    X[2 * c][0] = lds128(lds, xo0); X[2 * c][1] = lds128(lds, xo0 + 2048);
    X[2 * c + 1][0] = lds128(lds, xo1); X[2 * c + 1][1] = lds128(lds, xo1 + 2048);
    } else { X[2 * c][0] = X[0][0]; X[2 * c][1] = X[0][1]; X[2 * c + 1][0] = X[1][0]; X[2 * c + 1][1] = X[1][1]; }
    if (c <= 5) issue_kv(c + 3);
    LSTAMP(16 + c);
    if (c <= 5) { if (w < 2) wait_vm<18>(); else wait_vm<16>(); }
    if (c == 6) { if (w < 2) wait_vm<9>(); else wait_vm<8>(); }
    if (c == 7) wait_vm<0>();
    LSTAMP(25 + c);
    barrier();
    LSTAMP(34 + c);
  }
  issue_wq(0); issue_wq(1); issue_wq(2);
  STAMP(3);
  f32x4 qa[5][2];
#pragma unroll
  for (int n = 0; n < 5; ++n) { qa[n][0] = zero4(); qa[n][1] = zero4(); asm volatile("" : "+v"(qa[n][0]), "+v"(qa[n][1])); }
  const int wa2 = opaque(L_WQ + (4 * gg + (i >> 2)) * 160 + (i & 3) * 8), wa3 = opaque(wa2 + 12 * 5120);
  bf16x8 wq[2][5];
  if (w < 2) wait_vm<16>(); else wait_vm<14>();
  lds_arrive(lds, SY_WQ0, lane);
  lds_wait(lds, SY_WQ0);
  STAMP(4);
#pragma unroll
  for (int n = 0; n < 5; ++n) wq[0][n] = lds_tr(lds, wa2 + n * 32, 16 * 160);
#pragma unroll
  for (int ks = 0; ks < 18; ++ks) {
    if (ks == 5) { if (w < 2) wait_vm<8>(); else wait_vm<7>(); lds_arrive(lds, SY_WQ1, lane); lds_wait(lds, SY_WQ1); STAMP(5); }
    if (ks == 11) { wait_vm<0>(); lds_arrive(lds, SY_WQ2, lane); lds_wait(lds, SY_WQ2); STAMP(6); }
    SBAR();
#pragma unroll
    for (int n = 0; n < 5; ++n) {
      if (ks < 17) wq[(ks + 1) & 1][n] = lds_tr(lds, (ks + 1 < 12 ? wa2 + (ks + 1) * 5120 : wa3 + (ks + 1 - 12) * 5120) + n * 32, 16 * 160);
      mfma_acc(qa[n][0], wq[ks & 1][n], X[ks][0]);
      mfma_acc(qa[n][1], wq[ks & 1][n], X[ks][1]);
      SBAR();
    }
  }
  mfma_fence();
  lds_arrive(lds, SY_QDONE, lane);
  WF wf;
  load_wf(lds, lane, wf);
  STAMP(7);
  unsigned ph[2][ISB ? 18 : 10];
  {
    f32x4 P[2][5];
    logits2(wf, qa[0], qa[1], qa[2], qa[3], qa[4], P);
    feature_map<ISB>(P[0], ph[0]);
    feature_map<ISB>(P[1], ph[1]);
  }
  STAMP(8);
  lds_wait(lds, SY_KVIMG);
  STAMP(9);
  __builtin_amdgcn_s_setprio(3);
  f32x4 O[5][2];
  {
    const bool own = (i >> 2) == gg;
    bf16x8 af[5][C5];
#pragma unroll
    for (int n = 0; n < 5; ++n) {
      const int e = 4 * n + (i & 3);
      const int ad = opaque((own && e < 19) ? L_PHI + gg * KVHEAD + e * KVROW : L_ZERO);
#pragma unroll
      for (int s = 0; s < C5; ++s) af[n][s] = lds128(lds, ad + s * 16);
    }
    SBAR();
    constexpr int NP = ISB ? 18 : 10;
#pragma unroll
    for (int s = 0; s < C5; ++s) {
      bf16x8 bq[2];
#pragma unroll
      for (int m = 0; m < 2; ++m) {
        unsigned u[4];
#pragma unroll
        for (int j = 0; j < 4; ++j) u[j] = 4 * s + j < NP ? ph[m][4 * s + j < NP ? 4 * s + j : 0] : 0u;
        bq[m] = frag_of(u[0], u[1], u[2], u[3]);
      }
#pragma unroll
      for (int n = 0; n < 5; ++n) {
#pragma unroll
        for (int m = 0; m < 2; ++m) O[n][m] = mfma(af[n][s], bq[m], s == 0 ? zero4() : O[n][m]);
      }
    }
  }
  STAMP(10);
  LAS char* stg = lds + L_V + w * 4608;
#pragma unroll
  for (int m = 0; m < 2; ++m) {
    const float rc = __builtin_amdgcn_rcpf(O[4][m][2] + 1e-6f);
#pragma unroll
    for (int pp = 0; pp < 9; ++pp) {
      const int e = 2 * pp, cp = 9 * gg + pp;
      const unsigned v = nosent(pk2(O[e >> 2][m][e & 3] * rc, O[(e + 1) >> 2][m][(e + 1) & 3] * rc));
      *(LAS unsigned*)(stg + m * 2304 + (cp >> 2) * 256 + i * 16 + (cp & 3) * 4) = v;
    }
  }
  asm volatile("s_waitcnt lgkmcnt(0)" ::: "memory");
  const rsrc_t ro = mk_rsrc(a.ws + WS_O + (prob * 16 + t) * O_STRIDE, O_STRIDE);
#pragma unroll
  for (int k = 0; k < 5; ++k) {
    const int n = lane + 64 * k;
    if (n < 288) {
      const u32x4 v = *(LAS u32x4*)(stg + n * 16);
      const int m = n >= 144;
      st_sc1(v, ro, ((2 * w + m) * 72 + 9 * g) * 256 + (n - 144 * m) * 16);
    }
  }
  STAMP(11);
  STAMP_OUT(4 + w);
}

__global__ __launch_bounds__(512, 2) void hedgehog_fused(Args a) {
  extern __shared__ __attribute__((aligned(16))) char smem[];
  LAS char* lds = (LAS char*)smem;
  const int b = blockIdx.x, xcd = b & 7, r = b >> 3;
  const int prob = (xcd >> 2) ^ SWAP, t = (xcd & 3) * 4 + (r >> 3), g = r & 7;
  const int wave = __builtin_amdgcn_readfirstlane(threadIdx.x >> 6), lane = threadIdx.x & 63;
  unsigned cpf_x = 0;
  if (wave < 4) {
    if (prob) c_path<true>(a, lds, wave, lane, t, g); else c_path<false>(a, lds, wave, lane, t, g);
  } else {
    if (prob) h_path<true>(a, lds, wave - 4, lane, t, g); else h_path<false>(a, lds, wave - 4, lane, t, g);
  }
  asm volatile("" :: "v"(cpf_x));
}

}

extern "C" void kernel_launch(void* const* d_in, const int* in_sizes, int n_in, void* d_out, int out_size, void* d_ws,
                              size_t ws_size, hipStream_t stream) {
  (void)in_sizes; (void)n_in; (void)out_size; (void)ws_size;
  Args a;
  a.xA = d_in[0]; a.wA = d_in[1]; a.woA = d_in[2]; a.xB = d_in[3]; a.wB = d_in[4]; a.wphi = d_in[5]; a.woB = d_in[6];
  a.out = d_out; a.ws = (char*)d_ws;
  (void)hipFuncSetAttribute((const void*)hedgehog_fused, hipFuncAttributeMaxDynamicSharedMemorySize, LDS_BYTES);
  hedgehog_fused<<<256, 512, LDS_BYTES, stream>>>(a);
}
```

```cpp
#include <hip/hip_runtime.h>
#include <hip/hip_bf16.h>
#include <stdint.h>
#include <type_traits>

typedef __bf16 bf16x8 __attribute__((ext_vector_type(8)));
typedef __bf16 bf16x4 __attribute__((ext_vector_type(4)));
typedef __bf16 bf16x2 __attribute__((ext_vector_type(2)));
typedef short s16x4 __attribute__((ext_vector_type(4)));
typedef float f32x4 __attribute__((ext_vector_type(4)));
typedef float f32x2 __attribute__((ext_vector_type(2)));
typedef unsigned u32x4 __attribute__((ext_vector_type(4)));
typedef unsigned u32x2 __attribute__((ext_vector_type(2)));
#define LAS __attribute__((address_space(3)))
#define GAS __attribute__((address_space(1)))
#define DI __device__ __forceinline__
#define RLX_AGENT __ATOMIC_RELAXED, __HIP_MEMORY_SCOPE_AGENT
#define SBAR() __builtin_amdgcn_sched_barrier(0)
typedef __amdgpu_buffer_rsrc_t rsrc_t;

#define DBG 0
#define PROF 0
#define STAMP(n)
#define CSTAMP(n)
#define STAMP_DECL
#define STAMP_OUT(wv)
#define FSTAMP(n)
#define PREF 0
#define WO_EARLY 1
#define ROT 1

namespace {

constexpr int XROW = 1152;
constexpr int WROW = 3456;
constexpr int OUTROW = 2304;
constexpr int SLOT = 34816, WPART = 16384;
constexpr int L_WQ = 0;
constexpr int L_PHI = 92160;
constexpr int L_WO0 = 98304;
constexpr int L_V = 129024;
constexpr int L_WF = 149504;
constexpr int L_SYNC = 162304;
constexpr int L_ZERO = 162560;
constexpr int LDS_BYTES = 163840;
enum { SY_WQ0 = 0, SY_WQ1, SY_WQ2, SY_QDONE, SY_PHI, SY_KVDONE, SY_KVIMG, SY_WO, SY_KV8, SY_TAG = 63 };
constexpr int WS_E = 0, WS_F1 = 1024, WS_KVP = 65536, KVP_STRIDE = 6144, WS_O = 2097152, O_STRIDE = 147456;
constexpr int WS_H1 = WS_O + 32 * O_STRIDE, WS_H2 = WS_H1 + 131072;
constexpr int WS_USED = WS_H2 + 131072;
constexpr unsigned SENT = 0xFFFFFFFFu;
constexpr unsigned OOB = 0x7FFF0000u;
constexpr int SPIN_G = 1 << 18, SPIN_L = 1 << 22;
constexpr float LOG2E = 1.4426950408889634f;

struct Args {
  const void* xA; const void* wA; const void* woA; const void* xB; const void* wB; const void* wphi; const void* woB;
  void* out; char* ws;
};

DI f32x4 mfma(bf16x8 a, bf16x8 b, f32x4 c) { return __builtin_amdgcn_mfma_f32_16x16x32_bf16(a, b, c, 0, 0, 0); }
DI f32x4 mfma16(u32x2 a, u32x2 b, f32x4 c) {
  return __builtin_amdgcn_mfma_f32_16x16x16bf16_1k(__builtin_bit_cast(s16x4, a), __builtin_bit_cast(s16x4, b), c, 0, 0, 0);
}
DI void mfma_acc(f32x4& c, bf16x8 a, bf16x8 b) { asm volatile("v_mfma_f32_16x16x32_bf16 %0, %1, %2, %0" : "+v"(c) : "v"(a), "v"(b)); }
DI void mfma_fence() { asm volatile("s_nop 7\n\ts_nop 3" ::: "memory"); }
DI unsigned pk2(float a, float b) { f32x2 p = {a, b}; return __builtin_bit_cast(unsigned, __builtin_convertvector(p, bf16x2)); }
DI bf16x8 frag_of(unsigned a, unsigned b, unsigned c, unsigned d) { u32x4 v = {a, b, c, d}; return __builtin_bit_cast(bf16x8, v); }
DI bf16x8 pack_pair(f32x4 lo, f32x4 hi) { return frag_of(pk2(lo[0], lo[1]), pk2(lo[2], lo[3]), pk2(hi[0], hi[1]), pk2(hi[2], hi[3])); }
DI bf16x8 lds128(LAS char* l, int off) { return *(LAS bf16x8*)(l + off); }
DI bf16x8 lds_tr(LAS char* l, int off, int second) {
  bf16x4 a = __builtin_amdgcn_ds_read_tr16_b64_v4bf16((LAS bf16x4*)(l + off));
  bf16x4 b = __builtin_amdgcn_ds_read_tr16_b64_v4bf16((LAS bf16x4*)(l + off + second));
  return __builtin_shufflevector(a, b, 0, 1, 2, 3, 4, 5, 6, 7);
}
DI void dma16(rsrc_t r, unsigned voff, unsigned soff, unsigned ldsaddr) {
  asm volatile("s_mov_b32 m0, %0\n\ts_nop 0\n\tbuffer_load_dwordx4 %1, %2, %3 offen lds" ::"s"(ldsaddr), "v"(voff), "s"(r), "s"(soff) : "memory");
}
template <int N> DI void wait_vm() { asm volatile("s_waitcnt vmcnt(%0)" ::"n"(N) : "memory"); }
DI void cwait_lgkm0() { __builtin_amdgcn_s_waitcnt(0xC07F); }
template <int N> DI void cwait_vm_lgkm4() { __builtin_amdgcn_s_waitcnt((N & 15) | 0x70 | (4 << 8) | ((N >> 4) << 14)); }
DI void barrier() { asm volatile("s_waitcnt lgkmcnt(0)\n\ts_barrier" ::: "memory"); }
DI void lds_arrive(LAS char* l, int id) {
  const unsigned addr = (unsigned)(size_t)l + L_SYNC + id * 4, one = 1u;
  unsigned long long save;
  asm volatile("s_waitcnt lgkmcnt(0)\n\ts_mov_b64 %0, exec\n\ts_mov_b64 exec, 1\n\tds_add_u32 %1, %2\n\ts_mov_b64 exec, %0"
               : "=&s"(save) : "v"(addr), "v"(one) : "memory");
}
DI void lds_wait(LAS char* l, int id, unsigned n = 4u) {
  const unsigned addr = (unsigned)(size_t)l + L_SYNC + id * 4;
#pragma unroll 1
  for (int sp = 0; sp < SPIN_L; ++sp) {
    unsigned v;
    asm volatile("ds_read_b32 %0, %1\n\ts_waitcnt lgkmcnt(0)" : "=v"(v) : "v"(addr) : "memory");
    if ((unsigned)__builtin_amdgcn_readfirstlane(v) >= n) break;
    __builtin_amdgcn_s_sleep(1);
  }
}
DI rsrc_t mk_rsrc(const void* p, int bytes) { return __builtin_amdgcn_make_buffer_rsrc((void*)p, 0, bytes, 0x00020000); }
DI void st_sc1(u32x4 v, rsrc_t r, unsigned off) { __builtin_amdgcn_raw_buffer_store_b128(v, r, off, 0, 16); }
DI u32x4 ld_sc1(rsrc_t r, unsigned off, unsigned soff) { return __builtin_amdgcn_raw_buffer_load_b128(r, off, soff, 16); }
DI int opaque(int x) { asm volatile("" : "+v"(x)); return x; }
DI unsigned umax3(unsigned a, unsigned b, unsigned c) { return max(max(a, b), c); }
DI unsigned nosent(unsigned x) { return min(x, 0xFFFFFFFEu); }
DI void swap16(unsigned& a, unsigned& b) { auto r = __builtin_amdgcn_permlane16_swap(a, b, false, false); a = r[0]; b = r[1]; }
DI f32x4 zero4() { return f32x4{0.f, 0.f, 0.f, 0.f}; }
DI float fmax3(float a, float b, float c) { float r; asm("v_max3_f32 %0, %1, %2, %3" : "=v"(r) : "v"(a), "v"(b), "v"(c)); return r; }
DI float fmin3(float a, float b, float c) { float r; asm("v_min3_f32 %0, %1, %2, %3" : "=v"(r) : "v"(a), "v"(b), "v"(c)); return r; }
DI void poll_tag(rsrc_t r, unsigned off, unsigned tag, bool idle) {
  unsigned f0 = __builtin_amdgcn_raw_buffer_load_b32(r, off, 0, 16);
#pragma unroll 1
  for (int sp = 0; sp < SPIN_G; ++sp) {
    asm volatile("" ::: "memory");
    const unsigned f1 = __builtin_amdgcn_raw_buffer_load_b32(r, off, 0, 16);
    if (__all(idle || f0 == tag)) return;
    asm volatile("" ::: "memory");
    f0 = __builtin_amdgcn_raw_buffer_load_b32(r, off, 0, 16);
    if (__all(idle || f1 == tag)) return;
  }
}
DI void st_tag(rsrc_t r, unsigned off, unsigned tag) { __builtin_amdgcn_raw_buffer_store_b32(tag, r, off, 0, 16); }

DI int krow(int rho) { return 8 * ((rho >> 2) & 3) + 4 * (rho >> 4) + (rho & 3); }

template <bool ISB> DI void feature_map(const f32x4 (&P)[5], unsigned (&ph)[ISB ? 18 : 10]) {
  float p[18];
#pragma unroll
  for (int f = 0; f < 18; ++f) p[f] = P[f >> 2][f & 3];
  if constexpr (ISB) {
    float mx = fmax3(p[0], p[1], p[2]), mn = fmin3(p[0], p[1], p[2]);
#pragma unroll
    for (int f = 3; f < 17; f += 2) { mx = fmax3(mx, p[f], p[f + 1]); mn = fmin3(mn, p[f], p[f + 1]); }
    mx = fmax3(mx, p[17], p[17]); mn = fmin3(mn, p[17], p[17]);
    const f32x2 cp = {-mx * LOG2E, -mx * LOG2E}, cn = {mn * LOG2E, mn * LOG2E};
    f32x2 lp = {LOG2E, LOG2E}, ln = {-LOG2E, -LOG2E};
    asm("" : "+v"(lp), "+v"(ln));
    f32x2 ep[9], en[9];
#pragma unroll
    for (int f = 0; f < 9; ++f) {
      const f32x2 x = {p[2 * f], p[2 * f + 1]};
      const f32x2 a = __builtin_elementwise_fma(x, lp, cp), b = __builtin_elementwise_fma(x, ln, cn);
      ep[f] = f32x2{__builtin_amdgcn_exp2f(a[0]), __builtin_amdgcn_exp2f(a[1])};
      en[f] = f32x2{__builtin_amdgcn_exp2f(b[0]), __builtin_amdgcn_exp2f(b[1])};
    }
    f32x2 sp = ep[0], sn = en[0];
#pragma unroll
    for (int f = 1; f < 9; ++f) { sp += ep[f]; sn += en[f]; }
    const float rp = __builtin_amdgcn_rcpf(sp[0] + sp[1]), rn = __builtin_amdgcn_rcpf(sn[0] + sn[1]);
#pragma unroll
    for (int f = 0; f < 9; ++f) {
      const f32x2 a = ep[f] * rp, b = en[f] * rn;
      ph[f] = pk2(a[0], a[1]);
      ph[9 + f] = pk2(b[0], b[1]);
    }
  } else {
    f32x2 l2 = {LOG2E, LOG2E}, eps = {1e-6f, 1e-6f};
    asm("" : "+v"(l2), "+v"(eps));
#pragma unroll
    for (int f = 0; f < 9; ++f) {
      const f32x2 lo = f32x2{fminf(p[2 * f], 0.f), fminf(p[2 * f + 1], 0.f)} * l2;
      const f32x2 hi = f32x2{fmaxf(p[2 * f], 0.f), fmaxf(p[2 * f + 1], 0.f)};
      const f32x2 y = (hi + f32x2{__builtin_amdgcn_exp2f(lo[0]), __builtin_amdgcn_exp2f(lo[1])}) + eps;
      ph[f] = pk2(y[0], y[1]);
    }
    ph[9] = 0u;
  }
}

struct WF { bf16x8 a[5][2]; u32x2 h[5]; };
DI void load_wf_tile(LAS char* lds, int b0, int b1, WF& f, int t) {
  f.a[t][0] = lds128(lds, b0 + (2 * t) * 1024);
  f.a[t][1] = lds128(lds, b0 + (2 * t + 1) * 1024);
  f.h[t] = *(LAS u32x2*)(lds + b1 + t * 512);
}
DI void logits2(const WF& f, const f32x4 (&t0)[2], const f32x4 (&t1)[2], const f32x4 (&t2)[2], const f32x4 (&t3)[2],
                const f32x4 (&t4)[2], f32x4 (&P)[2][5]) {
#pragma unroll
  for (int m = 0; m < 2; ++m) {
    const bf16x8 b0 = pack_pair(t0[m], t1[m]), b1 = pack_pair(t2[m], t3[m]);
    const u32x2 b2 = u32x2{pk2(t4[m][0], t4[m][1]), pk2(t4[m][2], t4[m][3])};
#pragma unroll
    for (int t = 0; t < 5; ++t) P[m][t] = mfma(f.a[t][0], b0, zero4());
#pragma unroll
    for (int t = 0; t < 5; ++t) P[m][t] = mfma(f.a[t][1], b1, P[m][t]);
#pragma unroll
    for (int t = 0; t < 5; ++t) P[m][t] = mfma16(f.h[t], b2, P[m][t]);
  }
}
DI int wf_offset(int h, int d, int f) {
  const int col = 18 * h + d, s = col >> 5, ln = 16 * ((col >> 2) & 3) + 4 * h + (f & 3), tt = f >> 2;
  return s < 2 ? (tt * 2 + s) * 1024 + ln * 16 + (((col >> 4) & 1) * 4 + (col & 3)) * 2 : 10240 + tt * 512 + ln * 8 + (col & 3) * 2;
}
template <int N> DI void cwait_lgkm() { __builtin_amdgcn_s_waitcnt(0xC07F | (N << 8)); }
DI void step5(LAS char* lds, f32x4 (&acc)[5][2], const bf16x8 (&wc)[5], bf16x8 (&wn)[5], bf16x8 x0, bf16x8 x1, int next, bool more) {
#pragma unroll
  for (int j = 0; j < 10; ++j) {
    if (j == 6) { if (more) cwait_lgkm<10>(); else cwait_lgkm<0>(); }
    mfma_acc(acc[j >> 1][j & 1], wc[j >> 1], (j & 1) ? x1 : x0);
    if (more && j < 5) wn[j] = lds_tr(lds, next + j * 32, 16 * 160);
    SBAR();
  }
}

template <bool ISB> DI void c_path(const Args& a, LAS char* lds, const int w, const int lane0, const int t, const int g) {
  constexpr int prob = ISB ? 1 : 0;
  constexpr int RP = ISB ? 288 : 160;
  constexpr int C5 = ISB ? 5 : 3;
  constexpr int KVROW = C5 * 16, KVHEAD = ISB ? 1536 : 1024, NCH = 19 * C5;
  char* ws = a.ws;
  const rsrc_t rws = mk_rsrc(ws, WS_USED);
  const unsigned kvp_grp = WS_KVP + (prob * 8 + g) * 16 * KVP_STRIDE;
  const unsigned obuf = WS_O + (prob * 16 + t) * O_STRIDE;
  constexpr int NU = ISB ? 9 : 5, UMAIN = NU - 1;
  f32x4 acc[9][2];
  WF wf;
  unsigned T;
  int a_v[5][2], a_one, a_prow, a_pm, a_pl, a_b0, a_b1, a_mu, a_al, a_la;
  unsigned o_p0, o_p1, o_mu, o_ml;
  STAMP_DECL
  {
    const int lane = lane0, tid = w * 64 + lane, i = lane & 15, gg = lane >> 4;
    STAMP(0); CSTAMP(43);
    T = __hip_atomic_load((GAS unsigned*)(ws + WS_E + prob * 256), RLX_AGENT) + 1u;
    u32x4 wel = {0u, 0u, 0u, 0u};
    if constexpr (ISB) wel = *(const u32x4*)((const char*)a.wphi + g * 2592 + (tid < 162 ? tid : 0) * 16);
    unsigned pf0 = 0u, pf1 = 0u;
    if (PREF) {
      const int r32 = blockIdx.x >> 3, row = 18 * r32 + tid / 9, ln = tid - 9 * (tid / 9);
      const rsrc_t rq = mk_rsrc(ISB ? a.wB : a.wA, 576 * WROW), ro2 = mk_rsrc(ISB ? a.woB : a.woA, 576 * XROW);
      pf0 = __builtin_amdgcn_raw_buffer_load_b32(rq, tid < 162 ? row * WROW + ln * 128 : OOB, 0, 0);
      pf1 = __builtin_amdgcn_raw_buffer_load_b32(ro2, tid < 162 ? row * XROW + ln * 128 : OOB, 0, 0);
    }
    {
      const u32x4 s = {SENT, SENT, SENT, SENT};
#pragma unroll
      for (int k = 0; k < (4 * KVHEAD / 16 + 255) / 256; ++k) {
        const int n = tid + 256 * k;
        st_sc1(s, rws, n < 4 * KVHEAD / 16 ? kvp_grp + t * KVP_STRIDE + n * 16 : OOB);
      }
    }
    if (tid < 64) *(LAS unsigned*)(lds + L_SYNC + tid * 4) = tid == SY_TAG ? T : 0u;
    if (tid < 32) *(LAS unsigned*)(lds + L_ZERO + tid * 4) = 0u;
#pragma unroll
    for (int n = 0; n < 9; ++n) { acc[n][0] = zero4(); acc[n][1] = zero4(); asm volatile("" : "+v"(acc[n][0]), "+v"(acc[n][1])); }
    const int xa0 = (32 * w + i) * 128 + ((gg ^ ((i >> 1) & 7)) * 16), xa1 = xa0 ^ 64;
    const int wa = WPART + (4 * gg + (i >> 2)) * 288 + (i & 3) * 8;
    const int wm = wa + ((i & 2) ? 256 : 128);
    {
      const int xs = ((i >> 2) & 3) << 3;
      const int vrow = L_V + (32 * w + i) * 160;
#pragma unroll
      for (int n = 0; n < 5; ++n) {
#pragma unroll
        for (int pp = 0; pp < 2; ++pp) {
          const int c0 = (n < 4 ? 16 * n : 56) + 2 * pp, h0 = c0 / 18;
          const int thr = (18 * (h0 + 1) - c0 + 3) / 4;
          a_v[n][pp] = opaque(vrow + ((2 * c0 + 4 * h0 + 8 * gg + ((thr < 4 && gg >= thr) ? 4 : 0)) ^ xs));
        }
      }
      a_one = opaque(vrow + ((40 * gg + 36) ^ xs));
      a_prow = opaque(L_PHI + (32 * w + i) * RP);
      a_pm = opaque((UMAIN * gg * 8) ^ xs);
      a_pl = opaque(L_PHI + (32 * w + i) * RP + (((4 * UMAIN + gg) * 8) ^ xs));
      const int q = i >> 2, p = i & 3;
      const int pa = L_PHI + (4 * gg + q) * RP, va = L_V + (4 * gg + q) * 160;
      a_b0 = opaque(pa + ((UMAIN * w + p) ^ gg) * 8); a_b1 = opaque(pa + ((UMAIN * w + 4 + p) ^ gg) * 8);
      a_mu = opaque(pa + ((4 * UMAIN + p) ^ gg) * 8);
      a_al = opaque(va + ((5 * w + p) ^ gg) * 8); a_la = opaque(va + ((5 * p + 4) ^ gg) * 8);
      const unsigned mine = kvp_grp + t * KVP_STRIDE + w * KVHEAD;
      const bool lam = (i >> 2) == w && (i & 3) < 3;
      const int el = 16 + (i & 3), ch = ISB ? ((gg & 1) << 1) | (gg >> 1) : gg >> 1;
      const unsigned o_main = mine + i * KVROW + ch * 16, o_lam = lam ? mine + el * KVROW + ch * 16 : OOB;
      o_p0 = opaque(ISB || !(gg & 1) ? o_main : o_lam); o_p1 = opaque(o_lam);
      o_mu = opaque(gg == w ? mine + i * KVROW + (C5 - 1) * 16 : OOB);
      o_ml = opaque((gg == w && lam) ? mine + el * KVROW + (C5 - 1) * 16 : OOB);
    }
    bf16x8 wb[2][9], xb[2][2];
    auto kstep = [&](auto SC, int xo, int wo, int wmo) {
      constexpr int S = decltype(SC)::value, N = S ^ 1;
#pragma unroll
      for (int n = 0; n < 9; ++n) {
        mfma_acc(acc[n][0], wb[S][n], xb[S][0]);
        if (n == 0) { xb[N][0] = lds128(lds, xo); xb[N][1] = lds128(lds, xo + 2048); }
        else if (n <= 4) wb[N][2 * n - 1] = lds_tr(lds, 2 * n - 1 < 8 ? wo + (2 * n - 1) * 32 + (2 * n - 1 >= 4 ? 16 : 0) : wmo, 4608);
        mfma_acc(acc[n][1], wb[S][n], xb[S][1]);
        if (n <= 4) wb[N][2 * n] = lds_tr(lds, 2 * n < 8 ? wo + (2 * n) * 32 + (2 * n >= 4 ? 16 : 0) : wmo, 4608);
        SBAR();
      }
    };
    using I0 = std::integral_constant<int, 0>;
    using I1 = std::integral_constant<int, 1>;
    STAMP(1);
    barrier();
    STAMP(2); CSTAMP(44);
    {
      const int xo = opaque(xa0 + 3 * SLOT), wo = opaque(wa + 3 * SLOT), wmo = opaque(wm + 3 * SLOT);
      xb[0][0] = lds128(lds, xo); xb[0][1] = lds128(lds, xo + 2048);
#pragma unroll
      for (int n = 0; n < 9; ++n) wb[0][n] = lds_tr(lds, n < 8 ? wo + n * 32 + (n >= 4 ? 16 : 0) : wmo, 4608);
    }
#pragma unroll
    for (int c = 0; c < 9; ++c) {
      {
        const int base = ((c + 3) & 3) * SLOT;
        const int xo = opaque(xa1 + base), wo = opaque(wa + base + 9216), wmo = opaque(wm + base + 9216);
        cwait_lgkm0();
        SBAR();
        kstep(I0{}, xo, wo, wmo);
      }
      if (c == 0) {
        const u32x4 z = {0u, 0u, 0u, 0u};
#pragma unroll
        for (int k = 0; k < 4; ++k) { const int n = tid + 256 * k; if (n < 800) *(LAS u32x4*)(lds + L_WF + n * 16) = z; }
      }
      if (c == 1) {
        if constexpr (ISB) {
          if (tid < 162) {
#pragma unroll
            for (int j = 0; j < 8; ++j) {
              const int idx = tid * 8 + j, h = idx / 324, rem = idx - h * 324, d = rem / 18, f = rem - d * 18;
              const unsigned u = wel[j >> 1];
              *(LAS unsigned short*)(lds + L_WF + wf_offset(h, d, f)) = (unsigned short)(j & 1 ? u >> 16 : u);
            }
          }
        } else if (tid < 72) {
          const int h = tid / 18, f = tid - h * 18;
          *(LAS unsigned short*)(lds + L_WF + wf_offset(h, f, f)) = (unsigned short)0x3F80;
        }
      }
      if (c == 2) {
        const u32x4 s = {SENT, SENT, SENT, SENT};
#pragma unroll
        for (int k = 0; k < 5; ++k) {
          const int n = tid + 256 * k, tile = n / 144, rem = n - 144 * tile;
          st_sc1(s, rws, n < 1152 ? obuf + (tile * 72 + 9 * g) * 256 + rem * 16 : OOB);
        }
      }
      if (c == 5) { asm volatile("" :: "v"(pf0), "v"(pf1)); wait_vm<0>(); }
      if (c == 6 && tid == 0) __hip_atomic_store((GAS unsigned*)(ws + WS_F1 + (prob * 128 + t * 8 + g) * 128), T, RLX_AGENT);
      cwait_lgkm0();
      if (c < 8) {
        barrier();
        const int base = ((c + 4) & 3) * SLOT;
        const int xo = opaque(xa0 + base), wo = opaque(wa + base), wmo = opaque(wm + base);
        SBAR();
        kstep(I1{}, xo, wo, wmo);
      } else {
        lds_arrive(lds, SY_KV8);
        const int b0 = opaque(L_WF + lane * 16), b1 = opaque(L_WF + 10240 + lane * 8);
        SBAR();
#pragma unroll
        for (int n = 0; n < 9; ++n) {
          mfma_acc(acc[n][0], wb[1][n], xb[1][0]);
          if (n < 5) load_wf_tile(lds, b0, b1, wf, n);
          mfma_acc(acc[n][1], wb[1][n], xb[1][1]);
          SBAR();
        }
        lds_wait(lds, SY_KV8, 8u);
        mfma_fence();
      }
    }
  }
  const int lane = opaque(lane0), tid = w * 64 + lane, i = lane & 15, gg = lane >> 4;
  GAS unsigned* fp = (GAS unsigned*)(ws + WS_F1 + (prob * 128 + ((lane & 16) ? t * 8 + (lane & 7) : (lane & 15) * 8 + g)) * 128);
  const unsigned f1_early = __hip_atomic_load(fp, RLX_AGENT);
  __builtin_amdgcn_s_setprio(2);
  STAMP(3); CSTAMP(45);

  {
    f32x4 P[2][5];
    bf16x8 kb[2][2];
    u32x2 kb2[2];
#pragma unroll
    for (int m = 0; m < 2; ++m) {
      kb[m][0] = pack_pair(acc[0][m], acc[1][m]); kb[m][1] = pack_pair(acc[2][m], acc[3][m]);
      kb2[m] = u32x2{pk2(acc[8][m][0], acc[8][m][1]), pk2(acc[8][m][2], acc[8][m][3])};
    }
    SBAR();
#pragma unroll
    for (int st = 0; st < 30; ++st) {
      const int m = st / 15, s = (st % 15) / 5, tt = st % 5;
      if (s == 0) P[m][tt] = mfma(wf.a[tt][0], kb[m][0], zero4());
      if (s == 1) P[m][tt] = mfma(wf.a[tt][1], kb[m][1], P[m][tt]);
      if (s == 2) P[m][tt] = mfma16(wf.h[tt], kb2[m], P[m][tt]);
      if (st < 20) {
        const int n = st >> 2, pp = (st >> 1) & 1, mv = st & 1;
        const f32x4 v = n < 4 ? acc[4 + n][mv] : acc[8][mv];
        const int am = n < 4 ? a_v[n][pp] + mv * 2560 : (gg >= 2 ? a_v[n][pp] + mv * 2560 : L_ZERO + 124);
        *(LAS unsigned*)(lds + am) = pk2(v[2 * pp], v[2 * pp + 1]);
      }
      SBAR();
    }
    *(LAS unsigned*)(lds + a_one) = 0x00003F80u; *(LAS unsigned*)(lds + a_one + 2560) = 0x00003F80u;
    FSTAMP(18);
#pragma unroll
    for (int m = 0; m < 2; ++m) {
      unsigned ph[ISB ? 18 : 10];
      feature_map<ISB>(P[m], ph);
#pragma unroll
      for (int j = 0; j < NU; ++j)
        *(LAS u32x2*)(lds + (j < UMAIN ? a_prow + (a_pm ^ (j * 8)) : a_pl) + m * 16 * RP) = u32x2{ph[2 * j], ph[2 * j + 1]};
      FSTAMP(19 + m);
    }
  }
  STAMP(4);
  lds_arrive(lds, SY_PHI);
  const bool f1ok = __all(f1_early == T);
  lds_wait(lds, SY_PHI);
  STAMP(5);

  {
    bf16x8 al[4], la[4], b0[4], mu[4], b1[4];
    f32x4 d0 = zero4(), d1 = zero4(), d2 = zero4(), d3 = zero4(), d4 = zero4(), d5 = zero4();
    asm volatile("" : "+v"(d0), "+v"(d1), "+v"(d2), "+v"(d3), "+v"(d4), "+v"(d5));
    auto rd = [&](int ks) {
      al[ks] = lds_tr(lds, a_al + ks * 32 * 160, 16 * 160);
      b0[ks] = lds_tr(lds, a_b0 + ks * 32 * RP, 16 * RP);
      if constexpr (ISB) b1[ks] = lds_tr(lds, a_b1 + ks * 32 * RP, 16 * RP);
      la[ks] = lds_tr(lds, a_la + ks * 32 * 160, 16 * 160);
      mu[ks] = lds_tr(lds, a_mu + ks * 32 * RP, 16 * RP);
      SBAR();
    };
    auto mm = [&](int ks) {
      mfma_acc(d0, b0[ks], al[ks]);
      if constexpr (ISB) mfma_acc(d1, b1[ks], al[ks]);
      mfma_acc(d2, b0[ks], la[ks]);
      if constexpr (ISB) mfma_acc(d3, b1[ks], la[ks]);
      mfma_acc(d4, mu[ks], al[ks]); mfma_acc(d5, mu[ks], la[ks]);
      SBAR();
    };
    constexpr int RD = ISB ? 10 : 8;
    rd(0); rd(1);
    __builtin_amdgcn_s_waitcnt(0xC07F & ~0xF00 | (RD << 8)); SBAR(); mm(0); rd(2);
    __builtin_amdgcn_s_waitcnt(0xC07F & ~0xF00 | (RD << 8)); SBAR(); mm(1); rd(3);
    __builtin_amdgcn_s_waitcnt(0xC07F & ~0xF00 | (RD << 8)); SBAR(); mm(2);
    cwait_lgkm0(); SBAR(); mm(3);
    mfma_fence();
    if constexpr (ISB) {
      unsigned x0 = pk2(d0[0], d0[1]), x1 = pk2(d0[2], d0[3]), y0 = pk2(d1[0], d1[1]), y1 = pk2(d1[2], d1[3]);
      swap16(x0, y0); swap16(x1, y1);
      st_sc1(u32x4{nosent(x0), nosent(x1), nosent(y0), nosent(y1)}, rws, o_p0);
      x0 = pk2(d2[0], d2[1]); x1 = pk2(d2[2], d2[3]); y0 = pk2(d3[0], d3[1]); y1 = pk2(d3[2], d3[3]);
      swap16(x0, y0); swap16(x1, y1);
      st_sc1(u32x4{nosent(x0), nosent(x1), nosent(y0), nosent(y1)}, rws, o_p1);
    } else {
      unsigned x0 = pk2(d0[0], d0[1]), x1 = pk2(d0[2], d0[3]), y0 = pk2(d2[0], d2[1]), y1 = pk2(d2[2], d2[3]);
      swap16(x0, y0); swap16(x1, y1);
      st_sc1(u32x4{nosent(x0), nosent(x1), nosent(y0), nosent(y1)}, rws, o_p0);
    }
    st_sc1(u32x4{nosent(pk2(d4[0], d4[1])), nosent(pk2(d4[2], d4[3])), 0u, 0u}, rws, o_mu);
    st_sc1(u32x4{nosent(pk2(d5[0], d5[1])), nosent(pk2(d5[2], d5[3])), 0u, 0u}, rws, o_ml);
    st_tag(rws, lane == 0 ? WS_H1 + (((prob * 8 + g) * 16 + t) * 4 + w) * 128 : OOB, T);
    lds_arrive(lds, SY_KVDONE);
  }
  __builtin_amdgcn_s_setprio(0);
  STAMP(6); CSTAMP(46);
  const rsrc_t rwo = mk_rsrc(ISB ? a.woB : a.woA, 576 * XROW);
  const unsigned lb = (unsigned)(size_t)lds;
  unsigned vo[5];
#pragma unroll
  for (int n = 0; n < 5; ++n) {
    const int nn = n * 64 + lane, rho = nn / 10, cp = nn - 10 * rho;
    vo[n] = krow(rho) * XROW + 144 * g + 16 * min(cp, 8);
  }
  if (WO_EARLY) {
    lds_wait(lds, SY_KVDONE);
#pragma unroll
    for (int kk = 0; kk < 2; ++kk) {
      const int ks = w + 4 * kk;
      if (kk == 0 || w < 2) {
#pragma unroll
        for (int n = 0; n < 5; ++n) dma16(rwo, vo[n], ks * 32 * XROW, lb + L_WO0 + ks * 5120 + n * 1024);
      }
    }
  }

  {
    if (!f1ok) {
#pragma unroll 1
      for (int sp = 0; sp < SPIN_G; ++sp) {
        const unsigned f = __hip_atomic_load(fp, RLX_AGENT);
        if (__all(f == T)) break;
      }
    }
    STAMP(7);
    const rsrc_t rkv = mk_rsrc(ws + kvp_grp, 16 * KVP_STRIDE);
    constexpr int NG = (NCH + 15) / 16;
    const unsigned one = (i & 1) ? 0x3F800000u : 0x3F80u;
    const bf16x8 sel = frag_of((i >> 1) == 0 ? one : 0u, (i >> 1) == 1 ? one : 0u, (i >> 1) == 2 ? one : 0u, (i >> 1) == 3 ? one : 0u);
    const unsigned off0 = gg * KVP_STRIDE + w * KVHEAD + i * 16;
    const unsigned offl = 16 * (NG - 1) + i < NCH ? off0 + (NG - 1) * 256 : OOB;
    const int kvw = L_PHI + w * KVHEAD + i * 16 + gg * 8;
    poll_tag(rws, lane < 16 ? WS_H1 + (((prob * 8 + g) * 16 + lane) * 4 + w) * 128 : OOB, T, lane >= 16);
    __builtin_amdgcn_s_setprio(2);
    STAMP(8);
    f32x4 sum[NG];
#pragma unroll 1
    for (int pass = 0; pass < 2; ++pass) {
      asm volatile("" ::: "memory");
      u32x4 v[NG][4];
#pragma unroll
      for (int cg = 0; cg < NG; ++cg) {
#pragma unroll
        for (int u = 0; u < 4; ++u) { v[cg][u] = ld_sc1(rkv, cg < NG - 1 ? off0 + cg * 256 : offl, 4 * u * KVP_STRIDE); SBAR(); }
      }
      float chk = 0.f;
#pragma unroll
      for (int cg = 0; cg < NG; ++cg) {
        f32x4 d = zero4();
#pragma unroll
        for (int u = 0; u < 4; ++u) d = mfma(sel, __builtin_bit_cast(bf16x8, v[cg][u]), d);
        sum[cg] = d;
        chk += d[0];
      }
      if (pass == 1 || __all(chk == chk)) break;
#pragma unroll 1
      for (int sp = 0; sp < SPIN_G; ++sp) {
        unsigned mx = 0u;
#pragma unroll 1
        for (int n = 0; n < NG * 4; ++n) {
          asm volatile("" ::: "memory");
          const u32x4 x = ld_sc1(rkv, (n >> 2) < NG - 1 ? off0 + (n >> 2) * 256 : offl, 4 * (n & 3) * KVP_STRIDE);
          mx = umax3(mx, x[0], x[1]); mx = umax3(mx, x[2], x[3]);
        }
        if (__all(mx != SENT)) break;
      }
    }
    lds_wait(lds, SY_KVDONE);
    if (gg < 2) {
#pragma unroll
      for (int cg = 0; cg < NG; ++cg)
        if (cg < NG - 1 || 16 * cg + i < NCH)
          *(LAS u32x2*)(lds + kvw + cg * 256) = u32x2{pk2(sum[cg][0], sum[cg][1]), pk2(sum[cg][2], sum[cg][3])};
    }
    lds_arrive(lds, SY_KVIMG);
  }
  STAMP(9);
  __builtin_amdgcn_s_setprio(0);
  {
    lds_wait(lds, SY_QDONE);
#pragma unroll
    for (int kk = 0; kk < 5; ++kk) {
      const int ks = (WO_EARLY ? 6 : 0) + w + 4 * kk;
      if (ks < 18) {
#pragma unroll
        for (int n = 0; n < 5; ++n) dma16(rwo, vo[n], ks * 32 * XROW, lb + L_WQ + ks * 5120 + n * 1024);
      }
    }
    wait_vm<0>();
    lds_arrive(lds, SY_WO);
  }
  STAMP(10);

  {
    const rsrc_t ro = mk_rsrc(ws + obuf, O_STRIDE);
    const rsrc_t rout = mk_rsrc(a.out, 2048 * OUTROW);
    const unsigned orow = (t * 128 + 32 * w + i) * OUTROW + prob * 1152 + 144 * g;
    const unsigned o_pair = orow + (2 * (gg & 1) + (gg >> 1)) * 16, o_last = gg < 2 ? orow + gg * 16 * OUTROW + 128 : OOB;
    const int wl = (4 * gg + (i >> 2)) * 160 + (i & 3) * 8;
    const int wa1 = opaque((WO_EARLY ? L_WO0 : L_WQ) + wl), wa2 = opaque(L_WQ + wl), wa3 = opaque(L_WQ + wl + 12 * 5120);
    lds_wait(lds, SY_WO);
    poll_tag(rws, lane < 8 ? WS_H2 + (((prob * 16 + t) * 8 + lane) * 4 + w) * 128 : OOB, T, lane >= 8);
    __builtin_amdgcn_s_setprio(2);
    STAMP(11);
    f32x4 oc[5][2];
#pragma unroll 1
    for (int pass = 0; pass < 2; ++pass) {
      asm volatile("" ::: "memory");
      u32x4 of[18][2];
#pragma unroll
      for (int ks = 0; ks < 18; ++ks) {
#pragma unroll
        for (int m = 0; m < 2; ++m) { of[ks][m] = ld_sc1(ro, lane * 16, ((2 * w + m) * 72 + 4 * ks) * 256); SBAR(); }
      }
      bf16x8 wq[2][5];
#pragma unroll
      for (int n = 0; n < 5; ++n) {
        wq[0][n] = lds_tr(lds, wa1 + n * 32, 16 * 160);
        oc[n][0] = zero4(); oc[n][1] = zero4();
        asm volatile("" : "+v"(oc[n][0]), "+v"(oc[n][1]));
      }
      SBAR();
#define OUT_STEP(ks) do { cwait_vm_lgkm4<34 - 2 * (ks)>(); SBAR(); \
      step5(lds, oc, wq[(ks) & 1], wq[((ks) + 1) & 1], __builtin_bit_cast(bf16x8, of[ks][0]), __builtin_bit_cast(bf16x8, of[ks][1]), \
            (ks) + 1 < 6 ? wa1 + ((ks) + 1) * 5120 : (ks) + 1 < 12 ? wa2 + ((ks) + 1) * 5120 : wa3 + ((ks) + 1 - 12) * 5120, (ks) < 17); } while (0)
      OUT_STEP(0); OUT_STEP(1); OUT_STEP(2); OUT_STEP(3); OUT_STEP(4); OUT_STEP(5); OUT_STEP(6); OUT_STEP(7); OUT_STEP(8);
      OUT_STEP(9); OUT_STEP(10); OUT_STEP(11); OUT_STEP(12); OUT_STEP(13); OUT_STEP(14); OUT_STEP(15); OUT_STEP(16); OUT_STEP(17);
#undef OUT_STEP
      mfma_fence();
      const float chk = oc[0][0][0] + oc[0][1][0];
      if (pass == 1 || __all(chk == chk)) break;
#pragma unroll 1
      for (int sp = 0; sp < SPIN_G; ++sp) {
        unsigned mx = 0u;
#pragma unroll 1
        for (int n = 0; n < 36; ++n) {
          asm volatile("" ::: "memory");
          const u32x4 x = ld_sc1(ro, lane * 16, ((2 * w + (n & 1)) * 72 + 4 * (n >> 1)) * 256);
          mx = umax3(mx, x[0], x[1]); mx = umax3(mx, x[2], x[3]);
        }
        if (__all(mx != SENT)) break;
      }
    }
    STAMP(12);
#pragma unroll
    for (int m = 0; m < 2; ++m) {
#pragma unroll
      for (int n = 0; n < 4; n += 2) {
        unsigned x0 = pk2(oc[n][m][0], oc[n][m][1]), x1 = pk2(oc[n][m][2], oc[n][m][3]);
        unsigned y0 = pk2(oc[n + 1][m][0], oc[n + 1][m][1]), y1 = pk2(oc[n + 1][m][2], oc[n + 1][m][3]);
        swap16(x0, y0); swap16(x1, y1);
        __builtin_amdgcn_raw_buffer_store_b128(u32x4{x0, x1, y0, y1}, rout, o_pair + m * 16 * OUTROW + n * 32, 0, 0);
      }
    }
    unsigned x0 = pk2(oc[4][0][0], oc[4][0][1]), x1 = pk2(oc[4][0][2], oc[4][0][3]);
    unsigned y0 = pk2(oc[4][1][0], oc[4][1][1]), y1 = pk2(oc[4][1][2], oc[4][1][3]);
    swap16(x0, y0); swap16(x1, y1);
    __builtin_amdgcn_raw_buffer_store_b128(u32x4{x0, x1, y0, y1}, rout, o_last, 0, 0);
    if (t == 0 && g == 0 && tid == 0) __hip_atomic_store((GAS unsigned*)(ws + WS_E + prob * 256), T, RLX_AGENT);
  }
  STAMP(13); CSTAMP(47);
  STAMP_OUT(w);
}

template <bool ISB> DI void h_path(const Args& a, LAS char* lds, const int w, const int lane, const int t, const int g) {
  constexpr int prob = ISB ? 1 : 0;
  constexpr int C5 = ISB ? 5 : 3;
  constexpr int KVROW = C5 * 16, KVHEAD = ISB ? 1536 : 1024;
  const int i = lane & 15, gg = lane >> 4;
  const rsrc_t rx = mk_rsrc((const char*)(ISB ? a.xB : a.xA) + (size_t)t * 128 * XROW, 128 * XROW);
  const rsrc_t rw = mk_rsrc(ISB ? a.wB : a.wA, 576 * WROW);
  const unsigned lb = (unsigned)(size_t)lds;
  STAMP_DECL
  STAMP(0);
  const int rot = ROT == 1 ? (g + 2 * (t & 3) + ((t >> 1) & 1)) % 9 : ROT == 2 ? (g + 8 * (t & 3)) % 9 : 0;
  auto kchunk = [&](int c) { const int k = c + rot; return k >= 9 ? k - 9 : k; };
  unsigned vx[2], vw[5], vq[8];
#pragma unroll
  for (int par = 0; par < 2; ++par) vx[par] = (lane >> 3) * XROW + (((lane & 7) ^ ((4 * par + (lane >> 4)) & 7)) * 16);
  auto issue_x = [&](int c) {
    const unsigned sb = lb + ((c + 3) & 3) * SLOT;
#pragma unroll
    for (int jj = 0; jj < 4; ++jj) dma16(rx, vx[jj & 1], (4 * w + jj) * 8 * XROW + kchunk(c) * 128, sb + (4 * w + jj) * 1024);
  };
  issue_x(0);
#pragma unroll
  for (int n = 0; n < 5; ++n) {
    const int nn = (w + 4 * n) * 64 + lane, r64 = nn / 18, cp = nn - 18 * r64;
    const int co = cp < 9 ? 1152 + 16 * cp : 2304 + 16 * (cp - 9);
    vw[n] = ((r64 & 32) + krow(r64 & 31)) * WROW + 144 * g + co;
  }
  auto issue_w = [&](int c) {
    const unsigned sb = lb + ((c + 3) & 3) * SLOT;
#pragma unroll
    for (int n = 0; n < 5; ++n)
      if (n < 4 || w < 2) dma16(rw, vw[n], kchunk(c) * 64 * WROW, sb + WPART + (w + 4 * n) * 1024);
  };
  issue_w(0); issue_x(1); issue_w(1);
  STAMP(1);
  if (w < 2) wait_vm<9>(); else wait_vm<8>();
  STAMP(2);
  barrier();
  issue_x(2); issue_w(2);
#pragma unroll
  for (int n = 0; n < 8; ++n) {
    const int v = w + 4 * n, ks = v / 5, nn = (v - 5 * ks) * 64 + lane, rho = nn / 10, cp = nn - 10 * rho;
    vq[n] = ((ks & 1) * 32 + krow(rho)) * WROW + 144 * g + 16 * min(cp, 8);
  }
  auto issue_wq = [&](int grp) {
#pragma unroll
    for (int n = 0; n < 8; ++n)
      if (n < 7 || w < 2)
        dma16(rw, vq[n], kchunk(3 * grp + ((w + 4 * n) / 5 >> 1)) * 64 * WROW, lb + L_WQ + grp * 30720 + (w + 4 * n) * 1024);
  };
  bf16x8 X[18][2];
  const int xa0 = (32 * w + i) * 128 + ((gg ^ ((i >> 1) & 7)) * 16), xa1 = xa0 ^ 64;
#pragma unroll
  for (int c = 0; c < 9; ++c) {
    const int base = ((c + 3) & 3) * SLOT;
    const int xo0 = opaque(xa0 + base), xo1 = opaque(xa1 + base);
    X[2 * c][0] = lds128(lds, xo0); X[2 * c][1] = lds128(lds, xo0 + 2048);
    X[2 * c + 1][0] = lds128(lds, xo1); X[2 * c + 1][1] = lds128(lds, xo1 + 2048);
    if (c <= 5) { issue_x(c + 3); issue_w(c + 3); }
    if (c <= 5) { if (w < 2) wait_vm<18>(); else wait_vm<16>(); }
    if (c == 6) { if (w < 2) wait_vm<9>(); else wait_vm<8>(); }
    if (c == 7) wait_vm<0>();
    if (c < 8) barrier(); else lds_arrive(lds, SY_KV8);
  }
  issue_wq(0); issue_wq(1); issue_wq(2);
  __builtin_amdgcn_s_setprio(1);
  STAMP(3);
  f32x4 qa[5][2];
#pragma unroll
  for (int n = 0; n < 5; ++n) { qa[n][0] = zero4(); qa[n][1] = zero4(); asm volatile("" : "+v"(qa[n][0]), "+v"(qa[n][1])); }
  const int wa2 = opaque(L_WQ + (4 * gg + (i >> 2)) * 160 + (i & 3) * 8), wa3 = opaque(wa2 + 12 * 5120);
  bf16x8 wq[2][5];
  if (w < 2) wait_vm<16>(); else wait_vm<14>();
  lds_arrive(lds, SY_WQ0);
  lds_wait(lds, SY_WQ0);
  STAMP(4);
#pragma unroll
  for (int n = 0; n < 5; ++n) wq[0][n] = lds_tr(lds, wa2 + n * 32, 16 * 160);
#pragma unroll
  for (int ks = 0; ks < 18; ++ks) {
    cwait_lgkm<4>();
    if (ks == 5) { if (w < 2) wait_vm<8>(); else wait_vm<7>(); lds_arrive(lds, SY_WQ1); lds_wait(lds, SY_WQ1); STAMP(5); }
    if (ks == 11) { wait_vm<0>(); lds_arrive(lds, SY_WQ2); lds_wait(lds, SY_WQ2); STAMP(6); }
    SBAR();
    step5(lds, qa, wq[ks & 1], wq[(ks + 1) & 1], X[ks][0], X[ks][1], ks + 1 < 12 ? wa2 + (ks + 1) * 5120 : wa3 + (ks + 1 - 12) * 5120, ks < 17);
  }
  WF wf;
  {
    const int b0 = opaque(L_WF + lane * 16), b1 = opaque(L_WF + 10240 + lane * 8);
#pragma unroll
    for (int n = 0; n < 5; ++n) load_wf_tile(lds, b0, b1, wf, n);
  }
  mfma_fence();
  lds_arrive(lds, SY_QDONE);
  STAMP(7);
  const unsigned T = *(LAS unsigned*)(lds + L_SYNC + SY_TAG * 4);
  unsigned ph[2][ISB ? 18 : 10];
  {
    f32x4 P[2][5];
    logits2(wf, qa[0], qa[1], qa[2], qa[3], qa[4], P);
    feature_map<ISB>(P[0], ph[0]);
    feature_map<ISB>(P[1], ph[1]);
#pragma unroll
    for (int j = 0; j < (ISB ? 18 : 10); ++j) asm volatile("" : "+v"(ph[0][j]), "+v"(ph[1][j]));
  }
  int a_kv[5], a_st[9];
  {
    const bool own = (i >> 2) == gg;
#pragma unroll
    for (int n = 0; n < 5; ++n) {
      const int e = 4 * n + (i & 3);
      a_kv[n] = opaque((own && e < 19) ? L_PHI + gg * KVHEAD + e * KVROW : L_ZERO);
    }
#pragma unroll
    for (int pp = 0; pp < 9; ++pp) {
      const int cp = 9 * gg + pp;
      a_st[pp] = opaque(L_V + w * 4608 + (cp >> 2) * 256 + i * 16 + (cp & 3) * 4);
    }
  }
  const rsrc_t ro = mk_rsrc(a.ws + WS_O + (prob * 16 + t) * O_STRIDE, O_STRIDE);
  const unsigned o_dst = (2 * w * 72 + 9 * g) * 256 + lane * 16;
  STAMP(8);
  lds_wait(lds, SY_KVIMG);
  __builtin_amdgcn_s_setprio(3);
  STAMP(9);
  f32x4 O[5][2];
  {
    bf16x8 af[5][C5];
#pragma unroll
    for (int s = 0; s < C5; ++s) {
#pragma unroll
      for (int n = 0; n < 5; ++n) af[n][s] = lds128(lds, a_kv[n] + s * 16);
    }
    SBAR();
    constexpr int NP = ISB ? 18 : 10;
#pragma unroll
    for (int s = 0; s < C5; ++s) {
      bf16x8 bq[2];
#pragma unroll
      for (int m = 0; m < 2; ++m) {
        unsigned u[4];
#pragma unroll
        for (int j = 0; j < 4; ++j) u[j] = 4 * s + j < NP ? ph[m][4 * s + j < NP ? 4 * s + j : 0] : 0u;
        bq[m] = frag_of(u[0], u[1], u[2], u[3]);
      }
#pragma unroll
      for (int n = 4; n >= 0; --n) {
#pragma unroll
        for (int m = 0; m < 2; ++m) O[n][m] = mfma(af[n][s], bq[m], s == 0 ? zero4() : O[n][m]);
      }
    }
  }
  STAMP(10);
#pragma unroll
  for (int m = 0; m < 2; ++m) {
    const float rc = __builtin_amdgcn_rcpf(O[4][m][2] + 1e-6f);
    const f32x2 rc2 = {rc, rc};
#pragma unroll
    for (int pp = 0; pp < 9; ++pp) {
      const int e = 2 * pp;
      const f32x2 y = f32x2{O[e >> 2][m][e & 3], O[(e + 1) >> 2][m][(e + 1) & 3]} * rc2;
      *(LAS unsigned*)(lds + a_st[pp] + m * 2304) = nosent(pk2(y[0], y[1]));
    }
  }
  {
    const int sb = L_V + w * 4608 + lane * 16;
    u32x4 v[5];
#pragma unroll
    for (int k = 0; k < 5; ++k) v[k] = *(LAS u32x4*)(lds + sb + (k < 4 || lane < 32 ? k * 1024 : 0));
#pragma unroll
    for (int k = 0; k < 5; ++k) {
      const int n = lane + 64 * k;
      st_sc1(v[k], ro, n < 288 ? o_dst + k * 1024 + (n >= 144 ? (72 - 9) * 256 : 0) : OOB);
    }
    st_tag(mk_rsrc(a.ws, WS_USED), lane == 0 ? WS_H2 + (((prob * 16 + t) * 8 + g) * 4 + w) * 128 : OOB, T);
  }
  STAMP(11);
  STAMP_OUT(4 + w);
}

__global__ __launch_bounds__(512, 2) void hedgehog_fused(Args a) {
  extern __shared__ __attribute__((aligned(16))) char smem[];
  LAS char* lds = (LAS char*)smem;
  const int b = blockIdx.x, xcd = b & 7, r = b >> 3;
  const int prob = xcd >> 2, t = (xcd & 3) * 4 + (r >> 3), g = r & 7;
  const int wave = __builtin_amdgcn_readfirstlane(threadIdx.x >> 6), lane = threadIdx.x & 63;
  if (wave < 4) {
    if (prob) c_path<true>(a, lds, wave, lane, t, g); else c_path<false>(a, lds, wave, lane, t, g);
  } else {
    if (prob) h_path<true>(a, lds, wave - 4, lane, t, g); else h_path<false>(a, lds, wave - 4, lane, t, g);
  }
}

}

extern "C" void kernel_launch(void* const* d_in, const int* in_sizes, int n_in, void* d_out, int out_size, void* d_ws,
                              size_t ws_size, hipStream_t stream) {
  (void)in_sizes; (void)n_in; (void)out_size; (void)ws_size;
  Args a;
  a.xA = d_in[0]; a.wA = d_in[1]; a.woA = d_in[2]; a.xB = d_in[3]; a.wB = d_in[4]; a.wphi = d_in[5]; a.woB = d_in[6];
  a.out = d_out; a.ws = (char*)d_ws;
  (void)hipFuncSetAttribute((const void*)hedgehog_fused, hipFuncAttributeMaxDynamicSharedMemorySize, LDS_BYTES);
  hedgehog_fused<<<256, 512, LDS_BYTES, stream>>>(a);
}
```

```cpp
#include <hip/hip_runtime.h>
#include <hip/hip_bf16.h>
#include <stdint.h>
#include <type_traits>

typedef __bf16 bf16x8 __attribute__((ext_vector_type(8)));
typedef __bf16 bf16x4 __attribute__((ext_vector_type(4)));
typedef __bf16 bf16x2 __attribute__((ext_vector_type(2)));
typedef short s16x4 __attribute__((ext_vector_type(4)));
typedef float f32x4 __attribute__((ext_vector_type(4)));
typedef float f32x2 __attribute__((ext_vector_type(2)));
typedef unsigned u32x4 __attribute__((ext_vector_type(4)));
typedef unsigned u32x2 __attribute__((ext_vector_type(2)));
#define LAS __attribute__((address_space(3)))
#define GAS __attribute__((address_space(1)))
#define DI __device__ __forceinline__
#define RLX_AGENT __ATOMIC_RELAXED, __HIP_MEMORY_SCOPE_AGENT
#define SBAR() __builtin_amdgcn_sched_barrier(0)
typedef __amdgpu_buffer_rsrc_t rsrc_t;

#define DBG 0
#define PROF 0
#define STAMP(n)
#define CSTAMP(n)
#define STAMP_DECL
#define STAMP_OUT(wv)
#define LSTAMP(n)
#define FSTAMP(n)
#define KV_UNROLL 0
#define ROT 1

namespace {

constexpr int XROW = 1152;
constexpr int WROW = 3456;
constexpr int OUTROW = 2304;
constexpr int SLOT = 34816, WPART = 16384;
constexpr int L_WQ = 0;
constexpr int L_PHI = 92160;
constexpr int L_V = 129024;
constexpr int L_WF = 149504;
constexpr int L_SYNC = 162304;
constexpr int L_ZERO = 162560;
constexpr int LDS_BYTES = 163840;
enum { SY_WQ0 = 0, SY_WQ1, SY_WQ2, SY_QDONE, SY_PHI, SY_KVDONE, SY_KVIMG, SY_WO, SY_KV8, SY_TAG = 63 };
constexpr int WS_E = 0, WS_KVP = 65536, KVP_STRIDE = 6144, WS_O = 2097152, O_STRIDE = 147456;
constexpr int WS_H1 = WS_O + 32 * O_STRIDE, WS_H2 = WS_H1 + 131072;
constexpr int WS_USED = WS_H2 + 131072;
constexpr unsigned SENT = 0xFFFFFFFFu;
constexpr unsigned OOB = 0x7FFF0000u;
constexpr int SPIN_G = 1 << 18, SPIN_L = 1 << 22;
constexpr float LOG2E = 1.4426950408889634f;

struct Args {
  const void* xA; const void* wA; const void* woA; const void* xB; const void* wB; const void* wphi; const void* woB;
  void* out; char* ws;
};

DI f32x4 mfma(bf16x8 a, bf16x8 b, f32x4 c) { return __builtin_amdgcn_mfma_f32_16x16x32_bf16(a, b, c, 0, 0, 0); }
DI f32x4 mfma16(u32x2 a, u32x2 b, f32x4 c) {
  return __builtin_amdgcn_mfma_f32_16x16x16bf16_1k(__builtin_bit_cast(s16x4, a), __builtin_bit_cast(s16x4, b), c, 0, 0, 0);
}
DI void mfma_acc(f32x4& c, bf16x8 a, bf16x8 b) { asm volatile("v_mfma_f32_16x16x32_bf16 %0, %1, %2, %0" : "+v"(c) : "v"(a), "v"(b)); }
template <int N> DI void mfma_fence(f32x4 (&acc)[N][2]) {
  asm volatile("s_nop 7\n\ts_nop 3" : "+v"(acc[N - 1][0]), "+v"(acc[N - 1][1]) :: "memory");
#pragma unroll
  for (int n = 0; n < N - 1; ++n) asm volatile("" : "+v"(acc[n][0]), "+v"(acc[n][1]));
}
DI unsigned pk2(float a, float b) { f32x2 p = {a, b}; return __builtin_bit_cast(unsigned, __builtin_convertvector(p, bf16x2)); }
DI bf16x8 frag_of(unsigned a, unsigned b, unsigned c, unsigned d) { u32x4 v = {a, b, c, d}; return __builtin_bit_cast(bf16x8, v); }
DI bf16x8 pack_pair(f32x4 lo, f32x4 hi) { return frag_of(pk2(lo[0], lo[1]), pk2(lo[2], lo[3]), pk2(hi[0], hi[1]), pk2(hi[2], hi[3])); }
DI bf16x8 lds128(LAS char* l, int off) { return *(LAS bf16x8*)(l + off); }
DI bf16x8 lds_tr(LAS char* l, int off, int second) {
  bf16x4 a = __builtin_amdgcn_ds_read_tr16_b64_v4bf16((LAS bf16x4*)(l + off));
  bf16x4 b = __builtin_amdgcn_ds_read_tr16_b64_v4bf16((LAS bf16x4*)(l + off + second));
  return __builtin_shufflevector(a, b, 0, 1, 2, 3, 4, 5, 6, 7);
}
DI void dma16(rsrc_t r, unsigned voff, unsigned soff, unsigned ldsaddr) {
  asm volatile("s_mov_b32 m0, %0\n\ts_nop 0\n\tbuffer_load_dwordx4 %1, %2, %3 offen lds" ::"s"(ldsaddr), "v"(voff), "s"(r), "s"(soff) : "memory");
}
template <int N> DI void wait_vm() { asm volatile("s_waitcnt vmcnt(%0)" ::"n"(N) : "memory"); }
DI void cwait_lgkm0() { __builtin_amdgcn_s_waitcnt(0xC07F); }
template <int N> DI void cwait_vm_lgkm4() { __builtin_amdgcn_s_waitcnt((N & 15) | 0x70 | (4 << 8) | ((N >> 4) << 14)); }
DI void barrier() { asm volatile("s_waitcnt lgkmcnt(0)\n\ts_barrier" ::: "memory"); }
DI void lds_arrive(LAS char* l, int id) {
  const unsigned addr = (unsigned)(size_t)l + L_SYNC + id * 4, one = 1u;
  unsigned long long save;
  asm volatile("s_waitcnt lgkmcnt(0)\n\ts_mov_b64 %0, exec\n\ts_mov_b64 exec, 1\n\tds_add_u32 %1, %2\n\ts_mov_b64 exec, %0"
               : "=&s"(save) : "v"(addr), "v"(one) : "memory");
}
DI void lds_wait(LAS char* l, int id, unsigned n = 4u) {
  const unsigned addr = (unsigned)(size_t)l + L_SYNC + id * 4;
#pragma unroll 1
  for (int sp = 0; sp < SPIN_L; ++sp) {
    unsigned v;
    asm volatile("ds_read_b32 %0, %1\n\ts_waitcnt lgkmcnt(0)" : "=v"(v) : "v"(addr) : "memory");
    if ((unsigned)__builtin_amdgcn_readfirstlane(v) >= n) break;
    __builtin_amdgcn_s_sleep(1);
  }
}
DI rsrc_t mk_rsrc(const void* p, int bytes) { return __builtin_amdgcn_make_buffer_rsrc((void*)p, 0, bytes, 0x00020000); }
DI void st_sc1(u32x4 v, rsrc_t r, unsigned off) { __builtin_amdgcn_raw_buffer_store_b128(v, r, off, 0, 16); }
DI u32x4 ld_sc1(rsrc_t r, unsigned off, unsigned soff) { return __builtin_amdgcn_raw_buffer_load_b128(r, off, soff, 16); }
DI int opaque(int x) { asm volatile("" : "+v"(x)); return x; }
DI unsigned umax3(unsigned a, unsigned b, unsigned c) { return max(max(a, b), c); }
DI unsigned nosent(unsigned x) { return min(x, 0xFFFFFFFEu); }
DI void swap16(unsigned& a, unsigned& b) { auto r = __builtin_amdgcn_permlane16_swap(a, b, false, false); a = r[0]; b = r[1]; }
DI f32x4 zero4() { return f32x4{0.f, 0.f, 0.f, 0.f}; }
DI float fmax3(float a, float b, float c) { return fmaxf(fmaxf(a, b), c); }
DI float fmin3(float a, float b, float c) { return fminf(fminf(a, b), c); }
DI void poll_tag(rsrc_t r, unsigned off, unsigned tag, unsigned long long mask, unsigned& p0, unsigned& p1) {
  unsigned cnt = SPIN_G;
  asm volatile(
      "buffer_load_dword %0, %3, %4, 0 offen sc1\n"
      "L_poll_%=:\n\t"
      "buffer_load_dword %1, %3, %4, 0 offen sc1\n\t"
      "s_waitcnt vmcnt(1)\n\t"
      "v_cmp_ne_u32 vcc, %5, %0\n\t"
      "s_nop 1\n\t"
      "s_and_b64 vcc, vcc, %6\n\t"
      "s_cbranch_vccz L_done_%=\n\t"
      "buffer_load_dword %0, %3, %4, 0 offen sc1\n\t"
      "s_waitcnt vmcnt(1)\n\t"
      "v_cmp_ne_u32 vcc, %5, %1\n\t"
      "s_nop 1\n\t"
      "s_and_b64 vcc, vcc, %6\n\t"
      "s_cbranch_vccz L_done_%=\n\t"
      "s_sub_u32 %2, %2, 1\n\t"
      "s_cmp_lg_u32 %2, 0\n\t"
      "s_cbranch_scc1 L_poll_%=\n"
      "L_done_%=:"
      : "=&v"(p0), "=&v"(p1), "+s"(cnt) : "v"(off), "s"(r), "s"(tag), "s"(mask) : "vcc", "scc", "memory");
}
DI void st_tag(rsrc_t r, unsigned off, unsigned tag) { __builtin_amdgcn_raw_buffer_store_b32(tag, r, off, 0, 16); }

DI int krow(int rho) { return 8 * ((rho >> 2) & 3) + 4 * (rho >> 4) + (rho & 3); }

template <bool ISB> DI void feature_map(const f32x4 (&P)[5], unsigned (&ph)[ISB ? 18 : 10]) {
  float p[18];
#pragma unroll
  for (int f = 0; f < 18; ++f) p[f] = P[f >> 2][f & 3];
  if constexpr (ISB) {
    float mx = fmax3(p[0], p[1], p[2]), mn = fmin3(p[0], p[1], p[2]);
#pragma unroll
    for (int f = 3; f < 17; f += 2) { mx = fmax3(mx, p[f], p[f + 1]); mn = fmin3(mn, p[f], p[f + 1]); }
    mx = fmax3(mx, p[17], p[17]); mn = fmin3(mn, p[17], p[17]);
    const f32x2 cp = {-mx * LOG2E, -mx * LOG2E}, cn = {mn * LOG2E, mn * LOG2E};
    f32x2 lp = {LOG2E, LOG2E}, ln = {-LOG2E, -LOG2E};
    asm("" : "+v"(lp), "+v"(ln));
    f32x2 ep[9], en[9];
#pragma unroll
    for (int f = 0; f < 9; ++f) {
      const f32x2 x = {p[2 * f], p[2 * f + 1]};
      const f32x2 a = __builtin_elementwise_fma(x, lp, cp), b = __builtin_elementwise_fma(x, ln, cn);
      ep[f] = f32x2{__builtin_amdgcn_exp2f(a[0]), __builtin_amdgcn_exp2f(a[1])};
      en[f] = f32x2{__builtin_amdgcn_exp2f(b[0]), __builtin_amdgcn_exp2f(b[1])};
    }
    f32x2 sp = ep[0], sn = en[0];
#pragma unroll
    for (int f = 1; f < 9; ++f) { sp += ep[f]; sn += en[f]; }
    const float rp = __builtin_amdgcn_rcpf(sp[0] + sp[1]), rn = __builtin_amdgcn_rcpf(sn[0] + sn[1]);
#pragma unroll
    for (int f = 0; f < 9; ++f) {
      const f32x2 a = ep[f] * rp, b = en[f] * rn;
      ph[f] = pk2(a[0], a[1]);
      ph[9 + f] = pk2(b[0], b[1]);
    }
  } else {
    f32x2 l2 = {LOG2E, LOG2E}, eps = {1e-6f, 1e-6f};
    asm("" : "+v"(l2), "+v"(eps));
#pragma unroll
    for (int f = 0; f < 9; ++f) {
      const f32x2 lo = f32x2{fminf(p[2 * f], 0.f), fminf(p[2 * f + 1], 0.f)} * l2;
      const f32x2 hi = f32x2{fmaxf(p[2 * f], 0.f), fmaxf(p[2 * f + 1], 0.f)};
      const f32x2 y = (hi + f32x2{__builtin_amdgcn_exp2f(lo[0]), __builtin_amdgcn_exp2f(lo[1])}) + eps;
      ph[f] = pk2(y[0], y[1]);
    }
    ph[9] = 0u;
  }
}

struct WF { bf16x8 a[5][2]; u32x2 h[5]; };
DI void load_wf_tile(LAS char* lds, int b0, int b1, WF& f, int t) {
  f.a[t][0] = lds128(lds, b0 + (2 * t) * 1024);
  f.a[t][1] = lds128(lds, b0 + (2 * t + 1) * 1024);
  f.h[t] = *(LAS u32x2*)(lds + b1 + t * 512);
}
DI void logits2(const WF& f, const f32x4 (&t0)[2], const f32x4 (&t1)[2], const f32x4 (&t2)[2], const f32x4 (&t3)[2],
                const f32x4 (&t4)[2], f32x4 (&P)[2][5]) {
#pragma unroll
  for (int m = 0; m < 2; ++m) {
    const bf16x8 b0 = pack_pair(t0[m], t1[m]), b1 = pack_pair(t2[m], t3[m]);
    const u32x2 b2 = u32x2{pk2(t4[m][0], t4[m][1]), pk2(t4[m][2], t4[m][3])};
#pragma unroll
    for (int t = 0; t < 5; ++t) P[m][t] = mfma(f.a[t][0], b0, zero4());
#pragma unroll
    for (int t = 0; t < 5; ++t) P[m][t] = mfma(f.a[t][1], b1, P[m][t]);
#pragma unroll
    for (int t = 0; t < 5; ++t) P[m][t] = mfma16(f.h[t], b2, P[m][t]);
  }
}
DI int wf_offset(int h, int d, int f) {
  const int col = 18 * h + d, s = col >> 5, ln = 16 * ((col >> 2) & 3) + 4 * h + (f & 3), tt = f >> 2;
  return s < 2 ? (tt * 2 + s) * 1024 + ln * 16 + (((col >> 4) & 1) * 4 + (col & 3)) * 2 : 10240 + tt * 512 + ln * 8 + (col & 3) * 2;
}
template <int N> DI void cwait_lgkm() { __builtin_amdgcn_s_waitcnt(0xC07F | (N << 8)); }
DI void step5(LAS char* lds, f32x4 (&acc)[5][2], const bf16x8 (&wc)[5], bf16x8 (&wn)[5], bf16x8 x0, bf16x8 x1, int next, bool more) {
#pragma unroll
  for (int j = 0; j < 10; ++j) {
    if (j == 6) { if (more) cwait_lgkm<10>(); else cwait_lgkm<0>(); }
    mfma_acc(acc[j >> 1][j & 1], wc[j >> 1], (j & 1) ? x1 : x0);
    if (more && j < 5) wn[j] = lds_tr(lds, next + j * 32, 16 * 160);
    SBAR();
  }
}

template <bool ISB> DI void c_path(const Args& a, LAS char* lds, const int w, const int lane0, const int t, const int g) {
  constexpr int prob = ISB ? 1 : 0;
  constexpr int RP = ISB ? 288 : 160;
  constexpr int C5 = ISB ? 5 : 3;
  constexpr int KVROW = C5 * 16, KVHEAD = ISB ? 1536 : 1024, NCH = 19 * C5;
  char* ws = a.ws;
  const rsrc_t rws = mk_rsrc(ws, WS_USED);
  const unsigned kvp_grp = WS_KVP + (prob * 8 + g) * 16 * KVP_STRIDE;
  const unsigned obuf = WS_O + (prob * 16 + t) * O_STRIDE;
  constexpr int NU = ISB ? 9 : 5, UMAIN = NU - 1;
  f32x4 acc[9][2];
  WF wf;
  unsigned T;
  int a_v[5][2], a_one, a_prow, a_pm, a_pl, a_b0, a_b1, a_mu, a_al, a_la;
  unsigned o_p0, o_p1, o_mu, o_ml;
  STAMP_DECL
  {
    const int lane = lane0, tid = w * 64 + lane, i = lane & 15, gg = lane >> 4;
    STAMP(0); CSTAMP(43);
    T = __hip_atomic_load((GAS unsigned*)(ws + WS_E + prob * 256), RLX_AGENT) + 1u;
    unsigned short wel[6];
    if constexpr (ISB) {
      const rsrc_t rp = mk_rsrc(a.wphi, 32 * 324 * 2);
#pragma unroll
      for (int k = 0; k < 6; ++k) {
        const int idx = lane + 64 * k;
        wel[k] = __builtin_amdgcn_raw_buffer_load_b16(rp, idx < 324 ? ((4 * g + w) * 324 + idx) * 2 : OOB, 0, 0);
      }
    }
    {
      const u32x4 s = {SENT, SENT, SENT, SENT};
#pragma unroll
      for (int k = 0; k < (KVHEAD / 16 + 63) / 64; ++k) {
        const int n = lane + 64 * k;
        st_sc1(s, rws, n < KVHEAD / 16 ? kvp_grp + t * KVP_STRIDE + w * KVHEAD + n * 16 : OOB);
      }
    }
    if (tid < 64) *(LAS unsigned*)(lds + L_SYNC + tid * 4) = tid == SY_TAG ? T : 0u;
    if (tid < 32) *(LAS unsigned*)(lds + L_ZERO + tid * 4) = 0u;
#pragma unroll
    for (int n = 0; n < 9; ++n) { acc[n][0] = zero4(); acc[n][1] = zero4(); asm volatile("" : "+v"(acc[n][0]), "+v"(acc[n][1])); }
    const int xa0 = (32 * w + i) * 128 + ((gg ^ ((i >> 1) & 7)) * 16), xa1 = xa0 ^ 64;
    const int wa = WPART + (4 * gg + (i >> 2)) * 288 + (i & 3) * 8;
    const int wm = wa + ((i & 2) ? 256 : 128);
    {
      const int xs = ((i >> 2) & 3) << 3;
      const int vrow = L_V + (32 * w + i) * 160;
#pragma unroll
      for (int n = 0; n < 5; ++n) {
#pragma unroll
        for (int pp = 0; pp < 2; ++pp) {
          const int c0 = (n < 4 ? 16 * n : 56) + 2 * pp, h0 = c0 / 18;
          const int thr = (18 * (h0 + 1) - c0 + 3) / 4;
          a_v[n][pp] = opaque(vrow + ((2 * c0 + 4 * h0 + 8 * gg + ((thr < 4 && gg >= thr) ? 4 : 0)) ^ xs));
        }
      }
      a_one = opaque(vrow + ((40 * gg + 36) ^ xs));
      a_prow = opaque(L_PHI + (32 * w + i) * RP);
      a_pm = opaque((UMAIN * gg * 8) ^ xs);
      a_pl = opaque(L_PHI + (32 * w + i) * RP + (((4 * UMAIN + gg) * 8) ^ xs));
      const int q = i >> 2, p = i & 3;
      const int pa = L_PHI + (4 * gg + q) * RP, va = L_V + (4 * gg + q) * 160;
      a_b0 = opaque(pa + ((UMAIN * w + p) ^ gg) * 8); a_b1 = opaque(pa + ((UMAIN * w + 4 + p) ^ gg) * 8);
      a_mu = opaque(pa + ((4 * UMAIN + p) ^ gg) * 8);
      a_al = opaque(va + ((5 * w + p) ^ gg) * 8); a_la = opaque(va + ((5 * p + 4) ^ gg) * 8);
      const unsigned mine = kvp_grp + t * KVP_STRIDE + w * KVHEAD;
      const bool lam = (i >> 2) == w && (i & 3) < 3;
      const int el = 16 + (i & 3), ch = ISB ? ((gg & 1) << 1) | (gg >> 1) : gg >> 1;
      const unsigned o_main = mine + i * KVROW + ch * 16, o_lam = lam ? mine + el * KVROW + ch * 16 : OOB;
      o_p0 = opaque(ISB || !(gg & 1) ? o_main : o_lam); o_p1 = opaque(o_lam);
      o_mu = opaque(gg == w ? mine + i * KVROW + (C5 - 1) * 16 : OOB);
      o_ml = opaque((gg == w && lam) ? mine + el * KVROW + (C5 - 1) * 16 : OOB);
    }
    int a_wf[6];
    {
      const u32x4 z = {0u, 0u, 0u, 0u};
#pragma unroll
      for (int k = 0; k < 3; ++k) {
        const int n = lane + 64 * k, fr = n >> 4, sl = n & 15;
        if (n < 160) *(LAS u32x4*)(lds + L_WF + fr * 1024 + (16 * (sl >> 2) + 4 * w + (sl & 3)) * 16) = z;
      }
#pragma unroll
      for (int k = 0; k < 2; ++k) {
        const int n = lane + 64 * k, fr = n >> 4, sl = n & 15;
        if (n < 80) *(LAS u32x2*)(lds + L_WF + 10240 + fr * 512 + (16 * (sl >> 2) + 4 * w + (sl & 3)) * 8) = u32x2{0u, 0u};
      }
      if constexpr (ISB) {
#pragma unroll
        for (int k = 0; k < 6; ++k) {
          const int idx = lane + 64 * k, d = idx / 18, f = idx - d * 18;
          a_wf[k] = opaque(idx < 324 ? L_WF + wf_offset(w, d, f) : L_ZERO + 124);
        }
      } else if (lane < 18) {
        *(LAS unsigned short*)(lds + L_WF + wf_offset(w, lane, lane)) = (unsigned short)0x3F80;
      }
    }
    bf16x8 wb[2][9], xb[2][2];
    auto kstep = [&](auto SC, int xo, int wo, int wmo) {
      constexpr int S = decltype(SC)::value, N = S ^ 1;
#pragma unroll
      for (int n = 0; n < 9; ++n) {
        mfma_acc(acc[n][0], wb[S][n], xb[S][0]);
        if (n == 0) { xb[N][0] = lds128(lds, xo); xb[N][1] = lds128(lds, xo + 2048); }
        else if (n <= 4) wb[N][2 * n - 1] = lds_tr(lds, 2 * n - 1 < 8 ? wo + (2 * n - 1) * 32 + (2 * n - 1 >= 4 ? 16 : 0) : wmo, 4608);
        mfma_acc(acc[n][1], wb[S][n], xb[S][1]);
        if (n <= 4) wb[N][2 * n] = lds_tr(lds, 2 * n < 8 ? wo + (2 * n) * 32 + (2 * n >= 4 ? 16 : 0) : wmo, 4608);
        SBAR();
      }
    };
    using I0 = std::integral_constant<int, 0>;
    using I1 = std::integral_constant<int, 1>;
    STAMP(1);
    barrier();
    STAMP(2); CSTAMP(44);
    {
      const int xo = opaque(xa0 + 3 * SLOT), wo = opaque(wa + 3 * SLOT), wmo = opaque(wm + 3 * SLOT);
      xb[0][0] = lds128(lds, xo); xb[0][1] = lds128(lds, xo + 2048);
#pragma unroll
      for (int n = 0; n < 9; ++n) wb[0][n] = lds_tr(lds, n < 8 ? wo + n * 32 + (n >= 4 ? 16 : 0) : wmo, 4608);
    }
    int base = 3 * SLOT;
#pragma unroll 1
    for (int c = 0; c < 8; ++c) {
      {
        const int xo = opaque(xa1 + base), wo = opaque(wa + base + 9216), wmo = opaque(wm + base + 9216);
        cwait_lgkm0();
        SBAR();
        kstep(I0{}, xo, wo, wmo);
      }
      if constexpr (ISB) {
        if (c == 3) {
#pragma unroll
          for (int k = 0; k < 6; ++k) *(LAS unsigned short*)(lds + a_wf[k]) = wel[k];
        }
      }
      if (c == 5) wait_vm<0>();
      cwait_lgkm0();
      barrier();
      base = base == 3 * SLOT ? 0 : base + SLOT;
      {
        const int xo = opaque(xa0 + base), wo = opaque(wa + base), wmo = opaque(wm + base);
        SBAR();
        kstep(I1{}, xo, wo, wmo);
      }
    }
    {
      const int xo = opaque(xa1 + base), wo = opaque(wa + base + 9216), wmo = opaque(wm + base + 9216);
      cwait_lgkm0();
      SBAR();
      kstep(I0{}, xo, wo, wmo);
      cwait_lgkm0();
      lds_arrive(lds, SY_KV8);
      const int b0 = opaque(L_WF + lane * 16), b1 = opaque(L_WF + 10240 + lane * 8);
      SBAR();
#pragma unroll
      for (int n = 0; n < 9; ++n) {
        mfma_acc(acc[n][0], wb[1][n], xb[1][0]);
        if (n < 5) load_wf_tile(lds, b0, b1, wf, n);
        mfma_acc(acc[n][1], wb[1][n], xb[1][1]);
        SBAR();
      }
      lds_wait(lds, SY_KV8, 8u);
      mfma_fence(acc);
    }
  }
  const int lane = opaque(lane0), tid = w * 64 + lane, i = lane & 15, gg = lane >> 4;
  const unsigned Ts = __builtin_amdgcn_readfirstlane(T);
  __builtin_amdgcn_s_setprio(2);
  STAMP(3); CSTAMP(45);

  {
    f32x4 P[2][5];
    bf16x8 kb[2][2];
    u32x2 kb2[2];
#pragma unroll
    for (int m = 0; m < 2; ++m) {
      kb[m][0] = pack_pair(acc[0][m], acc[1][m]); kb[m][1] = pack_pair(acc[2][m], acc[3][m]);
      kb2[m] = u32x2{pk2(acc[8][m][0], acc[8][m][1]), pk2(acc[8][m][2], acc[8][m][3])};
    }
    SBAR();
#pragma unroll
    for (int st = 0; st < 30; ++st) {
      const int m = st / 15, s = (st % 15) / 5, tt = st % 5;
      if (s == 0) P[m][tt] = mfma(wf.a[tt][0], kb[m][0], zero4());
      if (s == 1) P[m][tt] = mfma(wf.a[tt][1], kb[m][1], P[m][tt]);
      if (s == 2) P[m][tt] = mfma16(wf.h[tt], kb2[m], P[m][tt]);
      if (st < 20) {
        const int n = st >> 2, pp = (st >> 1) & 1, mv = st & 1;
        const f32x4 v = n < 4 ? acc[4 + n][mv] : acc[8][mv];
        const int am = n < 4 ? a_v[n][pp] + mv * 2560 : (gg >= 2 ? a_v[n][pp] + mv * 2560 : L_ZERO + 124);
        *(LAS unsigned*)(lds + am) = pk2(v[2 * pp], v[2 * pp + 1]);
      }
      SBAR();
    }
    *(LAS unsigned*)(lds + a_one) = 0x00003F80u; *(LAS unsigned*)(lds + a_one + 2560) = 0x00003F80u;
    FSTAMP(18);
#pragma unroll
    for (int m = 0; m < 2; ++m) {
      unsigned ph[ISB ? 18 : 10];
      feature_map<ISB>(P[m], ph);
#pragma unroll
      for (int j = 0; j < NU; ++j)
        *(LAS u32x2*)(lds + (j < UMAIN ? a_prow + (a_pm ^ (j * 8)) : a_pl) + m * 16 * RP) = u32x2{ph[2 * j], ph[2 * j + 1]};
      FSTAMP(19 + m);
    }
  }
  STAMP(4);
  lds_arrive(lds, SY_PHI);
  lds_wait(lds, SY_PHI);
  STAMP(5);

  {
    bf16x8 al[4], la[4], b0[4], mu[4], b1[4];
    f32x4 d0 = zero4(), d1 = zero4(), d2 = zero4(), d3 = zero4(), d4 = zero4(), d5 = zero4();
    asm volatile("" : "+v"(d0), "+v"(d1), "+v"(d2), "+v"(d3), "+v"(d4), "+v"(d5));
    auto rd = [&](int ks) {
      al[ks] = lds_tr(lds, a_al + ks * 32 * 160, 16 * 160);
      b0[ks] = lds_tr(lds, a_b0 + ks * 32 * RP, 16 * RP);
      if constexpr (ISB) b1[ks] = lds_tr(lds, a_b1 + ks * 32 * RP, 16 * RP);
      la[ks] = lds_tr(lds, a_la + ks * 32 * 160, 16 * 160);
      mu[ks] = lds_tr(lds, a_mu + ks * 32 * RP, 16 * RP);
      SBAR();
    };
    auto mm = [&](int ks) {
      mfma_acc(d0, b0[ks], al[ks]);
      if constexpr (ISB) mfma_acc(d1, b1[ks], al[ks]);
      mfma_acc(d2, b0[ks], la[ks]);
      if constexpr (ISB) mfma_acc(d3, b1[ks], la[ks]);
      mfma_acc(d4, mu[ks], al[ks]); mfma_acc(d5, mu[ks], la[ks]);
      SBAR();
    };
    constexpr int RD = ISB ? 10 : 8;
    rd(0); rd(1);
    __builtin_amdgcn_s_waitcnt(0xC07F & ~0xF00 | (RD << 8)); SBAR(); mm(0); rd(2);
    __builtin_amdgcn_s_waitcnt(0xC07F & ~0xF00 | (RD << 8)); SBAR(); mm(1); rd(3);
    __builtin_amdgcn_s_waitcnt(0xC07F & ~0xF00 | (RD << 8)); SBAR(); mm(2);
    cwait_lgkm0(); SBAR(); mm(3);
    asm volatile("s_nop 7\n\ts_nop 3" : "+v"(d0), "+v"(d1), "+v"(d2), "+v"(d3), "+v"(d4), "+v"(d5));
    if constexpr (ISB) {
      unsigned x0 = pk2(d0[0], d0[1]), x1 = pk2(d0[2], d0[3]), y0 = pk2(d1[0], d1[1]), y1 = pk2(d1[2], d1[3]);
      swap16(x0, y0); swap16(x1, y1);
      st_sc1(u32x4{nosent(x0), nosent(x1), nosent(y0), nosent(y1)}, rws, o_p0);
      x0 = pk2(d2[0], d2[1]); x1 = pk2(d2[2], d2[3]); y0 = pk2(d3[0], d3[1]); y1 = pk2(d3[2], d3[3]);
      swap16(x0, y0); swap16(x1, y1);
      st_sc1(u32x4{nosent(x0), nosent(x1), nosent(y0), nosent(y1)}, rws, o_p1);
    } else {
      unsigned x0 = pk2(d0[0], d0[1]), x1 = pk2(d0[2], d0[3]), y0 = pk2(d2[0], d2[1]), y1 = pk2(d2[2], d2[3]);
      swap16(x0, y0); swap16(x1, y1);
      st_sc1(u32x4{nosent(x0), nosent(x1), nosent(y0), nosent(y1)}, rws, o_p0);
    }
    st_sc1(u32x4{nosent(pk2(d4[0], d4[1])), nosent(pk2(d4[2], d4[3])), 0u, 0u}, rws, o_mu);
    st_sc1(u32x4{nosent(pk2(d5[0], d5[1])), nosent(pk2(d5[2], d5[3])), 0u, 0u}, rws, o_ml);
    st_tag(rws, lane == 0 ? WS_H1 + (((prob * 8 + g) * 16 + t) * 4 + w) * 128 : OOB, T);
    lds_arrive(lds, SY_KVDONE);
  }
  __builtin_amdgcn_s_setprio(0);
  STAMP(6); CSTAMP(46);
  const rsrc_t rwo = mk_rsrc(ISB ? a.woB : a.woA, 576 * XROW);
  const unsigned lb = (unsigned)(size_t)lds;
  unsigned vo[5];
#pragma unroll
  for (int n = 0; n < 5; ++n) {
    const int nn = ((w + 4 * n) % 5) * 64 + lane, rho = nn / 10, cp = nn - 10 * rho;
    vo[n] = (unsigned)opaque(krow(rho) * XROW + 144 * g + 16 * min(cp, 8));
  }
  {
    {
      const u32x4 s = {SENT, SENT, SENT, SENT};
#pragma unroll
      for (int k = 0; k < 5; ++k) {
        const int n = lane + 64 * k;
        st_sc1(s, rws, n < 288 ? obuf + (2 * w * 72 + 9 * g) * 256 + n * 16 + (n >= 144 ? (72 - 9) * 256 : 0) : OOB);
      }
    }
    STAMP(7);
    const rsrc_t rkv = mk_rsrc(ws + kvp_grp, 16 * KVP_STRIDE);
    constexpr int NG = (NCH + 15) / 16;
    const unsigned one = (i & 1) ? 0x3F800000u : 0x3F80u;
    const bf16x8 sel = frag_of((i >> 1) == 0 ? one : 0u, (i >> 1) == 1 ? one : 0u, (i >> 1) == 2 ? one : 0u, (i >> 1) == 3 ? one : 0u);
    const unsigned off0 = gg * KVP_STRIDE + w * KVHEAD + i * 16;
    const unsigned offl = 16 * (NG - 1) + i < NCH ? off0 + (NG - 1) * 256 : OOB;
    const int kvw = L_PHI + w * KVHEAD + i * 16 + gg * 8;
    unsigned p0, p1;
    poll_tag(rws, lane < 16 ? WS_H1 + (((prob * 8 + g) * 16 + lane) * 4 + w) * 128 : OOB, Ts, 0xFFFFull, p0, p1);
    __builtin_amdgcn_s_setprio(2);
    STAMP(8);
    f32x4 sum[NG];
    {
      u32x4 v[NG][4];
#pragma unroll
      for (int cg = 0; cg < NG; ++cg) {
        const unsigned off = cg < NG - 1 ? off0 + cg * 256 : offl;
#pragma unroll
        for (int u = 0; u < 4; ++u)
          asm volatile("buffer_load_dwordx4 %0, %1, %2, %3 offen sc1" : "=v"(v[cg][u]) : "v"(off), "s"(rkv), "s"(4 * u * KVP_STRIDE) : "memory");
      }
      float chk = 0.f;
#pragma unroll
      for (int cg = 0; cg < NG; ++cg) {
        asm volatile("s_waitcnt vmcnt(%6)" : "+v"(v[cg][0]), "+v"(v[cg][1]), "+v"(v[cg][2]), "+v"(v[cg][3]), "+v"(p0), "+v"(p1)
                     : "n"((NG - 1 - cg) * 4));
        SBAR();
        f32x4 d = zero4();
#pragma unroll
        for (int u = 0; u < 4; ++u) d = mfma(sel, __builtin_bit_cast(bf16x8, v[cg][u]), d);
        sum[cg] = d;
        chk += d[0];
      }
      if (!__all(chk == chk)) {
#pragma unroll 1
        for (int sp = 0; sp < SPIN_G; ++sp) {
          unsigned mx = 0u;
#pragma unroll 1
          for (int n = 0; n < NG * 4; ++n) {
            asm volatile("" ::: "memory");
            const u32x4 x = ld_sc1(rkv, (n >> 2) < NG - 1 ? off0 + (n >> 2) * 256 : offl, 4 * (n & 3) * KVP_STRIDE);
            mx = umax3(mx, x[0], x[1]); mx = umax3(mx, x[2], x[3]);
          }
          if (__all(mx != SENT)) break;
        }
#pragma unroll
        for (int cg = 0; cg < NG; ++cg) {
          f32x4 d = zero4();
#pragma unroll
          for (int u = 0; u < 4; ++u)
            d = mfma(sel, __builtin_bit_cast(bf16x8, ld_sc1(rkv, cg < NG - 1 ? off0 + cg * 256 : offl, 4 * u * KVP_STRIDE)), d);
          sum[cg] = d;
        }
      }
    }
    lds_wait(lds, SY_KVDONE);
    if (gg < 2) {
#pragma unroll
      for (int cg = 0; cg < NG; ++cg)
        if (cg < NG - 1 || 16 * cg + i < NCH)
          *(LAS u32x2*)(lds + kvw + cg * 256) = u32x2{pk2(sum[cg][0], sum[cg][1]), pk2(sum[cg][2], sum[cg][3])};
    }
    lds_arrive(lds, SY_KVIMG);
  }
  STAMP(9);
  __builtin_amdgcn_s_setprio(0);
  lds_wait(lds, SY_QDONE);
#pragma unroll
  for (int n = 0; n < 23; ++n) {
    const int v = w + 4 * n;
    if (n < 22 || w < 2) dma16(rwo, vo[n % 5], (v / 5) * 32 * XROW, lb + L_WQ + v * 1024);
  }
  wait_vm<0>();
  lds_arrive(lds, SY_WO);
  STAMP(10);

  {
    const rsrc_t ro = mk_rsrc(ws + obuf, O_STRIDE);
    const rsrc_t rout = mk_rsrc(a.out, 2048 * OUTROW);
    const unsigned orow = (t * 128 + 32 * w + i) * OUTROW + prob * 1152 + 144 * g;
    const unsigned o_pair = orow + (2 * (gg & 1) + (gg >> 1)) * 16, o_last = gg < 2 ? orow + gg * 16 * OUTROW + 128 : OOB;
    const int wl = (4 * gg + (i >> 2)) * 160 + (i & 3) * 8;
    const int wa2 = opaque(L_WQ + wl), wa3 = opaque(L_WQ + wl + 12 * 5120);
    lds_wait(lds, SY_WO);
    unsigned p0, p1;
    poll_tag(rws, lane < 8 ? WS_H2 + (((prob * 16 + t) * 8 + lane) * 4 + w) * 128 : OOB, Ts, 0xFFull, p0, p1);
    __builtin_amdgcn_s_setprio(2);
    STAMP(11);
    f32x4 oc[5][2];
#pragma unroll 1
    for (int pass = 0; pass < 2; ++pass) {
      asm volatile("" ::: "memory");
      u32x4 of[18][2];
#pragma unroll
      for (int ks = 0; ks < 18; ++ks) {
#pragma unroll
        for (int m = 0; m < 2; ++m) { of[ks][m] = ld_sc1(ro, lane * 16, ((2 * w + m) * 72 + 4 * ks) * 256); SBAR(); }
      }
      bf16x8 wq[2][5];
#pragma unroll
      for (int n = 0; n < 5; ++n) {
        wq[0][n] = lds_tr(lds, wa2 + n * 32, 16 * 160);
        oc[n][0] = zero4(); oc[n][1] = zero4();
        asm volatile("" : "+v"(oc[n][0]), "+v"(oc[n][1]));
      }
      SBAR();
#define OUT_STEP(ks) do { cwait_vm_lgkm4<34 - 2 * (ks)>(); SBAR(); \
      step5(lds, oc, wq[(ks) & 1], wq[((ks) + 1) & 1], __builtin_bit_cast(bf16x8, of[ks][0]), __builtin_bit_cast(bf16x8, of[ks][1]), \
            (ks) + 1 < 12 ? wa2 + ((ks) + 1) * 5120 : wa3 + ((ks) + 1 - 12) * 5120, (ks) < 17); } while (0)
      OUT_STEP(0);
      asm volatile("" :: "v"(p0), "v"(p1));
      OUT_STEP(1); OUT_STEP(2); OUT_STEP(3); OUT_STEP(4); OUT_STEP(5); OUT_STEP(6); OUT_STEP(7); OUT_STEP(8);
      OUT_STEP(9); OUT_STEP(10); OUT_STEP(11); OUT_STEP(12); OUT_STEP(13); OUT_STEP(14); OUT_STEP(15); OUT_STEP(16); OUT_STEP(17);
#undef OUT_STEP
      mfma_fence(oc);
      const float chk = oc[0][0][0] + oc[0][1][0];
      if (pass == 1 || __all(chk == chk)) break;
#pragma unroll 1
      for (int sp = 0; sp < SPIN_G; ++sp) {
        unsigned mx = 0u;
#pragma unroll 1
        for (int n = 0; n < 36; ++n) {
          asm volatile("" ::: "memory");
          const u32x4 x = ld_sc1(ro, lane * 16, ((2 * w + (n & 1)) * 72 + 4 * (n >> 1)) * 256);
          mx = umax3(mx, x[0], x[1]); mx = umax3(mx, x[2], x[3]);
        }
        if (__all(mx != SENT)) break;
      }
    }
    STAMP(12);
#pragma unroll
    for (int m = 0; m < 2; ++m) {
#pragma unroll
      for (int n = 0; n < 4; n += 2) {
        unsigned x0 = pk2(oc[n][m][0], oc[n][m][1]), x1 = pk2(oc[n][m][2], oc[n][m][3]);
        unsigned y0 = pk2(oc[n + 1][m][0], oc[n + 1][m][1]), y1 = pk2(oc[n + 1][m][2], oc[n + 1][m][3]);
        swap16(x0, y0); swap16(x1, y1);
        __builtin_amdgcn_raw_buffer_store_b128(u32x4{x0, x1, y0, y1}, rout, o_pair + m * 16 * OUTROW + n * 32, 0, 0);
      }
    }
    unsigned x0 = pk2(oc[4][0][0], oc[4][0][1]), x1 = pk2(oc[4][0][2], oc[4][0][3]);
    unsigned y0 = pk2(oc[4][1][0], oc[4][1][1]), y1 = pk2(oc[4][1][2], oc[4][1][3]);
    swap16(x0, y0); swap16(x1, y1);
    __builtin_amdgcn_raw_buffer_store_b128(u32x4{x0, x1, y0, y1}, rout, o_last, 0, 0);
    if (t == 0 && g == 0 && tid == 0) __hip_atomic_store((GAS unsigned*)(ws + WS_E + prob * 256), T, RLX_AGENT);
  }
  STAMP(13); CSTAMP(47);
  STAMP_OUT(w);
}

template <bool ISB> DI void h_path(const Args& a, LAS char* lds, const int w, const int lane, const int t, const int g) {
  constexpr int prob = ISB ? 1 : 0;
  constexpr int C5 = ISB ? 5 : 3;
  constexpr int KVROW = C5 * 16, KVHEAD = ISB ? 1536 : 1024;
  const int i = lane & 15, gg = lane >> 4;
  const rsrc_t rx = mk_rsrc((const char*)(ISB ? a.xB : a.xA) + (size_t)t * 128 * XROW, 128 * XROW);
  const rsrc_t rw = mk_rsrc(ISB ? a.wB : a.wA, 576 * WROW);
  const unsigned lb = (unsigned)(size_t)lds;
  STAMP_DECL
  STAMP(0);
  const int rot = ROT == 1 ? (g + 2 * (t & 3) + ((t >> 1) & 1)) % 9 : ROT == 2 ? (g + 8 * (t & 3)) % 9 : 0;
  auto kchunk = [&](int c) { const int k = c + rot; return k >= 9 ? k - 9 : k; };
  unsigned vx[2], vw[5], vq[8];
#pragma unroll
  for (int par = 0; par < 2; ++par) vx[par] = (lane >> 3) * XROW + (((lane & 7) ^ ((4 * par + (lane >> 4)) & 7)) * 16);
  auto issue_x = [&](int c) {
    const unsigned sb = lb + ((c + 3) & 3) * SLOT;
#pragma unroll
    for (int jj = 0; jj < 4; ++jj) dma16(rx, vx[jj & 1], (4 * w + jj) * 8 * XROW + kchunk(c) * 128, sb + (4 * w + jj) * 1024);
  };
  issue_x(0);
#pragma unroll
  for (int n = 0; n < 5; ++n) {
    const int nn = (w + 4 * n) * 64 + lane, r64 = nn / 18, cp = nn - 18 * r64;
    const int co = cp < 9 ? 1152 + 16 * cp : 2304 + 16 * (cp - 9);
    vw[n] = ((r64 & 32) + krow(r64 & 31)) * WROW + 144 * g + co;
  }
  auto issue_w = [&](int c) {
    const unsigned sb = lb + ((c + 3) & 3) * SLOT;
#pragma unroll
    for (int n = 0; n < 5; ++n)
      if (n < 4 || w < 2) dma16(rw, vw[n], kchunk(c) * 64 * WROW, sb + WPART + (w + 4 * n) * 1024);
  };
  issue_w(0); issue_x(1); issue_w(1);
  STAMP(1);
  if (w < 2) wait_vm<9>(); else wait_vm<8>();
  STAMP(2);
  barrier();
  issue_x(2); issue_w(2);
#pragma unroll
  for (int n = 0; n < 8; ++n) {
    const int v = w + 4 * n, ks = v / 5, nn = (v - 5 * ks) * 64 + lane, rho = nn / 10, cp = nn - 10 * rho;
    vq[n] = ((ks & 1) * 32 + krow(rho)) * WROW + 144 * g + 16 * min(cp, 8);
  }
  auto issue_wq = [&](int grp) {
#pragma unroll
    for (int n = 0; n < 8; ++n)
      if (n < 7 || w < 2)
        dma16(rw, vq[n], kchunk(3 * grp + ((w + 4 * n) / 5 >> 1)) * 64 * WROW, lb + L_WQ + grp * 30720 + (w + 4 * n) * 1024);
  };
  bf16x8 X[18][2];
  const int xa0 = (32 * w + i) * 128 + ((gg ^ ((i >> 1) & 7)) * 16), xa1 = xa0 ^ 64;
#pragma unroll
  for (int c = 0; c < 9; ++c) {
    const int base = ((c + 3) & 3) * SLOT;
    const int xo0 = opaque(xa0 + base), xo1 = opaque(xa1 + base);
    X[2 * c][0] = lds128(lds, xo0); X[2 * c][1] = lds128(lds, xo0 + 2048);
    X[2 * c + 1][0] = lds128(lds, xo1); X[2 * c + 1][1] = lds128(lds, xo1 + 2048);
    if (c <= 5) { issue_x(c + 3); issue_w(c + 3); }
    LSTAMP(16 + c);
    if (c <= 5) { if (w < 2) wait_vm<18>(); else wait_vm<16>(); }
    if (c == 6) { if (w < 2) wait_vm<9>(); else wait_vm<8>(); }
    if (c == 7) wait_vm<0>();
    LSTAMP(25 + c);
    if (c < 8) barrier(); else lds_arrive(lds, SY_KV8);
    LSTAMP(34 + c);
  }
  issue_wq(0); issue_wq(1); issue_wq(2);
  __builtin_amdgcn_s_setprio(1);
  STAMP(3);
  f32x4 qa[5][2];
#pragma unroll
  for (int n = 0; n < 5; ++n) { qa[n][0] = zero4(); qa[n][1] = zero4(); asm volatile("" : "+v"(qa[n][0]), "+v"(qa[n][1])); }
  const int wa2 = opaque(L_WQ + (4 * gg + (i >> 2)) * 160 + (i & 3) * 8), wa3 = opaque(wa2 + 12 * 5120);
  bf16x8 wq[2][5];
  if (w < 2) wait_vm<16>(); else wait_vm<14>();
  lds_arrive(lds, SY_WQ0);
  lds_wait(lds, SY_WQ0);
  STAMP(4);
#pragma unroll
  for (int n = 0; n < 5; ++n) wq[0][n] = lds_tr(lds, wa2 + n * 32, 16 * 160);
#pragma unroll
  for (int ks = 0; ks < 18; ++ks) {
    cwait_lgkm<4>();
    if (ks == 5) { if (w < 2) wait_vm<8>(); else wait_vm<7>(); lds_arrive(lds, SY_WQ1); lds_wait(lds, SY_WQ1); STAMP(5); }
    if (ks == 11) { wait_vm<0>(); lds_arrive(lds, SY_WQ2); lds_wait(lds, SY_WQ2); STAMP(6); }
    SBAR();
    step5(lds, qa, wq[ks & 1], wq[(ks + 1) & 1], X[ks][0], X[ks][1], ks + 1 < 12 ? wa2 + (ks + 1) * 5120 : wa3 + (ks + 1 - 12) * 5120, ks < 17);
  }
  WF wf;
  {
    const int b0 = opaque(L_WF + lane * 16), b1 = opaque(L_WF + 10240 + lane * 8);
#pragma unroll
    for (int n = 0; n < 5; ++n) load_wf_tile(lds, b0, b1, wf, n);
  }
  mfma_fence(qa);
  lds_arrive(lds, SY_QDONE);
  STAMP(7);
  const unsigned T = *(LAS unsigned*)(lds + L_SYNC + SY_TAG * 4);
  unsigned ph[2][ISB ? 18 : 10];
  {
    f32x4 P[2][5];
    logits2(wf, qa[0], qa[1], qa[2], qa[3], qa[4], P);
    feature_map<ISB>(P[0], ph[0]);
    feature_map<ISB>(P[1], ph[1]);
#pragma unroll
    for (int j = 0; j < (ISB ? 18 : 10); ++j) asm volatile("" : "+v"(ph[0][j]), "+v"(ph[1][j]));
  }
  int a_kv[5], a_st[9];
  {
    const bool own = (i >> 2) == gg;
#pragma unroll
    for (int n = 0; n < 5; ++n) {
      const int e = 4 * n + (i & 3);
      a_kv[n] = opaque((own && e < 19) ? L_PHI + gg * KVHEAD + e * KVROW : L_ZERO);
    }
#pragma unroll
    for (int pp = 0; pp < 9; ++pp) {
      const int cp = 9 * gg + pp;
      a_st[pp] = opaque(L_V + w * 4608 + (cp >> 2) * 256 + i * 16 + (cp & 3) * 4);
    }
  }
  const rsrc_t ro = mk_rsrc(a.ws + WS_O + (prob * 16 + t) * O_STRIDE, O_STRIDE);
  const unsigned o_dst = (2 * w * 72 + 9 * g) * 256 + lane * 16;
  STAMP(8);
  lds_wait(lds, SY_KVIMG);
  __builtin_amdgcn_s_setprio(3);
  STAMP(9);
  f32x4 O[5][2];
  {
    bf16x8 af[5][C5];
#pragma unroll
    for (int s = 0; s < C5; ++s) {
#pragma unroll
      for (int n = 0; n < 5; ++n) af[n][s] = lds128(lds, a_kv[n] + s * 16);
    }
    SBAR();
    constexpr int NP = ISB ? 18 : 10;
#pragma unroll
    for (int s = 0; s < C5; ++s) {
      bf16x8 bq[2];
#pragma unroll
      for (int m = 0; m < 2; ++m) {
        unsigned u[4];
#pragma unroll
        for (int j = 0; j < 4; ++j) u[j] = 4 * s + j < NP ? ph[m][4 * s + j < NP ? 4 * s + j : 0] : 0u;
        bq[m] = frag_of(u[0], u[1], u[2], u[3]);
      }
#pragma unroll
      for (int n = 4; n >= 0; --n) {
#pragma unroll
        for (int m = 0; m < 2; ++m) O[n][m] = mfma(af[n][s], bq[m], s == 0 ? zero4() : O[n][m]);
      }
    }
  }
  STAMP(10);
#pragma unroll
  for (int m = 0; m < 2; ++m) {
    const float rc = __builtin_amdgcn_rcpf(O[4][m][2] + 1e-6f);
    const f32x2 rc2 = {rc, rc};
#pragma unroll
    for (int pp = 0; pp < 9; ++pp) {
      const int e = 2 * pp;
      const f32x2 y = f32x2{O[e >> 2][m][e & 3], O[(e + 1) >> 2][m][(e + 1) & 3]} * rc2;
      *(LAS unsigned*)(lds + a_st[pp] + m * 2304) = nosent(pk2(y[0], y[1]));
    }
  }
  {
    const int sb = L_V + w * 4608 + lane * 16;
    u32x4 v[5];
#pragma unroll
    for (int k = 0; k < 5; ++k) v[k] = *(LAS u32x4*)(lds + sb + (k < 4 || lane < 32 ? k * 1024 : 0));
#pragma unroll
    for (int k = 0; k < 5; ++k) {
      const int n = lane + 64 * k;
      st_sc1(v[k], ro, n < 288 ? o_dst + k * 1024 + (n >= 144 ? (72 - 9) * 256 : 0) : OOB);
    }
    st_tag(mk_rsrc(a.ws, WS_USED), lane == 0 ? WS_H2 + (((prob * 16 + t) * 8 + g) * 4 + w) * 128 : OOB, T);
  }
  STAMP(11);
  STAMP_OUT(4 + w);
}

__global__ __launch_bounds__(512, 2) void hedgehog_fused(Args a) {
  extern __shared__ __attribute__((aligned(16))) char smem[];
  LAS char* lds = (LAS char*)smem;
  const int b = blockIdx.x, xcd = b & 7, r = b >> 3;
  const int prob = xcd >> 2, t = (xcd & 3) * 4 + (r >> 3), g = r & 7;
  const int wave = __builtin_amdgcn_readfirstlane(threadIdx.x >> 6), lane = threadIdx.x & 63;
  if (wave < 4) {
    if (prob) c_path<true>(a, lds, wave, lane, t, g); else c_path<false>(a, lds, wave, lane, t, g);
  } else {
    if (prob) h_path<true>(a, lds, wave - 4, lane, t, g); else h_path<false>(a, lds, wave - 4, lane, t, g);
  }
}

}

extern "C" void kernel_launch(void* const* d_in, const int* in_sizes, int n_in, void* d_out, int out_size, void* d_ws,
                              size_t ws_size, hipStream_t stream) {
  (void)in_sizes; (void)n_in; (void)out_size; (void)ws_size;
  Args a;
  a.xA = d_in[0]; a.wA = d_in[1]; a.woA = d_in[2]; a.xB = d_in[3]; a.wB = d_in[4]; a.wphi = d_in[5]; a.woB = d_in[6];
  a.out = d_out; a.ws = (char*)d_ws;
  (void)hipFuncSetAttribute((const void*)hedgehog_fused, hipFuncAttributeMaxDynamicSharedMemorySize, LDS_BYTES);
  hedgehog_fused<<<256, 512, LDS_BYTES, stream>>>(a);
}
```

```cpp
#include <hip/hip_runtime.h>
#include <hip/hip_bf16.h>
#include <stdint.h>
#include <type_traits>

typedef __bf16 bf16x8 __attribute__((ext_vector_type(8)));
typedef __bf16 bf16x4 __attribute__((ext_vector_type(4)));
typedef __bf16 bf16x2 __attribute__((ext_vector_type(2)));
typedef short s16x4 __attribute__((ext_vector_type(4)));
typedef float f32x4 __attribute__((ext_vector_type(4)));
typedef float f32x2 __attribute__((ext_vector_type(2)));
typedef unsigned u32x4 __attribute__((ext_vector_type(4)));
typedef unsigned u32x2 __attribute__((ext_vector_type(2)));
#define LAS __attribute__((address_space(3)))
#define GAS __attribute__((address_space(1)))
#define DI __device__ __forceinline__
#define RLX_AGENT __ATOMIC_RELAXED, __HIP_MEMORY_SCOPE_AGENT
#define SBAR() __builtin_amdgcn_sched_barrier(0)
typedef __amdgpu_buffer_rsrc_t rsrc_t;

#define DBG 0
#define PROF 0
#define STAMP(n)
#define CSTAMP(n)
#define STAMP_DECL
#define STAMP_OUT(wv)
#define PCOUNT(n)
#define LSTAMP(n)
#define FSTAMP(n)
#define PFQ 0
#define PFO 1
#define WO_FIRST 11
#define KV_UNROLL 1
#define ROT 1

namespace {

constexpr int XROW = 1152;
constexpr int WROW = 3456;
constexpr int OUTROW = 2304;
constexpr int SLOT = 34816, WPART = 16384;
constexpr int L_WQ = 0;
constexpr int L_PHI = 92160;
constexpr int L_V = 129024;
constexpr int L_WF = 149504;
constexpr int L_SYNC = 162304;
constexpr int L_ZERO = 162560;
constexpr int LDS_BYTES = 163840;
enum { SY_WQ0 = 0, SY_WQ1, SY_WQ2, SY_QDONE, SY_PHI, SY_KVDONE, SY_KVIMG, SY_WO, SY_KV8, SY_PRE, SY_TAG = 63 };
constexpr int WS_E = 0, WS_KVP = 65536, KVP_STRIDE = 6144, WS_O = 2097152, O_STRIDE = 147456;
constexpr int WS_H1 = WS_O + 32 * O_STRIDE, WS_H2 = WS_H1 + 131072;
constexpr int WS_USED = WS_H2 + 131072;
constexpr unsigned SENT = 0xFFFFFFFFu;
constexpr unsigned OOB = 0x7FFF0000u;
constexpr int SPIN_G = 1 << 18, SPIN_L = 1 << 22;
constexpr float LOG2E = 1.4426950408889634f;

struct Args {
  const void* xA; const void* wA; const void* woA; const void* xB; const void* wB; const void* wphi; const void* woB;
  void* out; char* ws;
};

DI f32x4 mfma(bf16x8 a, bf16x8 b, f32x4 c) { return __builtin_amdgcn_mfma_f32_16x16x32_bf16(a, b, c, 0, 0, 0); }
DI f32x4 mfma16(u32x2 a, u32x2 b, f32x4 c) {
  return __builtin_amdgcn_mfma_f32_16x16x16bf16_1k(__builtin_bit_cast(s16x4, a), __builtin_bit_cast(s16x4, b), c, 0, 0, 0);
}
DI void mfma_acc(f32x4& c, bf16x8 a, bf16x8 b) { asm volatile("v_mfma_f32_16x16x32_bf16 %0, %1, %2, %0" : "+v"(c) : "v"(a), "v"(b)); }
template <int N> DI void mfma_fence(f32x4 (&acc)[N][2]) {
  asm volatile("s_nop 7\n\ts_nop 3" : "+v"(acc[N - 1][0]), "+v"(acc[N - 1][1]) :: "memory");
#pragma unroll
  for (int n = 0; n < N - 1; ++n) asm volatile("" : "+v"(acc[n][0]), "+v"(acc[n][1]));
}
DI unsigned pk2(float a, float b) { f32x2 p = {a, b}; return __builtin_bit_cast(unsigned, __builtin_convertvector(p, bf16x2)); }
DI bf16x8 frag_of(unsigned a, unsigned b, unsigned c, unsigned d) { u32x4 v = {a, b, c, d}; return __builtin_bit_cast(bf16x8, v); }
DI bf16x8 pack_pair(f32x4 lo, f32x4 hi) { return frag_of(pk2(lo[0], lo[1]), pk2(lo[2], lo[3]), pk2(hi[0], hi[1]), pk2(hi[2], hi[3])); }
DI bf16x8 lds128(LAS char* l, int off) { return *(LAS bf16x8*)(l + off); }
DI bf16x8 lds_tr(LAS char* l, int off, int second) {
  bf16x4 a = __builtin_amdgcn_ds_read_tr16_b64_v4bf16((LAS bf16x4*)(l + off));
  bf16x4 b = __builtin_amdgcn_ds_read_tr16_b64_v4bf16((LAS bf16x4*)(l + off + second));
  return __builtin_shufflevector(a, b, 0, 1, 2, 3, 4, 5, 6, 7);
}
DI void dma16(rsrc_t r, unsigned voff, unsigned soff, unsigned ldsaddr) {
  asm volatile("s_mov_b32 m0, %0\n\ts_nop 0\n\tbuffer_load_dwordx4 %1, %2, %3 offen lds" ::"s"(ldsaddr), "v"(voff), "s"(r), "s"(soff) : "memory");
}
template <int N> DI void wait_vm() { asm volatile("s_waitcnt vmcnt(%0)" ::"n"(N) : "memory"); }
DI void cwait_lgkm0() { __builtin_amdgcn_s_waitcnt(0xC07F); }
template <int N> DI void cwait_vm_lgkm4() { __builtin_amdgcn_s_waitcnt((N & 15) | 0x70 | (4 << 8) | ((N >> 4) << 14)); }
DI void barrier() { asm volatile("s_waitcnt lgkmcnt(0)\n\ts_barrier" ::: "memory"); }
DI void lds_arrive(LAS char* l, int id) {
  const unsigned addr = (unsigned)(size_t)l + L_SYNC + id * 4, one = 1u;
  unsigned long long save;
  asm volatile("s_waitcnt lgkmcnt(0)\n\ts_mov_b64 %0, exec\n\ts_mov_b64 exec, 1\n\tds_add_u32 %1, %2\n\ts_mov_b64 exec, %0"
               : "=&s"(save) : "v"(addr), "v"(one) : "memory");
}
DI void lds_wait(LAS char* l, int id, unsigned n = 4u) {
  const unsigned addr = (unsigned)(size_t)l + L_SYNC + id * 4;
#pragma unroll 1
  for (int sp = 0; sp < SPIN_L; ++sp) {
    unsigned v;
    asm volatile("ds_read_b32 %0, %1\n\ts_waitcnt lgkmcnt(0)" : "=v"(v) : "v"(addr) : "memory");
    if ((unsigned)__builtin_amdgcn_readfirstlane(v) >= n) break;
    __builtin_amdgcn_s_sleep(1);
  }
}
DI rsrc_t mk_rsrc(const void* p, int bytes) { return __builtin_amdgcn_make_buffer_rsrc((void*)p, 0, bytes, 0x00020000); }
DI void st_sc1(u32x4 v, rsrc_t r, unsigned off) { __builtin_amdgcn_raw_buffer_store_b128(v, r, off, 0, 16); }
DI u32x4 ld_sc1(rsrc_t r, unsigned off, unsigned soff) { return __builtin_amdgcn_raw_buffer_load_b128(r, off, soff, 16); }
DI int opaque(int x) { asm volatile("" : "+v"(x)); return x; }
DI unsigned umax3(unsigned a, unsigned b, unsigned c) { return max(max(a, b), c); }
DI unsigned nosent(unsigned x) { return min(x, 0xFFFFFFFEu); }
DI void swap16(unsigned& a, unsigned& b) { auto r = __builtin_amdgcn_permlane16_swap(a, b, false, false); a = r[0]; b = r[1]; }
DI f32x4 zero4() { return f32x4{0.f, 0.f, 0.f, 0.f}; }
DI float fmax3(float a, float b, float c) { return fmaxf(fmaxf(a, b), c); }
DI float fmin3(float a, float b, float c) { return fminf(fminf(a, b), c); }
DI void poll_tag(rsrc_t r, unsigned off, unsigned tag, unsigned long long mask, unsigned& p0, unsigned& p1) {
  unsigned cnt = SPIN_G;
  asm volatile(
      "buffer_load_dword %0, %3, %4, 0 offen sc1\n"
      "L_poll_%=:\n\t"
      "buffer_load_dword %1, %3, %4, 0 offen sc1\n\t"
      "s_waitcnt vmcnt(1)\n\t"
      "v_cmp_ne_u32 vcc, %5, %0\n\t"
      "s_nop 1\n\t"
      "s_and_b64 vcc, vcc, %6\n\t"
      "s_cbranch_vccz L_done_%=\n\t"
      "buffer_load_dword %0, %3, %4, 0 offen sc1\n\t"
      "s_waitcnt vmcnt(1)\n\t"
      "v_cmp_ne_u32 vcc, %5, %1\n\t"
      "s_nop 1\n\t"
      "s_and_b64 vcc, vcc, %6\n\t"
      "s_cbranch_vccz L_done_%=\n\t"
      "s_sub_u32 %2, %2, 1\n\t"
      "s_cmp_lg_u32 %2, 0\n\t"
      "s_cbranch_scc1 L_poll_%=\n"
      "L_done_%=:"
      : "=&v"(p0), "=&v"(p1), "+s"(cnt) : "v"(off), "s"(r), "s"(tag), "s"(mask) : "vcc", "scc", "memory");
}
DI void st_tag(rsrc_t r, unsigned line, unsigned tag, int lane) { st_sc1(u32x4{tag, tag, tag, tag}, r, lane < 8 ? line + lane * 16 : OOB); }

DI int krow(int rho) { return 8 * ((rho >> 2) & 3) + 4 * (rho >> 4) + (rho & 3); }

template <bool ISB> DI void feature_map(const f32x4 (&P)[5], unsigned (&ph)[ISB ? 18 : 10]) {
  float p[18];
#pragma unroll
  for (int f = 0; f < 18; ++f) p[f] = P[f >> 2][f & 3];
  if constexpr (ISB) {
    float mx = fmax3(p[0], p[1], p[2]), mn = fmin3(p[0], p[1], p[2]);
#pragma unroll
    for (int f = 3; f < 17; f += 2) { mx = fmax3(mx, p[f], p[f + 1]); mn = fmin3(mn, p[f], p[f + 1]); }
    mx = fmax3(mx, p[17], p[17]); mn = fmin3(mn, p[17], p[17]);
    const f32x2 cp = {-mx * LOG2E, -mx * LOG2E}, cn = {mn * LOG2E, mn * LOG2E};
    f32x2 lp = {LOG2E, LOG2E}, ln = {-LOG2E, -LOG2E};
    asm("" : "+v"(lp), "+v"(ln));
    f32x2 ep[9], en[9];
#pragma unroll
    for (int f = 0; f < 9; ++f) {
      const f32x2 x = {p[2 * f], p[2 * f + 1]};
      const f32x2 a = __builtin_elementwise_fma(x, lp, cp), b = __builtin_elementwise_fma(x, ln, cn);
      ep[f] = f32x2{__builtin_amdgcn_exp2f(a[0]), __builtin_amdgcn_exp2f(a[1])};
      en[f] = f32x2{__builtin_amdgcn_exp2f(b[0]), __builtin_amdgcn_exp2f(b[1])};
    }
    f32x2 sp = ep[0], sn = en[0];
#pragma unroll
    for (int f = 1; f < 9; ++f) { sp += ep[f]; sn += en[f]; }
    const float rp = __builtin_amdgcn_rcpf(sp[0] + sp[1]), rn = __builtin_amdgcn_rcpf(sn[0] + sn[1]);
#pragma unroll
    for (int f = 0; f < 9; ++f) {
      const f32x2 a = ep[f] * rp, b = en[f] * rn;
      ph[f] = pk2(a[0], a[1]);
      ph[9 + f] = pk2(b[0], b[1]);
    }
  } else {
    f32x2 l2 = {LOG2E, LOG2E}, eps = {1e-6f, 1e-6f};
    asm("" : "+v"(l2), "+v"(eps));
#pragma unroll
    for (int f = 0; f < 9; ++f) {
      const f32x2 lo = f32x2{fminf(p[2 * f], 0.f), fminf(p[2 * f + 1], 0.f)} * l2;
      const f32x2 hi = f32x2{fmaxf(p[2 * f], 0.f), fmaxf(p[2 * f + 1], 0.f)};
      const f32x2 y = (hi + f32x2{__builtin_amdgcn_exp2f(lo[0]), __builtin_amdgcn_exp2f(lo[1])}) + eps;
      ph[f] = pk2(y[0], y[1]);
    }
    ph[9] = 0u;
  }
}

struct WF { bf16x8 a[5][2]; u32x2 h[5]; };
DI void load_wf_tile(LAS char* lds, int b0, int b1, WF& f, int t) {
  f.a[t][0] = lds128(lds, b0 + (2 * t) * 1024);
  f.a[t][1] = lds128(lds, b0 + (2 * t + 1) * 1024);
  f.h[t] = *(LAS u32x2*)(lds + b1 + t * 512);
}
DI void logits2(const WF& f, const f32x4 (&t0)[2], const f32x4 (&t1)[2], const f32x4 (&t2)[2], const f32x4 (&t3)[2],
                const f32x4 (&t4)[2], f32x4 (&P)[2][5]) {
#pragma unroll
  for (int m = 0; m < 2; ++m) {
    const bf16x8 b0 = pack_pair(t0[m], t1[m]), b1 = pack_pair(t2[m], t3[m]);
    const u32x2 b2 = u32x2{pk2(t4[m][0], t4[m][1]), pk2(t4[m][2], t4[m][3])};
#pragma unroll
    for (int t = 0; t < 5; ++t) P[m][t] = mfma(f.a[t][0], b0, zero4());
#pragma unroll
    for (int t = 0; t < 5; ++t) P[m][t] = mfma(f.a[t][1], b1, P[m][t]);
#pragma unroll
    for (int t = 0; t < 5; ++t) P[m][t] = mfma16(f.h[t], b2, P[m][t]);
  }
}
DI int wf_offset(int h, int d, int f) {
  const int col = 18 * h + d, s = col >> 5, ln = 16 * ((col >> 2) & 3) + 4 * h + (f & 3), tt = f >> 2;
  return s < 2 ? (tt * 2 + s) * 1024 + ln * 16 + (((col >> 4) & 1) * 4 + (col & 3)) * 2 : 10240 + tt * 512 + ln * 8 + (col & 3) * 2;
}
template <int N> DI void cwait_lgkm() { __builtin_amdgcn_s_waitcnt(0xC07F | (N << 8)); }
DI void step5(LAS char* lds, f32x4 (&acc)[5][2], const bf16x8 (&wc)[5], bf16x8 (&wn)[5], bf16x8 x0, bf16x8 x1, int next, bool more) {
#pragma unroll
  for (int j = 0; j < 10; ++j) {
    if (j == 6) { if (more) cwait_lgkm<10>(); else cwait_lgkm<0>(); }
    mfma_acc(acc[j >> 1][j & 1], wc[j >> 1], (j & 1) ? x1 : x0);
    if (more && j < 5) wn[j] = lds_tr(lds, next + j * 32, 16 * 160);
    SBAR();
  }
}

template <bool ISB> DI void c_path(const Args& a, LAS char* lds, const int w, const int lane0, const int t, const int g) {
  constexpr int prob = ISB ? 1 : 0;
  constexpr int RP = ISB ? 288 : 160;
  constexpr int C5 = ISB ? 5 : 3;
  constexpr int KVROW = C5 * 16, KVHEAD = ISB ? 1536 : 1024, NCH = 19 * C5;
  char* ws = a.ws;
  const rsrc_t rws = mk_rsrc(ws, WS_USED);
  const unsigned kvp_grp = WS_KVP + (prob * 8 + g) * 16 * KVP_STRIDE;
  const unsigned obuf = WS_O + (prob * 16 + t) * O_STRIDE;
  constexpr int NU = ISB ? 9 : 5, UMAIN = NU - 1;
  f32x4 acc[9][2];
  WF wf;
  unsigned T;
  int a_v[5][2], a_one, a_prow, a_pm, a_pl, a_b0, a_b1, a_mu, a_al, a_la;
  unsigned o_p0, o_p1, o_mu, o_ml;
  STAMP_DECL
  {
    const int lane = lane0, tid = w * 64 + lane, i = lane & 15, gg = lane >> 4;
    STAMP(0); CSTAMP(43);
    T = __hip_atomic_load((GAS unsigned*)(ws + WS_E + prob * 256), RLX_AGENT) + 1u;
    unsigned short wel[6];
    if constexpr (ISB) {
      const rsrc_t rp = mk_rsrc(a.wphi, 32 * 324 * 2);
#pragma unroll
      for (int k = 0; k < 6; ++k) {
        const int idx = lane + 64 * k;
        wel[k] = __builtin_amdgcn_raw_buffer_load_b16(rp, idx < 324 ? ((4 * g + w) * 324 + idx) * 2 : OOB, 0, 0);
      }
    }
    {
      const u32x4 s = {SENT, SENT, SENT, SENT};
#pragma unroll
      for (int k = 0; k < (KVHEAD / 16 + 63) / 64; ++k) {
        const int n = lane + 64 * k;
        st_sc1(s, rws, n < KVHEAD / 16 ? kvp_grp + t * KVP_STRIDE + w * KVHEAD + n * 16 : OOB);
      }
    }
    if (tid < 64) *(LAS unsigned*)(lds + L_SYNC + tid * 4) = tid == SY_TAG ? T : 0u;
    if (tid < 32) *(LAS unsigned*)(lds + L_ZERO + tid * 4) = 0u;
#pragma unroll
    for (int n = 0; n < 9; ++n) { acc[n][0] = zero4(); acc[n][1] = zero4(); asm volatile("" : "+v"(acc[n][0]), "+v"(acc[n][1])); }
    const int xa0 = (32 * w + i) * 128 + ((gg ^ ((i >> 1) & 7)) * 16), xa1 = xa0 ^ 64;
    const int wa = WPART + (4 * gg + (i >> 2)) * 288 + (i & 3) * 8;
    const int wm = wa + ((i & 2) ? 256 : 128);
    {
      const int xs = ((i >> 2) & 3) << 3;
      const int vrow = L_V + (32 * w + i) * 160;
#pragma unroll
      for (int n = 0; n < 5; ++n) {
#pragma unroll
        for (int pp = 0; pp < 2; ++pp) {
          const int c0 = (n < 4 ? 16 * n : 56) + 2 * pp, h0 = c0 / 18;
          const int thr = (18 * (h0 + 1) - c0 + 3) / 4;
          a_v[n][pp] = opaque(vrow + ((2 * c0 + 4 * h0 + 8 * gg + ((thr < 4 && gg >= thr) ? 4 : 0)) ^ xs));
        }
      }
      a_one = opaque(vrow + ((40 * gg + 36) ^ xs));
      a_prow = opaque(L_PHI + (32 * w + i) * RP);
      a_pm = opaque((UMAIN * gg * 8) ^ xs);
      a_pl = opaque(L_PHI + (32 * w + i) * RP + (((4 * UMAIN + gg) * 8) ^ xs));
      const int q = i >> 2, p = i & 3;
      const int pa = L_PHI + (4 * gg + q) * RP, va = L_V + (4 * gg + q) * 160;
      a_b0 = opaque(pa + ((UMAIN * w + p) ^ gg) * 8); a_b1 = opaque(pa + ((UMAIN * w + 4 + p) ^ gg) * 8);
      a_mu = opaque(pa + ((4 * UMAIN + p) ^ gg) * 8);
      a_al = opaque(va + ((5 * w + p) ^ gg) * 8); a_la = opaque(va + ((5 * p + 4) ^ gg) * 8);
      const unsigned mine = kvp_grp + t * KVP_STRIDE + w * KVHEAD;
      const bool lam = (i >> 2) == w && (i & 3) < 3;
      const int el = 16 + (i & 3), ch = ISB ? ((gg & 1) << 1) | (gg >> 1) : gg >> 1;
      const unsigned o_main = mine + i * KVROW + ch * 16, o_lam = lam ? mine + el * KVROW + ch * 16 : OOB;
      o_p0 = opaque(ISB || !(gg & 1) ? o_main : o_lam); o_p1 = opaque(o_lam);
      o_mu = opaque(gg == w ? mine + i * KVROW + (C5 - 1) * 16 : OOB);
      o_ml = opaque((gg == w && lam) ? mine + el * KVROW + (C5 - 1) * 16 : OOB);
    }
    int a_wf[6];
    {
      const u32x4 z = {0u, 0u, 0u, 0u};
#pragma unroll
      for (int k = 0; k < 3; ++k) {
        const int n = lane + 64 * k, fr = n >> 4, sl = n & 15;
        if (n < 160) *(LAS u32x4*)(lds + L_WF + fr * 1024 + (16 * (sl >> 2) + 4 * w + (sl & 3)) * 16) = z;
      }
#pragma unroll
      for (int k = 0; k < 2; ++k) {
        const int n = lane + 64 * k, fr = n >> 4, sl = n & 15;
        if (n < 80) *(LAS u32x2*)(lds + L_WF + 10240 + fr * 512 + (16 * (sl >> 2) + 4 * w + (sl & 3)) * 8) = u32x2{0u, 0u};
      }
      if constexpr (ISB) {
#pragma unroll
        for (int k = 0; k < 6; ++k) {
          const int idx = lane + 64 * k, d = idx / 18, f = idx - d * 18;
          a_wf[k] = opaque(idx < 324 ? L_WF + wf_offset(w, d, f) : L_ZERO + 124);
        }
      } else if (lane < 18) {
        *(LAS unsigned short*)(lds + L_WF + wf_offset(w, lane, lane)) = (unsigned short)0x3F80;
      }
    }
    bf16x8 wb[2][9], xb[2][2];
    auto kstep = [&](auto SC, int xo, int wo, int wmo) {
      constexpr int S = decltype(SC)::value, N = S ^ 1;
#pragma unroll
      for (int n = 0; n < 9; ++n) {
        mfma_acc(acc[n][0], wb[S][n], xb[S][0]);
        if (n == 0) { xb[N][0] = lds128(lds, xo); xb[N][1] = lds128(lds, xo + 2048); }
        else if (n <= 4) wb[N][2 * n - 1] = lds_tr(lds, 2 * n - 1 < 8 ? wo + (2 * n - 1) * 32 + (2 * n - 1 >= 4 ? 16 : 0) : wmo, 4608);
        mfma_acc(acc[n][1], wb[S][n], xb[S][1]);
        if (n <= 4) wb[N][2 * n] = lds_tr(lds, 2 * n < 8 ? wo + (2 * n) * 32 + (2 * n >= 4 ? 16 : 0) : wmo, 4608);
        SBAR();
      }
    };
    using I0 = std::integral_constant<int, 0>;
    using I1 = std::integral_constant<int, 1>;
    STAMP(1);
    barrier();
    STAMP(2); CSTAMP(44);
    {
      const int xo = opaque(xa0 + 3 * SLOT), wo = opaque(wa + 3 * SLOT), wmo = opaque(wm + 3 * SLOT);
      xb[0][0] = lds128(lds, xo); xb[0][1] = lds128(lds, xo + 2048);
#pragma unroll
      for (int n = 0; n < 9; ++n) wb[0][n] = lds_tr(lds, n < 8 ? wo + n * 32 + (n >= 4 ? 16 : 0) : wmo, 4608);
    }
    int base = 3 * SLOT;
    unsigned pfq = 0u;
#pragma unroll
    for (int c = 0; c < 8; ++c) {
      {
        const int xo = opaque(xa1 + base), wo = opaque(wa + base + 9216), wmo = opaque(wm + base + 9216);
        cwait_lgkm0();
        SBAR();
        kstep(I0{}, xo, wo, wmo);
      }
      if (PFQ && c == 1) {
        const int row = 18 * (blockIdx.x >> 3) + tid / 9, ln = tid - 9 * (tid / 9);
        pfq = __builtin_amdgcn_raw_buffer_load_b32(mk_rsrc(ISB ? a.wB : a.wA, 576 * WROW), tid < 162 ? row * WROW + ln * 128 : OOB, 0, 0);
      }
      if constexpr (ISB) {
        if (c == 3) {
#pragma unroll
          for (int k = 0; k < 6; ++k) *(LAS unsigned short*)(lds + a_wf[k]) = wel[k];
        }
      }
      if (c == 5) { asm volatile("" :: "v"(pfq)); wait_vm<0>(); }
      cwait_lgkm0();
      barrier();
      base = base == 3 * SLOT ? 0 : base + SLOT;
      {
        const int xo = opaque(xa0 + base), wo = opaque(wa + base), wmo = opaque(wm + base);
        SBAR();
        kstep(I1{}, xo, wo, wmo);
      }
    }
    {
      const int xo = opaque(xa1 + base), wo = opaque(wa + base + 9216), wmo = opaque(wm + base + 9216);
      cwait_lgkm0();
      SBAR();
      kstep(I0{}, xo, wo, wmo);
      cwait_lgkm0();
      lds_arrive(lds, SY_KV8);
      const int b0 = opaque(L_WF + lane * 16), b1 = opaque(L_WF + 10240 + lane * 8);
      SBAR();
#pragma unroll
      for (int n = 0; n < 9; ++n) {
        mfma_acc(acc[n][0], wb[1][n], xb[1][0]);
        if (n < 5) load_wf_tile(lds, b0, b1, wf, n);
        mfma_acc(acc[n][1], wb[1][n], xb[1][1]);
        SBAR();
      }
      lds_wait(lds, SY_KV8, 8u);
      mfma_fence(acc);
    }
  }
  const int lane = opaque(lane0), tid = w * 64 + lane, i = lane & 15, gg = lane >> 4;
  const unsigned Ts = __builtin_amdgcn_readfirstlane(T);
  __builtin_amdgcn_s_setprio(2);
  STAMP(3); CSTAMP(45);

  {
    f32x4 P[2][5];
    bf16x8 kb[2][2];
    u32x2 kb2[2];
#pragma unroll
    for (int m = 0; m < 2; ++m) {
      kb[m][0] = pack_pair(acc[0][m], acc[1][m]); kb[m][1] = pack_pair(acc[2][m], acc[3][m]);
      kb2[m] = u32x2{pk2(acc[8][m][0], acc[8][m][1]), pk2(acc[8][m][2], acc[8][m][3])};
    }
    SBAR();
#pragma unroll
    for (int st = 0; st < 30; ++st) {
      const int m = st / 15, s = (st % 15) / 5, tt = st % 5;
      if (s == 0) P[m][tt] = mfma(wf.a[tt][0], kb[m][0], zero4());
      if (s == 1) P[m][tt] = mfma(wf.a[tt][1], kb[m][1], P[m][tt]);
      if (s == 2) P[m][tt] = mfma16(wf.h[tt], kb2[m], P[m][tt]);
      if (st < 20) {
        const int n = st >> 2, pp = (st >> 1) & 1, mv = st & 1;
        const f32x4 v = n < 4 ? acc[4 + n][mv] : acc[8][mv];
        const int am = n < 4 ? a_v[n][pp] + mv * 2560 : (gg >= 2 ? a_v[n][pp] + mv * 2560 : L_ZERO + 124);
        *(LAS unsigned*)(lds + am) = pk2(v[2 * pp], v[2 * pp + 1]);
      }
      SBAR();
    }
    *(LAS unsigned*)(lds + a_one) = 0x00003F80u; *(LAS unsigned*)(lds + a_one + 2560) = 0x00003F80u;
    FSTAMP(18);
#pragma unroll
    for (int m = 0; m < 2; ++m) {
      unsigned ph[ISB ? 18 : 10];
      feature_map<ISB>(P[m], ph);
#pragma unroll
      for (int j = 0; j < NU; ++j)
        *(LAS u32x2*)(lds + (j < UMAIN ? a_prow + (a_pm ^ (j * 8)) : a_pl) + m * 16 * RP) = u32x2{ph[2 * j], ph[2 * j + 1]};
      FSTAMP(19 + m);
    }
  }
  STAMP(4);
  lds_arrive(lds, SY_PHI);
  lds_wait(lds, SY_PHI);
  STAMP(5);

  {
    bf16x8 al[4], la[4], b0[4], mu[4], b1[4];
    f32x4 d0 = zero4(), d1 = zero4(), d2 = zero4(), d3 = zero4(), d4 = zero4(), d5 = zero4();
    asm volatile("" : "+v"(d0), "+v"(d1), "+v"(d2), "+v"(d3), "+v"(d4), "+v"(d5));
    auto rd = [&](int ks) {
      al[ks] = lds_tr(lds, a_al + ks * 32 * 160, 16 * 160);
      b0[ks] = lds_tr(lds, a_b0 + ks * 32 * RP, 16 * RP);
      if constexpr (ISB) b1[ks] = lds_tr(lds, a_b1 + ks * 32 * RP, 16 * RP);
      la[ks] = lds_tr(lds, a_la + ks * 32 * 160, 16 * 160);
      mu[ks] = lds_tr(lds, a_mu + ks * 32 * RP, 16 * RP);
      SBAR();
    };
    auto mm = [&](int ks) {
      mfma_acc(d0, b0[ks], al[ks]);
      if constexpr (ISB) mfma_acc(d1, b1[ks], al[ks]);
      mfma_acc(d2, b0[ks], la[ks]);
      if constexpr (ISB) mfma_acc(d3, b1[ks], la[ks]);
      mfma_acc(d4, mu[ks], al[ks]); mfma_acc(d5, mu[ks], la[ks]);
      SBAR();
    };
    constexpr int RD = ISB ? 10 : 8;
    rd(0); rd(1);
    __builtin_amdgcn_s_waitcnt(0xC07F & ~0xF00 | (RD << 8)); SBAR(); mm(0); rd(2);
    __builtin_amdgcn_s_waitcnt(0xC07F & ~0xF00 | (RD << 8)); SBAR(); mm(1); rd(3);
    __builtin_amdgcn_s_waitcnt(0xC07F & ~0xF00 | (RD << 8)); SBAR(); mm(2);
    cwait_lgkm0(); SBAR(); mm(3);
    asm volatile("s_nop 7\n\ts_nop 3" : "+v"(d0), "+v"(d1), "+v"(d2), "+v"(d3), "+v"(d4), "+v"(d5));
    if constexpr (ISB) {
      unsigned x0 = pk2(d0[0], d0[1]), x1 = pk2(d0[2], d0[3]), y0 = pk2(d1[0], d1[1]), y1 = pk2(d1[2], d1[3]);
      swap16(x0, y0); swap16(x1, y1);
      st_sc1(u32x4{nosent(x0), nosent(x1), nosent(y0), nosent(y1)}, rws, o_p0);
      x0 = pk2(d2[0], d2[1]); x1 = pk2(d2[2], d2[3]); y0 = pk2(d3[0], d3[1]); y1 = pk2(d3[2], d3[3]);
      swap16(x0, y0); swap16(x1, y1);
      st_sc1(u32x4{nosent(x0), nosent(x1), nosent(y0), nosent(y1)}, rws, o_p1);
    } else {
      unsigned x0 = pk2(d0[0], d0[1]), x1 = pk2(d0[2], d0[3]), y0 = pk2(d2[0], d2[1]), y1 = pk2(d2[2], d2[3]);
      swap16(x0, y0); swap16(x1, y1);
      st_sc1(u32x4{nosent(x0), nosent(x1), nosent(y0), nosent(y1)}, rws, o_p0);
    }
    st_sc1(u32x4{nosent(pk2(d4[0], d4[1])), nosent(pk2(d4[2], d4[3])), 0u, 0u}, rws, o_mu);
    st_sc1(u32x4{nosent(pk2(d5[0], d5[1])), nosent(pk2(d5[2], d5[3])), 0u, 0u}, rws, o_ml);
    st_tag(rws, WS_H1 + (((prob * 8 + g) * 16 + t) * 4 + w) * 128, T, lane);
    lds_arrive(lds, SY_KVDONE);
  }
  __builtin_amdgcn_s_setprio(0);
  STAMP(6); CSTAMP(46);
  const rsrc_t rwo = mk_rsrc(ISB ? a.woB : a.woA, 576 * XROW);
  {
    STAMP(7);
    const rsrc_t rkv = mk_rsrc(ws + kvp_grp, 16 * KVP_STRIDE);
    constexpr int NG = (NCH + 15) / 16;
    const unsigned one = (i & 1) ? 0x3F800000u : 0x3F80u;
    const bf16x8 sel = frag_of((i >> 1) == 0 ? one : 0u, (i >> 1) == 1 ? one : 0u, (i >> 1) == 2 ? one : 0u, (i >> 1) == 3 ? one : 0u);
    const unsigned off0 = gg * KVP_STRIDE + w * KVHEAD + i * 16;
    const unsigned offl = 16 * (NG - 1) + i < NCH ? off0 + (NG - 1) * 256 : OOB;
    const int kvw = L_PHI + w * KVHEAD + i * 16 + gg * 8;
    unsigned pfo = 0u;
    if (PFO) {
      const int row = 18 * (blockIdx.x >> 3) + tid / 9, ln = tid - 9 * (tid / 9);
      asm volatile("buffer_load_dword %0, %1, %2, 0 offen" : "=v"(pfo) : "v"(tid < 162 ? row * XROW + ln * 128 : OOB), "s"(rwo) : "memory");
    }
    unsigned p0, p1;
    poll_tag(rws, lane < 16 ? WS_H1 + (((prob * 8 + g) * 16 + lane) * 4 + w) * 128 : OOB, Ts, 0xFFFFull, p0, p1);
    __builtin_amdgcn_s_setprio(2);
    STAMP(8);
    f32x4 sum[NG];
    {
      u32x4 v[NG][4];
#pragma unroll
      for (int cg = 0; cg < NG; ++cg) {
        const unsigned off = cg < NG - 1 ? off0 + cg * 256 : offl;
#pragma unroll
        for (int u = 0; u < 4; ++u)
          asm volatile("buffer_load_dwordx4 %0, %1, %2, %3 offen sc1" : "=v"(v[cg][u]) : "v"(off), "s"(rkv), "s"(4 * u * KVP_STRIDE) : "memory");
      }
      {
        const u32x4 s = {SENT, SENT, SENT, SENT};
#pragma unroll
        for (int k = 0; k < 5; ++k) {
          const int n = lane + 64 * k;
          st_sc1(s, rws, n < 288 ? obuf + (2 * w * 72 + 9 * g) * 256 + n * 16 + (n >= 144 ? (72 - 9) * 256 : 0) : OOB);
        }
        SBAR();
      }
      float chk = 0.f;
#pragma unroll
      for (int cg = 0; cg < NG; ++cg) {
        asm volatile("s_waitcnt vmcnt(%7)" : "+v"(v[cg][0]), "+v"(v[cg][1]), "+v"(v[cg][2]), "+v"(v[cg][3]), "+v"(p0), "+v"(p1), "+v"(pfo)
                     : "n"((NG - 1 - cg) * 4 + 5));
        SBAR();
        f32x4 d = zero4();
#pragma unroll
        for (int u = 0; u < 4; ++u) d = mfma(sel, __builtin_bit_cast(bf16x8, v[cg][u]), d);
        sum[cg] = d;
        chk += d[0];
      }
      FSTAMP(21);
      if (!__all(chk == chk)) {
        PCOUNT(22);
#pragma unroll 1
        for (int sp = 0; sp < SPIN_G; ++sp) {
          unsigned mx = 0u;
#pragma unroll 1
          for (int n = 0; n < NG * 4; ++n) {
            asm volatile("" ::: "memory");
            const u32x4 x = ld_sc1(rkv, (n >> 2) < NG - 1 ? off0 + (n >> 2) * 256 : offl, 4 * (n & 3) * KVP_STRIDE);
            mx = umax3(mx, x[0], x[1]); mx = umax3(mx, x[2], x[3]);
          }
          if (__all(mx != SENT)) break;
        }
#pragma unroll
        for (int cg = 0; cg < NG; ++cg) {
          f32x4 d = zero4();
#pragma unroll
          for (int u = 0; u < 4; ++u)
            d = mfma(sel, __builtin_bit_cast(bf16x8, ld_sc1(rkv, cg < NG - 1 ? off0 + cg * 256 : offl, 4 * u * KVP_STRIDE)), d);
          sum[cg] = d;
        }
      }
    }
    lds_wait(lds, SY_KVDONE);
    if (gg < 2) {
#pragma unroll
      for (int cg = 0; cg < NG; ++cg)
        if (cg < NG - 1 || 16 * cg + i < NCH)
          *(LAS u32x2*)(lds + kvw + cg * 256) = u32x2{pk2(sum[cg][0], sum[cg][1]), pk2(sum[cg][2], sum[cg][3])};
    }
    lds_arrive(lds, SY_KVIMG);
    wait_vm<0>();
    lds_arrive(lds, SY_PRE);
  }
  STAMP(9);
  __builtin_amdgcn_s_setprio(0);
  STAMP(10);

  {
    const rsrc_t ro = mk_rsrc(ws + obuf, O_STRIDE);
    const rsrc_t rout = mk_rsrc(a.out, 2048 * OUTROW);
    const unsigned orow = (t * 128 + 32 * w + i) * OUTROW + prob * 1152 + 144 * g;
    const unsigned o_pair = orow + (2 * (gg & 1) + (gg >> 1)) * 16, o_last = gg < 2 ? orow + gg * 16 * OUTROW + 128 : OOB;
    const int wl = (4 * gg + (i >> 2)) * 160 + (i & 3) * 8;
    const int wa2 = opaque(L_WQ + wl), wa3 = opaque(L_WQ + wl + 12 * 5120);
    lds_wait(lds, SY_WO);
    unsigned p0, p1;
    poll_tag(rws, lane < 8 ? WS_H2 + (((prob * 16 + t) * 8 + lane) * 4 + w) * 128 : OOB, Ts, 0xFFull, p0, p1);
    __builtin_amdgcn_s_setprio(2);
    STAMP(11);
    f32x4 oc[5][2];
#pragma unroll 1
    for (int pass = 0; pass < 2; ++pass) {
      asm volatile("" ::: "memory");
      u32x4 of[18][2];
#pragma unroll
      for (int ks = 0; ks < 18; ++ks) {
#pragma unroll
        for (int m = 0; m < 2; ++m) { of[ks][m] = ld_sc1(ro, lane * 16, ((2 * w + m) * 72 + 4 * ks) * 256); SBAR(); }
      }
      bf16x8 wq[2][5];
#pragma unroll
      for (int n = 0; n < 5; ++n) {
        wq[0][n] = lds_tr(lds, wa2 + n * 32, 16 * 160);
        oc[n][0] = zero4(); oc[n][1] = zero4();
        asm volatile("" : "+v"(oc[n][0]), "+v"(oc[n][1]));
      }
      SBAR();
#define OUT_STEP(ks) do { cwait_vm_lgkm4<34 - 2 * (ks)>(); SBAR(); \
      step5(lds, oc, wq[(ks) & 1], wq[((ks) + 1) & 1], __builtin_bit_cast(bf16x8, of[ks][0]), __builtin_bit_cast(bf16x8, of[ks][1]), \
            (ks) + 1 < 12 ? wa2 + ((ks) + 1) * 5120 : wa3 + ((ks) + 1 - 12) * 5120, (ks) < 17); } while (0)
      OUT_STEP(0);
      asm volatile("" :: "v"(p0), "v"(p1));
      OUT_STEP(1); OUT_STEP(2); OUT_STEP(3); OUT_STEP(4); OUT_STEP(5); OUT_STEP(6); OUT_STEP(7); OUT_STEP(8);
      OUT_STEP(9); OUT_STEP(10); OUT_STEP(11); OUT_STEP(12); OUT_STEP(13); OUT_STEP(14); OUT_STEP(15); OUT_STEP(16); OUT_STEP(17);
#undef OUT_STEP
      mfma_fence(oc);
      const float chk = oc[0][0][0] + oc[0][1][0];
      if (pass == 1 || __all(chk == chk)) break;
      PCOUNT(23);
#pragma unroll 1
      for (int sp = 0; sp < SPIN_G; ++sp) {
        unsigned mx = 0u;
#pragma unroll 1
        for (int n = 0; n < 36; ++n) {
          asm volatile("" ::: "memory");
          const u32x4 x = ld_sc1(ro, lane * 16, ((2 * w + (n & 1)) * 72 + 4 * (n >> 1)) * 256);
          mx = umax3(mx, x[0], x[1]); mx = umax3(mx, x[2], x[3]);
        }
        if (__all(mx != SENT)) break;
      }
    }
    STAMP(12);
#pragma unroll
    for (int m = 0; m < 2; ++m) {
#pragma unroll
      for (int n = 0; n < 4; n += 2) {
        unsigned x0 = pk2(oc[n][m][0], oc[n][m][1]), x1 = pk2(oc[n][m][2], oc[n][m][3]);
        unsigned y0 = pk2(oc[n + 1][m][0], oc[n + 1][m][1]), y1 = pk2(oc[n + 1][m][2], oc[n + 1][m][3]);
        swap16(x0, y0); swap16(x1, y1);
        __builtin_amdgcn_raw_buffer_store_b128(u32x4{x0, x1, y0, y1}, rout, o_pair + m * 16 * OUTROW + n * 32, 0, 0);
      }
    }
    unsigned x0 = pk2(oc[4][0][0], oc[4][0][1]), x1 = pk2(oc[4][0][2], oc[4][0][3]);
    unsigned y0 = pk2(oc[4][1][0], oc[4][1][1]), y1 = pk2(oc[4][1][2], oc[4][1][3]);
    swap16(x0, y0); swap16(x1, y1);
    __builtin_amdgcn_raw_buffer_store_b128(u32x4{x0, x1, y0, y1}, rout, o_last, 0, 0);
    if (t == 0 && g == 0 && tid == 0) __hip_atomic_store((GAS unsigned*)(ws + WS_E + prob * 256), T, RLX_AGENT);
  }
  STAMP(13); CSTAMP(47);
  STAMP_OUT(w);
}

template <bool ISB> DI void h_path(const Args& a, LAS char* lds, const int w, const int lane, const int t, const int g) {
  constexpr int prob = ISB ? 1 : 0;
  constexpr int C5 = ISB ? 5 : 3;
  constexpr int KVROW = C5 * 16, KVHEAD = ISB ? 1536 : 1024;
  const int i = lane & 15, gg = lane >> 4;
  const rsrc_t rx = mk_rsrc((const char*)(ISB ? a.xB : a.xA) + (size_t)t * 128 * XROW, 128 * XROW);
  const rsrc_t rw = mk_rsrc(ISB ? a.wB : a.wA, 576 * WROW);
  const unsigned lb = (unsigned)(size_t)lds;
  STAMP_DECL
  STAMP(0);
  const int rot = ROT == 1 ? (g + 2 * (t & 3) + ((t >> 1) & 1)) % 9 : ROT == 2 ? (g + 8 * (t & 3)) % 9 : 0;
  auto kchunk = [&](int c) { const int k = c + rot; return k >= 9 ? k - 9 : k; };
  unsigned vx[2], vw[5], vq[8];
#pragma unroll
  for (int par = 0; par < 2; ++par) vx[par] = (lane >> 3) * XROW + (((lane & 7) ^ ((4 * par + (lane >> 4)) & 7)) * 16);
  auto issue_x = [&](int c) {
    const unsigned sb = lb + ((c + 3) & 3) * SLOT;
#pragma unroll
    for (int jj = 0; jj < 4; ++jj) dma16(rx, vx[jj & 1], (4 * w + jj) * 8 * XROW + kchunk(c) * 128, sb + (4 * w + jj) * 1024);
  };
  issue_x(0);
#pragma unroll
  for (int n = 0; n < 5; ++n) {
    const int nn = (w + 4 * n) * 64 + lane, r64 = nn / 18, cp = nn - 18 * r64;
    const int co = cp < 9 ? 1152 + 16 * cp : 2304 + 16 * (cp - 9);
    vw[n] = ((r64 & 32) + krow(r64 & 31)) * WROW + 144 * g + co;
  }
  auto issue_w = [&](int c) {
    const unsigned sb = lb + ((c + 3) & 3) * SLOT;
#pragma unroll
    for (int n = 0; n < 5; ++n)
      if (n < 4 || w < 2) dma16(rw, vw[n], kchunk(c) * 64 * WROW, sb + WPART + (w + 4 * n) * 1024);
  };
  issue_w(0); issue_x(1); issue_w(1);
  STAMP(1);
  if (w < 2) wait_vm<9>(); else wait_vm<8>();
  STAMP(2);
  barrier();
  issue_x(2); issue_w(2);
#pragma unroll
  for (int n = 0; n < 8; ++n) {
    const int v = w + 4 * n, ks = v / 5, nn = (v - 5 * ks) * 64 + lane, rho = nn / 10, cp = nn - 10 * rho;
    vq[n] = ((ks & 1) * 32 + krow(rho)) * WROW + 144 * g + 16 * min(cp, 8);
  }
  auto issue_wq = [&](int grp) {
#pragma unroll
    for (int n = 0; n < 8; ++n)
      if (n < 7 || w < 2)
        dma16(rw, vq[n], kchunk(3 * grp + ((w + 4 * n) / 5 >> 1)) * 64 * WROW, lb + L_WQ + grp * 30720 + (w + 4 * n) * 1024);
  };
  bf16x8 X[18][2];
  const int xa0 = (32 * w + i) * 128 + ((gg ^ ((i >> 1) & 7)) * 16), xa1 = xa0 ^ 64;
#pragma unroll
  for (int c = 0; c < 9; ++c) {
    const int base = ((c + 3) & 3) * SLOT;
    const int xo0 = opaque(xa0 + base), xo1 = opaque(xa1 + base);
    X[2 * c][0] = lds128(lds, xo0); X[2 * c][1] = lds128(lds, xo0 + 2048);
    X[2 * c + 1][0] = lds128(lds, xo1); X[2 * c + 1][1] = lds128(lds, xo1 + 2048);
    if (c <= 5) { issue_x(c + 3); issue_w(c + 3); }
    LSTAMP(16 + c);
    if (c <= 5) { if (w < 2) wait_vm<18>(); else wait_vm<16>(); }
    if (c == 6) { if (w < 2) wait_vm<9>(); else wait_vm<8>(); }
    if (c == 7) wait_vm<0>();
    LSTAMP(25 + c);
    if (c < 8) barrier(); else lds_arrive(lds, SY_KV8);
    LSTAMP(34 + c);
  }
  issue_wq(0); issue_wq(1); issue_wq(2);
  __builtin_amdgcn_s_setprio(1);
  STAMP(3);
  f32x4 qa[5][2];
#pragma unroll
  for (int n = 0; n < 5; ++n) { qa[n][0] = zero4(); qa[n][1] = zero4(); asm volatile("" : "+v"(qa[n][0]), "+v"(qa[n][1])); }
  const int wa2 = opaque(L_WQ + (4 * gg + (i >> 2)) * 160 + (i & 3) * 8), wa3 = opaque(wa2 + 12 * 5120);
  bf16x8 wq[2][5];
  if (w < 2) wait_vm<16>(); else wait_vm<14>();
  lds_arrive(lds, SY_WQ0);
  lds_wait(lds, SY_WQ0);
  STAMP(4);
#pragma unroll
  for (int n = 0; n < 5; ++n) wq[0][n] = lds_tr(lds, wa2 + n * 32, 16 * 160);
#pragma unroll
  for (int ks = 0; ks < 18; ++ks) {
    cwait_lgkm<4>();
    if (ks == 5) { if (w < 2) wait_vm<8>(); else wait_vm<7>(); lds_arrive(lds, SY_WQ1); lds_wait(lds, SY_WQ1); STAMP(5); }
    if (ks == 11) { wait_vm<0>(); lds_arrive(lds, SY_WQ2); lds_wait(lds, SY_WQ2); STAMP(6); }
    SBAR();
    step5(lds, qa, wq[ks & 1], wq[(ks + 1) & 1], X[ks][0], X[ks][1], ks + 1 < 12 ? wa2 + (ks + 1) * 5120 : wa3 + (ks + 1 - 12) * 5120, ks < 17);
  }
  WF wf;
  {
    const int b0 = opaque(L_WF + lane * 16), b1 = opaque(L_WF + 10240 + lane * 8);
#pragma unroll
    for (int n = 0; n < 5; ++n) load_wf_tile(lds, b0, b1, wf, n);
  }
  mfma_fence(qa);
  lds_arrive(lds, SY_QDONE);
  STAMP(7);
  const unsigned T = *(LAS unsigned*)(lds + L_SYNC + SY_TAG * 4);
  unsigned ph[2][ISB ? 18 : 10];
  {
    f32x4 P[2][5];
    logits2(wf, qa[0], qa[1], qa[2], qa[3], qa[4], P);
    feature_map<ISB>(P[0], ph[0]);
    feature_map<ISB>(P[1], ph[1]);
#pragma unroll
    for (int j = 0; j < (ISB ? 18 : 10); ++j) asm volatile("" : "+v"(ph[0][j]), "+v"(ph[1][j]));
  }
  int a_kv[5], a_st[9];
  {
    const bool own = (i >> 2) == gg;
#pragma unroll
    for (int n = 0; n < 5; ++n) {
      const int e = 4 * n + (i & 3);
      a_kv[n] = opaque((own && e < 19) ? L_PHI + gg * KVHEAD + e * KVROW : L_ZERO);
    }
#pragma unroll
    for (int pp = 0; pp < 9; ++pp) {
      const int cp = 9 * gg + pp;
      a_st[pp] = opaque(L_V + w * 4608 + (cp >> 2) * 256 + i * 16 + (cp & 3) * 4);
    }
  }
  const rsrc_t ro = mk_rsrc(a.ws + WS_O + (prob * 16 + t) * O_STRIDE, O_STRIDE);
  const unsigned o_dst = (2 * w * 72 + 9 * g) * 256 + lane * 16;
  const rsrc_t rwo = mk_rsrc(ISB ? a.woB : a.woA, 576 * XROW);
  unsigned vo[5];
#pragma unroll
  for (int n = 0; n < 5; ++n) {
    const int nn = ((w + 4 * n) % 5) * 64 + lane, rho = nn / 10, cp = nn - 10 * rho;
    vo[n] = (unsigned)opaque(krow(rho) * XROW + 144 * g + 16 * min(cp, 8));
  }
  lds_wait(lds, SY_QDONE);
#pragma unroll
  for (int n = 0; n < WO_FIRST; ++n) { const int v = w + 4 * n; dma16(rwo, vo[n % 5], (v / 5) * 32 * XROW, lb + L_WQ + v * 1024); }
  STAMP(8);
  lds_wait(lds, SY_KVIMG);
  __builtin_amdgcn_s_setprio(3);
  STAMP(9);
  f32x4 O[5][2];
  {
    bf16x8 af[5][C5];
#pragma unroll
    for (int s = 0; s < C5; ++s) {
#pragma unroll
      for (int n = 0; n < 5; ++n) af[n][s] = lds128(lds, a_kv[n] + s * 16);
    }
    SBAR();
    constexpr int NP = ISB ? 18 : 10;
#pragma unroll
    for (int s = 0; s < C5; ++s) {
      bf16x8 bq[2];
#pragma unroll
      for (int m = 0; m < 2; ++m) {
        unsigned u[4];
#pragma unroll
        for (int j = 0; j < 4; ++j) u[j] = 4 * s + j < NP ? ph[m][4 * s + j < NP ? 4 * s + j : 0] : 0u;
        bq[m] = frag_of(u[0], u[1], u[2], u[3]);
      }
#pragma unroll
      for (int n = 4; n >= 0; --n) {
#pragma unroll
        for (int m = 0; m < 2; ++m) O[n][m] = mfma(af[n][s], bq[m], s == 0 ? zero4() : O[n][m]);
      }
    }
  }
  STAMP(10);
#pragma unroll
  for (int m = 0; m < 2; ++m) {
    const float rc = __builtin_amdgcn_rcpf(O[4][m][2] + 1e-6f);
    const f32x2 rc2 = {rc, rc};
#pragma unroll
    for (int pp = 0; pp < 9; ++pp) {
      const int e = 2 * pp;
      const f32x2 y = f32x2{O[e >> 2][m][e & 3], O[(e + 1) >> 2][m][(e + 1) & 3]} * rc2;
      *(LAS unsigned*)(lds + a_st[pp] + m * 2304) = nosent(pk2(y[0], y[1]));
    }
  }
  lds_wait(lds, SY_PRE);
  {
    const int sb = L_V + w * 4608 + lane * 16;
    u32x4 v[5];
#pragma unroll
    for (int k = 0; k < 5; ++k) v[k] = *(LAS u32x4*)(lds + sb + (k < 4 || lane < 32 ? k * 1024 : 0));
#pragma unroll
    for (int k = 0; k < 5; ++k) {
      const int n = lane + 64 * k;
      st_sc1(v[k], ro, n < 288 ? o_dst + k * 1024 + (n >= 144 ? (72 - 9) * 256 : 0) : OOB);
    }
    st_tag(mk_rsrc(a.ws, WS_USED), WS_H2 + (((prob * 16 + t) * 8 + g) * 4 + w) * 128, T, lane);
  }
  STAMP(11);
#pragma unroll
  for (int n = WO_FIRST; n < 23; ++n) {
    const int v = w + 4 * n;
    if (n < 22 || w < 2) dma16(rwo, vo[n % 5], (v / 5) * 32 * XROW, lb + L_WQ + v * 1024);
  }
  wait_vm<0>();
  lds_arrive(lds, SY_WO);
  STAMP(12);
  STAMP_OUT(4 + w);
}

__global__ __launch_bounds__(512, 2) void hedgehog_fused(Args a) {
  extern __shared__ __attribute__((aligned(16))) char smem[];
  LAS char* lds = (LAS char*)smem;
  const int b = blockIdx.x, xcd = b & 7, r = b >> 3;
  const int prob = xcd >> 2, t = (xcd & 3) * 4 + (r >> 3), g = r & 7;
  const int wave = __builtin_amdgcn_readfirstlane(threadIdx.x >> 6), lane = threadIdx.x & 63;
  if (wave < 4) {
    if (prob) c_path<true>(a, lds, wave, lane, t, g); else c_path<false>(a, lds, wave, lane, t, g);
  } else {
    if (prob) h_path<true>(a, lds, wave - 4, lane, t, g); else h_path<false>(a, lds, wave - 4, lane, t, g);
  }
}

}

extern "C" void kernel_launch(void* const* d_in, const int* in_sizes, int n_in, void* d_out, int out_size, void* d_ws,
                              size_t ws_size, hipStream_t stream) {
  (void)in_sizes; (void)n_in; (void)out_size; (void)ws_size;
  Args a;
  a.xA = d_in[0]; a.wA = d_in[1]; a.woA = d_in[2]; a.xB = d_in[3]; a.wB = d_in[4]; a.wphi = d_in[5]; a.woB = d_in[6];
  a.out = d_out; a.ws = (char*)d_ws;
  (void)hipFuncSetAttribute((const void*)hedgehog_fused, hipFuncAttributeMaxDynamicSharedMemorySize, LDS_BYTES);
  hedgehog_fused<<<256, 512, LDS_BYTES, stream>>>(a);
}
```

```cpp
#include <hip/hip_runtime.h>
#include <hip/hip_bf16.h>
#include <stdint.h>
#include <type_traits>

typedef __bf16 bf16x8 __attribute__((ext_vector_type(8)));
typedef __bf16 bf16x4 __attribute__((ext_vector_type(4)));
typedef __bf16 bf16x2 __attribute__((ext_vector_type(2)));
typedef short s16x4 __attribute__((ext_vector_type(4)));
typedef float f32x4 __attribute__((ext_vector_type(4)));
typedef float f32x2 __attribute__((ext_vector_type(2)));
typedef unsigned u32x4 __attribute__((ext_vector_type(4)));
typedef unsigned u32x2 __attribute__((ext_vector_type(2)));
#define LAS __attribute__((address_space(3)))
#define GAS __attribute__((address_space(1)))
#define DI __device__ __forceinline__
#define RLX_AGENT __ATOMIC_RELAXED, __HIP_MEMORY_SCOPE_AGENT
#define SBAR() __builtin_amdgcn_sched_barrier(0)
typedef __amdgpu_buffer_rsrc_t rsrc_t;

#define DBG 0
#define PROF 0
#define STAMP(n)
#define CSTAMP(n)
#define STAMP_DECL
#define STAMP_OUT(wv)
#define PCOUNT(n)
#define LSTAMP(n)
#define FSTAMP(n)
#define PFQ 0
#define PFO 1
#define WO_FIRST 11
#define TLBW 0
#define KV_UNROLL 1
#define ROT 1

namespace {

constexpr int XROW = 1152;
constexpr int WROW = 3456;
constexpr int OUTROW = 2304;
constexpr int SLOT = 34816, WPART = 16384;
constexpr int L_WQ = 0;
constexpr int L_PHI = 92160;
constexpr int L_V = 129024;
constexpr int L_WF = 149504;
constexpr int L_SYNC = 162304;
constexpr int L_ZERO = 162560;
constexpr int LDS_BYTES = 163840;
enum { SY_WQ0 = 0, SY_WQ1, SY_WQ2, SY_QDONE, SY_PHI, SY_KVDONE, SY_KVIMG, SY_WO, SY_KV8, SY_PRE, SY_TAG = 63 };
constexpr int WS_E = 0, WS_KVP = 65536, KVP_STRIDE = 6144, WS_O = 2097152, O_STRIDE = 147456;
constexpr int WS_H1 = WS_O + 32 * O_STRIDE, WS_H2 = WS_H1 + 131072;
constexpr int WS_USED = WS_H2 + 131072;
constexpr unsigned SENT = 0xFFFFFFFFu;
constexpr unsigned OOB = 0x7FFF0000u;
constexpr int SPIN_G = 1 << 18, SPIN_L = 1 << 22;
constexpr float LOG2E = 1.4426950408889634f;

struct Args {
  const void* xA; const void* wA; const void* woA; const void* xB; const void* wB; const void* wphi; const void* woB;
  void* out; char* ws;
};

DI f32x4 mfma(bf16x8 a, bf16x8 b, f32x4 c) { return __builtin_amdgcn_mfma_f32_16x16x32_bf16(a, b, c, 0, 0, 0); }
DI f32x4 mfma16(u32x2 a, u32x2 b, f32x4 c) {
  return __builtin_amdgcn_mfma_f32_16x16x16bf16_1k(__builtin_bit_cast(s16x4, a), __builtin_bit_cast(s16x4, b), c, 0, 0, 0);
}
DI void mfma_acc(f32x4& c, bf16x8 a, bf16x8 b) { asm volatile("v_mfma_f32_16x16x32_bf16 %0, %1, %2, %0" : "+v"(c) : "v"(a), "v"(b)); }
template <int N> DI void mfma_fence(f32x4 (&acc)[N][2]) {
  asm volatile("s_nop 7\n\ts_nop 3" : "+v"(acc[N - 1][0]), "+v"(acc[N - 1][1]) :: "memory");
#pragma unroll
  for (int n = 0; n < N - 1; ++n) asm volatile("" : "+v"(acc[n][0]), "+v"(acc[n][1]));
}
DI unsigned pk2(float a, float b) { f32x2 p = {a, b}; return __builtin_bit_cast(unsigned, __builtin_convertvector(p, bf16x2)); }
DI bf16x8 frag_of(unsigned a, unsigned b, unsigned c, unsigned d) { u32x4 v = {a, b, c, d}; return __builtin_bit_cast(bf16x8, v); }
DI bf16x8 pack_pair(f32x4 lo, f32x4 hi) { return frag_of(pk2(lo[0], lo[1]), pk2(lo[2], lo[3]), pk2(hi[0], hi[1]), pk2(hi[2], hi[3])); }
DI bf16x8 lds128(LAS char* l, int off) { return *(LAS bf16x8*)(l + off); }
DI bf16x8 lds_tr(LAS char* l, int off, int second) {
  bf16x4 a = __builtin_amdgcn_ds_read_tr16_b64_v4bf16((LAS bf16x4*)(l + off));
  bf16x4 b = __builtin_amdgcn_ds_read_tr16_b64_v4bf16((LAS bf16x4*)(l + off + second));
  return __builtin_shufflevector(a, b, 0, 1, 2, 3, 4, 5, 6, 7);
}
DI void dma16(rsrc_t r, unsigned voff, unsigned soff, unsigned ldsaddr) {
  asm volatile("s_mov_b32 m0, %0\n\ts_nop 0\n\tbuffer_load_dwordx4 %1, %2, %3 offen lds" ::"s"(ldsaddr), "v"(voff), "s"(r), "s"(soff) : "memory");
}
template <int N> DI void wait_vm() { asm volatile("s_waitcnt vmcnt(%0)" ::"n"(N) : "memory"); }
DI void cwait_lgkm0() { __builtin_amdgcn_s_waitcnt(0xC07F); }
template <int N> DI void cwait_vm_lgkm4() { __builtin_amdgcn_s_waitcnt((N & 15) | 0x70 | (4 << 8) | ((N >> 4) << 14)); }
DI void barrier() { asm volatile("s_waitcnt lgkmcnt(0)\n\ts_barrier" ::: "memory"); }
DI void lds_arrive(LAS char* l, int id) {
  const unsigned addr = (unsigned)(size_t)l + L_SYNC + id * 4, one = 1u;
  unsigned long long save;
  asm volatile("s_waitcnt lgkmcnt(0)\n\ts_mov_b64 %0, exec\n\ts_mov_b64 exec, 1\n\tds_add_u32 %1, %2\n\ts_mov_b64 exec, %0"
               : "=&s"(save) : "v"(addr), "v"(one) : "memory");
}
DI void lds_wait(LAS char* l, int id, unsigned n = 4u) {
  const unsigned addr = (unsigned)(size_t)l + L_SYNC + id * 4;
#pragma unroll 1
  for (int sp = 0; sp < SPIN_L; ++sp) {
    unsigned v;
    asm volatile("ds_read_b32 %0, %1\n\ts_waitcnt lgkmcnt(0)" : "=v"(v) : "v"(addr) : "memory");
    if ((unsigned)__builtin_amdgcn_readfirstlane(v) >= n) break;
    __builtin_amdgcn_s_sleep(1);
  }
}
DI rsrc_t mk_rsrc(const void* p, int bytes) { return __builtin_amdgcn_make_buffer_rsrc((void*)p, 0, bytes, 0x00020000); }
DI void st_sc1(u32x4 v, rsrc_t r, unsigned off) { __builtin_amdgcn_raw_buffer_store_b128(v, r, off, 0, 16); }
DI u32x4 ld_sc1(rsrc_t r, unsigned off, unsigned soff) { return __builtin_amdgcn_raw_buffer_load_b128(r, off, soff, 16); }
DI int opaque(int x) { asm volatile("" : "+v"(x)); return x; }
DI unsigned umax3(unsigned a, unsigned b, unsigned c) { return max(max(a, b), c); }
DI unsigned nosent(unsigned x) { return min(x, 0xFFFFFFFEu); }
DI void swap16(unsigned& a, unsigned& b) { auto r = __builtin_amdgcn_permlane16_swap(a, b, false, false); a = r[0]; b = r[1]; }
DI f32x4 zero4() { return f32x4{0.f, 0.f, 0.f, 0.f}; }
DI float fmax3(float a, float b, float c) { return fmaxf(fmaxf(a, b), c); }
DI float fmin3(float a, float b, float c) { return fminf(fminf(a, b), c); }
DI void poll_tag(rsrc_t r, unsigned off, unsigned tag, unsigned long long mask, unsigned& p0, unsigned& p1) {
  unsigned cnt = SPIN_G;
  asm volatile(
      "buffer_load_dword %0, %3, %4, 0 offen sc1\n"
      "L_poll_%=:\n\t"
      "buffer_load_dword %1, %3, %4, 0 offen sc1\n\t"
      "s_waitcnt vmcnt(1)\n\t"
      "v_cmp_ne_u32 vcc, %5, %0\n\t"
      "s_nop 1\n\t"
      "s_and_b64 vcc, vcc, %6\n\t"
      "s_cbranch_vccz L_done_%=\n\t"
      "buffer_load_dword %0, %3, %4, 0 offen sc1\n\t"
      "s_waitcnt vmcnt(1)\n\t"
      "v_cmp_ne_u32 vcc, %5, %1\n\t"
      "s_nop 1\n\t"
      "s_and_b64 vcc, vcc, %6\n\t"
      "s_cbranch_vccz L_done_%=\n\t"
      "s_sub_u32 %2, %2, 1\n\t"
      "s_cmp_lg_u32 %2, 0\n\t"
      "s_cbranch_scc1 L_poll_%=\n"
      "L_done_%=:"
      : "=&v"(p0), "=&v"(p1), "+s"(cnt) : "v"(off), "s"(r), "s"(tag), "s"(mask) : "vcc", "scc", "memory");
}
DI void st_tag(rsrc_t r, unsigned line, unsigned tag, int lane) { st_sc1(u32x4{tag, tag, tag, tag}, r, lane < 8 ? line + lane * 16 : OOB); }

DI int krow(int rho) { return 8 * ((rho >> 2) & 3) + 4 * (rho >> 4) + (rho & 3); }

template <bool ISB> DI void feature_map(const f32x4 (&P)[5], unsigned (&ph)[ISB ? 18 : 10]) {
  float p[18];
#pragma unroll
  for (int f = 0; f < 18; ++f) p[f] = P[f >> 2][f & 3];
  if constexpr (ISB) {
    float mx = fmax3(p[0], p[1], p[2]), mn = fmin3(p[0], p[1], p[2]);
#pragma unroll
    for (int f = 3; f < 17; f += 2) { mx = fmax3(mx, p[f], p[f + 1]); mn = fmin3(mn, p[f], p[f + 1]); }
    mx = fmax3(mx, p[17], p[17]); mn = fmin3(mn, p[17], p[17]);
    const f32x2 cp = {-mx * LOG2E, -mx * LOG2E}, cn = {mn * LOG2E, mn * LOG2E};
    f32x2 lp = {LOG2E, LOG2E}, ln = {-LOG2E, -LOG2E};
    asm("" : "+v"(lp), "+v"(ln));
    f32x2 ep[9], en[9];
#pragma unroll
    for (int f = 0; f < 9; ++f) {
      const f32x2 x = {p[2 * f], p[2 * f + 1]};
      const f32x2 a = __builtin_elementwise_fma(x, lp, cp), b = __builtin_elementwise_fma(x, ln, cn);
      ep[f] = f32x2{__builtin_amdgcn_exp2f(a[0]), __builtin_amdgcn_exp2f(a[1])};
      en[f] = f32x2{__builtin_amdgcn_exp2f(b[0]), __builtin_amdgcn_exp2f(b[1])};
    }
    f32x2 sp = ep[0], sn = en[0];
#pragma unroll
    for (int f = 1; f < 9; ++f) { sp += ep[f]; sn += en[f]; }
    const float rp = __builtin_amdgcn_rcpf(sp[0] + sp[1]), rn = __builtin_amdgcn_rcpf(sn[0] + sn[1]);
#pragma unroll
    for (int f = 0; f < 9; ++f) {
      const f32x2 a = ep[f] * rp, b = en[f] * rn;
      ph[f] = pk2(a[0], a[1]);
      ph[9 + f] = pk2(b[0], b[1]);
    }
  } else {
    f32x2 l2 = {LOG2E, LOG2E}, eps = {1e-6f, 1e-6f};
    asm("" : "+v"(l2), "+v"(eps));
#pragma unroll
    for (int f = 0; f < 9; ++f) {
      const f32x2 lo = f32x2{fminf(p[2 * f], 0.f), fminf(p[2 * f + 1], 0.f)} * l2;
      const f32x2 hi = f32x2{fmaxf(p[2 * f], 0.f), fmaxf(p[2 * f + 1], 0.f)};
      const f32x2 y = (hi + f32x2{__builtin_amdgcn_exp2f(lo[0]), __builtin_amdgcn_exp2f(lo[1])}) + eps;
      ph[f] = pk2(y[0], y[1]);
    }
    ph[9] = 0u;
  }
}

struct WF { bf16x8 a[5][2]; u32x2 h[5]; };
DI void load_wf_tile(LAS char* lds, int b0, int b1, WF& f, int t) {
  f.a[t][0] = lds128(lds, b0 + (2 * t) * 1024);
  f.a[t][1] = lds128(lds, b0 + (2 * t + 1) * 1024);
  f.h[t] = *(LAS u32x2*)(lds + b1 + t * 512);
}
DI void logits2(const WF& f, const f32x4 (&t0)[2], const f32x4 (&t1)[2], const f32x4 (&t2)[2], const f32x4 (&t3)[2],
                const f32x4 (&t4)[2], f32x4 (&P)[2][5]) {
#pragma unroll
  for (int m = 0; m < 2; ++m) {
    const bf16x8 b0 = pack_pair(t0[m], t1[m]), b1 = pack_pair(t2[m], t3[m]);
    const u32x2 b2 = u32x2{pk2(t4[m][0], t4[m][1]), pk2(t4[m][2], t4[m][3])};
#pragma unroll
    for (int t = 0; t < 5; ++t) P[m][t] = mfma(f.a[t][0], b0, zero4());
#pragma unroll
    for (int t = 0; t < 5; ++t) P[m][t] = mfma(f.a[t][1], b1, P[m][t]);
#pragma unroll
    for (int t = 0; t < 5; ++t) P[m][t] = mfma16(f.h[t], b2, P[m][t]);
  }
}
DI int wf_offset(int h, int d, int f) {
  const int col = 18 * h + d, s = col >> 5, ln = 16 * ((col >> 2) & 3) + 4 * h + (f & 3), tt = f >> 2;
  return s < 2 ? (tt * 2 + s) * 1024 + ln * 16 + (((col >> 4) & 1) * 4 + (col & 3)) * 2 : 10240 + tt * 512 + ln * 8 + (col & 3) * 2;
}
template <int N> DI void cwait_lgkm() { __builtin_amdgcn_s_waitcnt(0xC07F | (N << 8)); }
DI void step5(LAS char* lds, f32x4 (&acc)[5][2], const bf16x8 (&wc)[5], bf16x8 (&wn)[5], bf16x8 x0, bf16x8 x1, int next, bool more) {
#pragma unroll
  for (int j = 0; j < 10; ++j) {
    if (j == 6) { if (more) cwait_lgkm<10>(); else cwait_lgkm<0>(); }
    mfma_acc(acc[j >> 1][j & 1], wc[j >> 1], (j & 1) ? x1 : x0);
    if (more && j < 5) wn[j] = lds_tr(lds, next + j * 32, 16 * 160);
    SBAR();
  }
}

template <bool ISB> DI void c_path(const Args& a, LAS char* lds, const int w, const int lane0, const int t, const int g) {
  constexpr int prob = ISB ? 1 : 0;
  constexpr int RP = ISB ? 288 : 160;
  constexpr int C5 = ISB ? 5 : 3;
  constexpr int KVROW = C5 * 16, KVHEAD = ISB ? 1536 : 1024, NCH = 19 * C5;
  char* ws = a.ws;
  const rsrc_t rws = mk_rsrc(ws, WS_USED);
  const unsigned kvp_grp = WS_KVP + (prob * 8 + g) * 16 * KVP_STRIDE;
  const unsigned obuf = WS_O + (prob * 16 + t) * O_STRIDE;
  constexpr int NU = ISB ? 9 : 5, UMAIN = NU - 1;
  f32x4 acc[9][2];
  WF wf;
  unsigned T;
  int a_v[5][2], a_one, a_prow, a_pm, a_pl, a_b0, a_b1, a_mu, a_al, a_la;
  unsigned o_p0, o_p1, o_mu, o_ml;
  STAMP_DECL
  {
    const int lane = lane0, tid = w * 64 + lane, i = lane & 15, gg = lane >> 4;
    STAMP(0); CSTAMP(43);
    T = __hip_atomic_load((GAS unsigned*)(ws + WS_E + prob * 256), RLX_AGENT) + 1u;
    unsigned tl0 = 0u, tl1 = 0u, tl2 = 0u;
    if (TLBW && w == 3) {
      const unsigned wsoff = lane < 3 ? obuf + lane * 65536 : lane < 5 ? kvp_grp + (lane - 3) * 65536
                           : lane == 5 ? WS_H1 + (prob * 8 + g) * 8192 : lane == 6 ? WS_H2 + (prob * 16 + t) * 4096 : OOB;
      tl0 = __builtin_amdgcn_raw_buffer_load_b32(rws, wsoff, 0, 0);
      tl1 = __builtin_amdgcn_raw_buffer_load_b32(mk_rsrc(a.out, 2048 * OUTROW), lane < 6 ? t * 128 * OUTROW + min(lane * 65536, 128 * OUTROW - 4) : OOB, 0, 0);
      tl2 = lane < 32 ? __builtin_amdgcn_raw_buffer_load_b32(mk_rsrc(ISB ? a.wB : a.wA, 576 * WROW), lane * 65536, 0, 0)
                      : __builtin_amdgcn_raw_buffer_load_b32(mk_rsrc(ISB ? a.woB : a.woA, 576 * XROW), (lane - 32) * 65536, 0, 0);
    }
    unsigned short wel[6];
    if constexpr (ISB) {
      const rsrc_t rp = mk_rsrc(a.wphi, 32 * 324 * 2);
#pragma unroll
      for (int k = 0; k < 6; ++k) {
        const int idx = lane + 64 * k;
        wel[k] = __builtin_amdgcn_raw_buffer_load_b16(rp, idx < 324 ? ((4 * g + w) * 324 + idx) * 2 : OOB, 0, 0);
      }
    }
    {
      const u32x4 s = {SENT, SENT, SENT, SENT};
#pragma unroll
      for (int k = 0; k < (KVHEAD / 16 + 63) / 64; ++k) {
        const int n = lane + 64 * k;
        st_sc1(s, rws, n < KVHEAD / 16 ? kvp_grp + t * KVP_STRIDE + w * KVHEAD + n * 16 : OOB);
      }
    }
    if (tid < 64) *(LAS unsigned*)(lds + L_SYNC + tid * 4) = tid == SY_TAG ? T : 0u;
    if (tid < 32) *(LAS unsigned*)(lds + L_ZERO + tid * 4) = 0u;
#pragma unroll
    for (int n = 0; n < 9; ++n) { acc[n][0] = zero4(); acc[n][1] = zero4(); asm volatile("" : "+v"(acc[n][0]), "+v"(acc[n][1])); }
    const int xa0 = (32 * w + i) * 128 + ((gg ^ ((i >> 1) & 7)) * 16), xa1 = xa0 ^ 64;
    const int wa = WPART + (4 * gg + (i >> 2)) * 288 + (i & 3) * 8;
    const int wm = wa + ((i & 2) ? 256 : 128);
    {
      const int xs = ((i >> 2) & 3) << 3;
      const int vrow = L_V + (32 * w + i) * 160;
#pragma unroll
      for (int n = 0; n < 5; ++n) {
#pragma unroll
        for (int pp = 0; pp < 2; ++pp) {
          const int c0 = (n < 4 ? 16 * n : 56) + 2 * pp, h0 = c0 / 18;
          const int thr = (18 * (h0 + 1) - c0 + 3) / 4;
          a_v[n][pp] = opaque(vrow + ((2 * c0 + 4 * h0 + 8 * gg + ((thr < 4 && gg >= thr) ? 4 : 0)) ^ xs));
        }
      }
      a_one = opaque(vrow + ((40 * gg + 36) ^ xs));
      a_prow = opaque(L_PHI + (32 * w + i) * RP);
      a_pm = opaque((UMAIN * gg * 8) ^ xs);
      a_pl = opaque(L_PHI + (32 * w + i) * RP + (((4 * UMAIN + gg) * 8) ^ xs));
      const int q = i >> 2, p = i & 3;
      const int pa = L_PHI + (4 * gg + q) * RP, va = L_V + (4 * gg + q) * 160;
      a_b0 = opaque(pa + ((UMAIN * w + p) ^ gg) * 8); a_b1 = opaque(pa + ((UMAIN * w + 4 + p) ^ gg) * 8);
      a_mu = opaque(pa + ((4 * UMAIN + p) ^ gg) * 8);
      a_al = opaque(va + ((5 * w + p) ^ gg) * 8); a_la = opaque(va + ((5 * p + 4) ^ gg) * 8);
      const unsigned mine = kvp_grp + t * KVP_STRIDE + w * KVHEAD;
      const bool lam = (i >> 2) == w && (i & 3) < 3;
      const int el = 16 + (i & 3), ch = ISB ? ((gg & 1) << 1) | (gg >> 1) : gg >> 1;
      const unsigned o_main = mine + i * KVROW + ch * 16, o_lam = lam ? mine + el * KVROW + ch * 16 : OOB;
      o_p0 = opaque(ISB || !(gg & 1) ? o_main : o_lam); o_p1 = opaque(o_lam);
      o_mu = opaque(gg == w ? mine + i * KVROW + (C5 - 1) * 16 : OOB);
      o_ml = opaque((gg == w && lam) ? mine + el * KVROW + (C5 - 1) * 16 : OOB);
    }
    int a_wf[6];
    {
      const u32x4 z = {0u, 0u, 0u, 0u};
#pragma unroll
      for (int k = 0; k < 3; ++k) {
        const int n = lane + 64 * k, fr = n >> 4, sl = n & 15;
        if (n < 160) *(LAS u32x4*)(lds + L_WF + fr * 1024 + (16 * (sl >> 2) + 4 * w + (sl & 3)) * 16) = z;
      }
#pragma unroll
      for (int k = 0; k < 2; ++k) {
        const int n = lane + 64 * k, fr = n >> 4, sl = n & 15;
        if (n < 80) *(LAS u32x2*)(lds + L_WF + 10240 + fr * 512 + (16 * (sl >> 2) + 4 * w + (sl & 3)) * 8) = u32x2{0u, 0u};
      }
      if constexpr (ISB) {
#pragma unroll
        for (int k = 0; k < 6; ++k) {
          const int idx = lane + 64 * k, d = idx / 18, f = idx - d * 18;
          a_wf[k] = opaque(idx < 324 ? L_WF + wf_offset(w, d, f) : L_ZERO + 124);
        }
      } else if (lane < 18) {
        *(LAS unsigned short*)(lds + L_WF + wf_offset(w, lane, lane)) = (unsigned short)0x3F80;
      }
    }
    bf16x8 wb[2][9], xb[2][2];
    auto kstep = [&](auto SC, int xo, int wo, int wmo) {
      constexpr int S = decltype(SC)::value, N = S ^ 1;
#pragma unroll
      for (int n = 0; n < 9; ++n) {
        mfma_acc(acc[n][0], wb[S][n], xb[S][0]);
        if (n == 0) { xb[N][0] = lds128(lds, xo); xb[N][1] = lds128(lds, xo + 2048); }
        else if (n <= 4) wb[N][2 * n - 1] = lds_tr(lds, 2 * n - 1 < 8 ? wo + (2 * n - 1) * 32 + (2 * n - 1 >= 4 ? 16 : 0) : wmo, 4608);
        mfma_acc(acc[n][1], wb[S][n], xb[S][1]);
        if (n <= 4) wb[N][2 * n] = lds_tr(lds, 2 * n < 8 ? wo + (2 * n) * 32 + (2 * n >= 4 ? 16 : 0) : wmo, 4608);
        SBAR();
      }
    };
    using I0 = std::integral_constant<int, 0>;
    using I1 = std::integral_constant<int, 1>;
    STAMP(1);
    barrier();
    STAMP(2); CSTAMP(44);
    {
      const int xo = opaque(xa0 + 3 * SLOT), wo = opaque(wa + 3 * SLOT), wmo = opaque(wm + 3 * SLOT);
      xb[0][0] = lds128(lds, xo); xb[0][1] = lds128(lds, xo + 2048);
#pragma unroll
      for (int n = 0; n < 9; ++n) wb[0][n] = lds_tr(lds, n < 8 ? wo + n * 32 + (n >= 4 ? 16 : 0) : wmo, 4608);
    }
    int base = 3 * SLOT;
    unsigned pfq = 0u;
#pragma unroll
    for (int c = 0; c < 8; ++c) {
      {
        const int xo = opaque(xa1 + base), wo = opaque(wa + base + 9216), wmo = opaque(wm + base + 9216);
        cwait_lgkm0();
        SBAR();
        kstep(I0{}, xo, wo, wmo);
      }
      if (PFQ && c == 1) {
        const int row = 18 * (blockIdx.x >> 3) + tid / 9, ln = tid - 9 * (tid / 9);
        pfq = __builtin_amdgcn_raw_buffer_load_b32(mk_rsrc(ISB ? a.wB : a.wA, 576 * WROW), tid < 162 ? row * WROW + ln * 128 : OOB, 0, 0);
      }
      if constexpr (ISB) {
        if (c == 3) {
#pragma unroll
          for (int k = 0; k < 6; ++k) *(LAS unsigned short*)(lds + a_wf[k]) = wel[k];
        }
      }
      if (c == 5) { asm volatile("" :: "v"(pfq), "v"(tl0), "v"(tl1), "v"(tl2)); wait_vm<0>(); }
      cwait_lgkm0();
      barrier();
      base = base == 3 * SLOT ? 0 : base + SLOT;
      {
        const int xo = opaque(xa0 + base), wo = opaque(wa + base), wmo = opaque(wm + base);
        SBAR();
        kstep(I1{}, xo, wo, wmo);
      }
    }
    {
      const int xo = opaque(xa1 + base), wo = opaque(wa + base + 9216), wmo = opaque(wm + base + 9216);
      cwait_lgkm0();
      SBAR();
      kstep(I0{}, xo, wo, wmo);
      cwait_lgkm0();
      lds_arrive(lds, SY_KV8);
      const int b0 = opaque(L_WF + lane * 16), b1 = opaque(L_WF + 10240 + lane * 8);
      SBAR();
#pragma unroll
      for (int n = 0; n < 9; ++n) {
        mfma_acc(acc[n][0], wb[1][n], xb[1][0]);
        if (n < 5) load_wf_tile(lds, b0, b1, wf, n);
        mfma_acc(acc[n][1], wb[1][n], xb[1][1]);
        SBAR();
      }
      lds_wait(lds, SY_KV8, 8u);
      mfma_fence(acc);
    }
  }
  const int lane = opaque(lane0), tid = w * 64 + lane, i = lane & 15, gg = lane >> 4;
  const unsigned Ts = __builtin_amdgcn_readfirstlane(T);
  __builtin_amdgcn_s_setprio(2);
  STAMP(3); CSTAMP(45);

  {
    f32x4 P[2][5];
    bf16x8 kb[2][2];
    u32x2 kb2[2];
#pragma unroll
    for (int m = 0; m < 2; ++m) {
      kb[m][0] = pack_pair(acc[0][m], acc[1][m]); kb[m][1] = pack_pair(acc[2][m], acc[3][m]);
      kb2[m] = u32x2{pk2(acc[8][m][0], acc[8][m][1]), pk2(acc[8][m][2], acc[8][m][3])};
    }
    SBAR();
#pragma unroll
    for (int st = 0; st < 30; ++st) {
      const int m = st / 15, s = (st % 15) / 5, tt = st % 5;
      if (s == 0) P[m][tt] = mfma(wf.a[tt][0], kb[m][0], zero4());
      if (s == 1) P[m][tt] = mfma(wf.a[tt][1], kb[m][1], P[m][tt]);
      if (s == 2) P[m][tt] = mfma16(wf.h[tt], kb2[m], P[m][tt]);
      if (st < 20) {
        const int n = st >> 2, pp = (st >> 1) & 1, mv = st & 1;
        const f32x4 v = n < 4 ? acc[4 + n][mv] : acc[8][mv];
        const int am = n < 4 ? a_v[n][pp] + mv * 2560 : (gg >= 2 ? a_v[n][pp] + mv * 2560 : L_ZERO + 124);
        *(LAS unsigned*)(lds + am) = pk2(v[2 * pp], v[2 * pp + 1]);
      }
      SBAR();
    }
    *(LAS unsigned*)(lds + a_one) = 0x00003F80u; *(LAS unsigned*)(lds + a_one + 2560) = 0x00003F80u;
    FSTAMP(18);
#pragma unroll
    for (int m = 0; m < 2; ++m) {
      unsigned ph[ISB ? 18 : 10];
      feature_map<ISB>(P[m], ph);
#pragma unroll
      for (int j = 0; j < NU; ++j)
        *(LAS u32x2*)(lds + (j < UMAIN ? a_prow + (a_pm ^ (j * 8)) : a_pl) + m * 16 * RP) = u32x2{ph[2 * j], ph[2 * j + 1]};
      FSTAMP(19 + m);
    }
  }
  STAMP(4);
  lds_arrive(lds, SY_PHI);
  lds_wait(lds, SY_PHI);
  STAMP(5);

  {
    bf16x8 al[4], la[4], b0[4], mu[4], b1[4];
    f32x4 d0 = zero4(), d1 = zero4(), d2 = zero4(), d3 = zero4(), d4 = zero4(), d5 = zero4();
    asm volatile("" : "+v"(d0), "+v"(d1), "+v"(d2), "+v"(d3), "+v"(d4), "+v"(d5));
    auto rd = [&](int ks) {
      al[ks] = lds_tr(lds, a_al + ks * 32 * 160, 16 * 160);
      b0[ks] = lds_tr(lds, a_b0 + ks * 32 * RP, 16 * RP);
      if constexpr (ISB) b1[ks] = lds_tr(lds, a_b1 + ks * 32 * RP, 16 * RP);
      la[ks] = lds_tr(lds, a_la + ks * 32 * 160, 16 * 160);
      mu[ks] = lds_tr(lds, a_mu + ks * 32 * RP, 16 * RP);
      SBAR();
    };
    auto mm = [&](int ks) {
      mfma_acc(d0, b0[ks], al[ks]);
      if constexpr (ISB) mfma_acc(d1, b1[ks], al[ks]);
      mfma_acc(d2, b0[ks], la[ks]);
      if constexpr (ISB) mfma_acc(d3, b1[ks], la[ks]);
      mfma_acc(d4, mu[ks], al[ks]); mfma_acc(d5, mu[ks], la[ks]);
      SBAR();
    };
    constexpr int RD = ISB ? 10 : 8;
    rd(0); rd(1);
    __builtin_amdgcn_s_waitcnt(0xC07F & ~0xF00 | (RD << 8)); SBAR(); mm(0); rd(2);
    __builtin_amdgcn_s_waitcnt(0xC07F & ~0xF00 | (RD << 8)); SBAR(); mm(1); rd(3);
    __builtin_amdgcn_s_waitcnt(0xC07F & ~0xF00 | (RD << 8)); SBAR(); mm(2);
    cwait_lgkm0(); SBAR(); mm(3);
    asm volatile("s_nop 7\n\ts_nop 3" : "+v"(d0), "+v"(d1), "+v"(d2), "+v"(d3), "+v"(d4), "+v"(d5));
    if constexpr (ISB) {
      unsigned x0 = pk2(d0[0], d0[1]), x1 = pk2(d0[2], d0[3]), y0 = pk2(d1[0], d1[1]), y1 = pk2(d1[2], d1[3]);
      swap16(x0, y0); swap16(x1, y1);
      st_sc1(u32x4{nosent(x0), nosent(x1), nosent(y0), nosent(y1)}, rws, o_p0);
      x0 = pk2(d2[0], d2[1]); x1 = pk2(d2[2], d2[3]); y0 = pk2(d3[0], d3[1]); y1 = pk2(d3[2], d3[3]);
      swap16(x0, y0); swap16(x1, y1);
      st_sc1(u32x4{nosent(x0), nosent(x1), nosent(y0), nosent(y1)}, rws, o_p1);
    } else {
      unsigned x0 = pk2(d0[0], d0[1]), x1 = pk2(d0[2], d0[3]), y0 = pk2(d2[0], d2[1]), y1 = pk2(d2[2], d2[3]);
      swap16(x0, y0); swap16(x1, y1);
      st_sc1(u32x4{nosent(x0), nosent(x1), nosent(y0), nosent(y1)}, rws, o_p0);
    }
    st_sc1(u32x4{nosent(pk2(d4[0], d4[1])), nosent(pk2(d4[2], d4[3])), 0u, 0u}, rws, o_mu);
    st_sc1(u32x4{nosent(pk2(d5[0], d5[1])), nosent(pk2(d5[2], d5[3])), 0u, 0u}, rws, o_ml);
    st_tag(rws, WS_H1 + (((prob * 8 + g) * 16 + t) * 4 + w) * 128, T, lane);
    lds_arrive(lds, SY_KVDONE);
  }
  __builtin_amdgcn_s_setprio(0);
  STAMP(6); CSTAMP(46);
  const rsrc_t rwo = mk_rsrc(ISB ? a.woB : a.woA, 576 * XROW);
  {
    STAMP(7);
    const rsrc_t rkv = mk_rsrc(ws + kvp_grp, 16 * KVP_STRIDE);
    constexpr int NG = (NCH + 15) / 16;
    const unsigned one = (i & 1) ? 0x3F800000u : 0x3F80u;
    const bf16x8 sel = frag_of((i >> 1) == 0 ? one : 0u, (i >> 1) == 1 ? one : 0u, (i >> 1) == 2 ? one : 0u, (i >> 1) == 3 ? one : 0u);
    const unsigned off0 = gg * KVP_STRIDE + w * KVHEAD + i * 16;
    const unsigned offl = 16 * (NG - 1) + i < NCH ? off0 + (NG - 1) * 256 : OOB;
    const int kvw = L_PHI + w * KVHEAD + i * 16 + gg * 8;
    unsigned pfo = 0u;
    if (PFO) {
      const int row = 18 * (blockIdx.x >> 3) + tid / 9, ln = tid - 9 * (tid / 9);
      asm volatile("buffer_load_dword %0, %1, %2, 0 offen" : "=v"(pfo) : "v"(tid < 162 ? row * XROW + ln * 128 : OOB), "s"(rwo) : "memory");
    }
    unsigned p0, p1;
    poll_tag(rws, lane < 16 ? WS_H1 + (((prob * 8 + g) * 16 + lane) * 4 + w) * 128 : OOB, Ts, 0xFFFFull, p0, p1);
    __builtin_amdgcn_s_setprio(2);
    STAMP(8);
    f32x4 sum[NG];
    {
      u32x4 v[NG][4];
#pragma unroll
      for (int cg = 0; cg < NG; ++cg) {
        const unsigned off = cg < NG - 1 ? off0 + cg * 256 : offl;
#pragma unroll
        for (int u = 0; u < 4; ++u)
          asm volatile("buffer_load_dwordx4 %0, %1, %2, %3 offen sc1" : "=v"(v[cg][u]) : "v"(off), "s"(rkv), "s"(4 * u * KVP_STRIDE) : "memory");
      }
      {
        const u32x4 s = {SENT, SENT, SENT, SENT};
#pragma unroll
        for (int k = 0; k < 5; ++k) {
          const int n = lane + 64 * k;
          st_sc1(s, rws, n < 288 ? obuf + (2 * w * 72 + 9 * g) * 256 + n * 16 + (n >= 144 ? (72 - 9) * 256 : 0) : OOB);
        }
        SBAR();
      }
      float chk = 0.f;
#pragma unroll
      for (int cg = 0; cg < NG; ++cg) {
        asm volatile("s_waitcnt vmcnt(%7)" : "+v"(v[cg][0]), "+v"(v[cg][1]), "+v"(v[cg][2]), "+v"(v[cg][3]), "+v"(p0), "+v"(p1), "+v"(pfo)
                     : "n"((NG - 1 - cg) * 4 + 5));
        SBAR();
        f32x4 d = zero4();
#pragma unroll
        for (int u = 0; u < 4; ++u) d = mfma(sel, __builtin_bit_cast(bf16x8, v[cg][u]), d);
        sum[cg] = d;
        chk += d[0];
      }
      FSTAMP(21);
      if (!__all(chk == chk)) {
        PCOUNT(22);
#pragma unroll 1
        for (int sp = 0; sp < SPIN_G; ++sp) {
          unsigned mx = 0u;
#pragma unroll 1
          for (int n = 0; n < NG * 4; ++n) {
            asm volatile("" ::: "memory");
            const u32x4 x = ld_sc1(rkv, (n >> 2) < NG - 1 ? off0 + (n >> 2) * 256 : offl, 4 * (n & 3) * KVP_STRIDE);
            mx = umax3(mx, x[0], x[1]); mx = umax3(mx, x[2], x[3]);
          }
          if (__all(mx != SENT)) break;
        }
#pragma unroll
        for (int cg = 0; cg < NG; ++cg) {
          f32x4 d = zero4();
#pragma unroll
          for (int u = 0; u < 4; ++u)
            d = mfma(sel, __builtin_bit_cast(bf16x8, ld_sc1(rkv, cg < NG - 1 ? off0 + cg * 256 : offl, 4 * u * KVP_STRIDE)), d);
          sum[cg] = d;
        }
      }
    }
    lds_wait(lds, SY_KVDONE);
    if (gg < 2) {
#pragma unroll
      for (int cg = 0; cg < NG; ++cg)
        if (cg < NG - 1 || 16 * cg + i < NCH)
          *(LAS u32x2*)(lds + kvw + cg * 256) = u32x2{pk2(sum[cg][0], sum[cg][1]), pk2(sum[cg][2], sum[cg][3])};
    }
    lds_arrive(lds, SY_KVIMG);
    wait_vm<0>();
    lds_arrive(lds, SY_PRE);
  }
  STAMP(9);
  __builtin_amdgcn_s_setprio(0);
  STAMP(10);

  {
    const rsrc_t ro = mk_rsrc(ws + obuf, O_STRIDE);
    const rsrc_t rout = mk_rsrc(a.out, 2048 * OUTROW);
    const unsigned orow = (t * 128 + 32 * w + i) * OUTROW + prob * 1152 + 144 * g;
    const unsigned o_pair = orow + (2 * (gg & 1) + (gg >> 1)) * 16, o_last = gg < 2 ? orow + gg * 16 * OUTROW + 128 : OOB;
    const int wl = (4 * gg + (i >> 2)) * 160 + (i & 3) * 8;
    const int wa2 = opaque(L_WQ + wl), wa3 = opaque(L_WQ + wl + 12 * 5120);
    lds_wait(lds, SY_WO);
    unsigned p0, p1;
    poll_tag(rws, lane < 8 ? WS_H2 + (((prob * 16 + t) * 8 + lane) * 4 + w) * 128 : OOB, Ts, 0xFFull, p0, p1);
    __builtin_amdgcn_s_setprio(2);
    STAMP(11);
    f32x4 oc[5][2];
#pragma unroll 1
    for (int pass = 0; pass < 2; ++pass) {
      asm volatile("" ::: "memory");
      u32x4 of[18][2];
#pragma unroll
      for (int ks = 0; ks < 18; ++ks) {
#pragma unroll
        for (int m = 0; m < 2; ++m) { of[ks][m] = ld_sc1(ro, lane * 16, ((2 * w + m) * 72 + 4 * ks) * 256); SBAR(); }
      }
      bf16x8 wq[2][5];
#pragma unroll
      for (int n = 0; n < 5; ++n) {
        wq[0][n] = lds_tr(lds, wa2 + n * 32, 16 * 160);
        oc[n][0] = zero4(); oc[n][1] = zero4();
        asm volatile("" : "+v"(oc[n][0]), "+v"(oc[n][1]));
      }
      SBAR();
#define OUT_STEP(ks) do { cwait_vm_lgkm4<34 - 2 * (ks)>(); SBAR(); \
      step5(lds, oc, wq[(ks) & 1], wq[((ks) + 1) & 1], __builtin_bit_cast(bf16x8, of[ks][0]), __builtin_bit_cast(bf16x8, of[ks][1]), \
            (ks) + 1 < 12 ? wa2 + ((ks) + 1) * 5120 : wa3 + ((ks) + 1 - 12) * 5120, (ks) < 17); } while (0)
      OUT_STEP(0);
      asm volatile("" :: "v"(p0), "v"(p1));
      OUT_STEP(1); OUT_STEP(2); OUT_STEP(3); OUT_STEP(4); OUT_STEP(5); OUT_STEP(6); OUT_STEP(7); OUT_STEP(8);
      OUT_STEP(9); OUT_STEP(10); OUT_STEP(11); OUT_STEP(12); OUT_STEP(13); OUT_STEP(14); OUT_STEP(15); OUT_STEP(16); OUT_STEP(17);
#undef OUT_STEP
      mfma_fence(oc);
      const float chk = oc[0][0][0] + oc[0][1][0];
      if (pass == 1 || __all(chk == chk)) break;
      PCOUNT(23);
#pragma unroll 1
      for (int sp = 0; sp < SPIN_G; ++sp) {
        unsigned mx = 0u;
#pragma unroll 1
        for (int n = 0; n < 36; ++n) {
          asm volatile("" ::: "memory");
          const u32x4 x = ld_sc1(ro, lane * 16, ((2 * w + (n & 1)) * 72 + 4 * (n >> 1)) * 256);
          mx = umax3(mx, x[0], x[1]); mx = umax3(mx, x[2], x[3]);
        }
        if (__all(mx != SENT)) break;
      }
    }
    STAMP(12);
#pragma unroll
    for (int m = 0; m < 2; ++m) {
#pragma unroll
      for (int n = 0; n < 4; n += 2) {
        unsigned x0 = pk2(oc[n][m][0], oc[n][m][1]), x1 = pk2(oc[n][m][2], oc[n][m][3]);
        unsigned y0 = pk2(oc[n + 1][m][0], oc[n + 1][m][1]), y1 = pk2(oc[n + 1][m][2], oc[n + 1][m][3]);
        swap16(x0, y0); swap16(x1, y1);
        __builtin_amdgcn_raw_buffer_store_b128(u32x4{x0, x1, y0, y1}, rout, o_pair + m * 16 * OUTROW + n * 32, 0, 0);
      }
    }
    unsigned x0 = pk2(oc[4][0][0], oc[4][0][1]), x1 = pk2(oc[4][0][2], oc[4][0][3]);
    unsigned y0 = pk2(oc[4][1][0], oc[4][1][1]), y1 = pk2(oc[4][1][2], oc[4][1][3]);
    swap16(x0, y0); swap16(x1, y1);
    __builtin_amdgcn_raw_buffer_store_b128(u32x4{x0, x1, y0, y1}, rout, o_last, 0, 0);
    if (t == 0 && g == 0 && tid == 0) __hip_atomic_store((GAS unsigned*)(ws + WS_E + prob * 256), T, RLX_AGENT);
  }
  STAMP(13); CSTAMP(47);
  STAMP_OUT(w);
}

template <bool ISB> DI void h_path(const Args& a, LAS char* lds, const int w, const int lane, const int t, const int g) {
  constexpr int prob = ISB ? 1 : 0;
  constexpr int C5 = ISB ? 5 : 3;
  constexpr int KVROW = C5 * 16, KVHEAD = ISB ? 1536 : 1024;
  const int i = lane & 15, gg = lane >> 4;
  const rsrc_t rx = mk_rsrc((const char*)(ISB ? a.xB : a.xA) + (size_t)t * 128 * XROW, 128 * XROW);
  const rsrc_t rw = mk_rsrc(ISB ? a.wB : a.wA, 576 * WROW);
  const unsigned lb = (unsigned)(size_t)lds;
  STAMP_DECL
  STAMP(0);
  const int rot = ROT == 1 ? (g + 2 * (t & 3) + ((t >> 1) & 1)) % 9 : ROT == 2 ? (g + 8 * (t & 3)) % 9 : 0;
  auto kchunk = [&](int c) { const int k = c + rot; return k >= 9 ? k - 9 : k; };
  unsigned vx[2], vw[5], vq[8];
#pragma unroll
  for (int par = 0; par < 2; ++par) vx[par] = (lane >> 3) * XROW + (((lane & 7) ^ ((4 * par + (lane >> 4)) & 7)) * 16);
  auto issue_x = [&](int c) {
    const unsigned sb = lb + ((c + 3) & 3) * SLOT;
#pragma unroll
    for (int jj = 0; jj < 4; ++jj) dma16(rx, vx[jj & 1], (4 * w + jj) * 8 * XROW + kchunk(c) * 128, sb + (4 * w + jj) * 1024);
  };
  issue_x(0);
#pragma unroll
  for (int n = 0; n < 5; ++n) {
    const int nn = (w + 4 * n) * 64 + lane, r64 = nn / 18, cp = nn - 18 * r64;
    const int co = cp < 9 ? 1152 + 16 * cp : 2304 + 16 * (cp - 9);
    vw[n] = ((r64 & 32) + krow(r64 & 31)) * WROW + 144 * g + co;
  }
  auto issue_w = [&](int c) {
    const unsigned sb = lb + ((c + 3) & 3) * SLOT;
#pragma unroll
    for (int n = 0; n < 5; ++n)
      if (n < 4 || w < 2) dma16(rw, vw[n], kchunk(c) * 64 * WROW, sb + WPART + (w + 4 * n) * 1024);
  };
  issue_w(0); issue_x(1); issue_w(1);
  STAMP(1);
  if (w < 2) wait_vm<9>(); else wait_vm<8>();
  STAMP(2);
  barrier();
  issue_x(2); issue_w(2);
#pragma unroll
  for (int n = 0; n < 8; ++n) {
    const int v = w + 4 * n, ks = v / 5, nn = (v - 5 * ks) * 64 + lane, rho = nn / 10, cp = nn - 10 * rho;
    vq[n] = ((ks & 1) * 32 + krow(rho)) * WROW + 144 * g + 16 * min(cp, 8);
  }
  auto issue_wq = [&](int grp) {
#pragma unroll
    for (int n = 0; n < 8; ++n)
      if (n < 7 || w < 2)
        dma16(rw, vq[n], kchunk(3 * grp + ((w + 4 * n) / 5 >> 1)) * 64 * WROW, lb + L_WQ + grp * 30720 + (w + 4 * n) * 1024);
  };
  bf16x8 X[18][2];
  const int xa0 = (32 * w + i) * 128 + ((gg ^ ((i >> 1) & 7)) * 16), xa1 = xa0 ^ 64;
#pragma unroll
  for (int c = 0; c < 9; ++c) {
    const int base = ((c + 3) & 3) * SLOT;
    const int xo0 = opaque(xa0 + base), xo1 = opaque(xa1 + base);
    X[2 * c][0] = lds128(lds, xo0); X[2 * c][1] = lds128(lds, xo0 + 2048);
    X[2 * c + 1][0] = lds128(lds, xo1); X[2 * c + 1][1] = lds128(lds, xo1 + 2048);
    if (c <= 5) { issue_x(c + 3); issue_w(c + 3); }
    LSTAMP(16 + c);
    if (c <= 5) { if (w < 2) wait_vm<18>(); else wait_vm<16>(); }
    if (c == 6) { if (w < 2) wait_vm<9>(); else wait_vm<8>(); }
    if (c == 7) wait_vm<0>();
    LSTAMP(25 + c);
    if (c < 8) barrier(); else lds_arrive(lds, SY_KV8);
    LSTAMP(34 + c);
  }
  issue_wq(0); issue_wq(1); issue_wq(2);
  __builtin_amdgcn_s_setprio(1);
  STAMP(3);
  f32x4 qa[5][2];
#pragma unroll
  for (int n = 0; n < 5; ++n) { qa[n][0] = zero4(); qa[n][1] = zero4(); asm volatile("" : "+v"(qa[n][0]), "+v"(qa[n][1])); }
  const int wa2 = opaque(L_WQ + (4 * gg + (i >> 2)) * 160 + (i & 3) * 8), wa3 = opaque(wa2 + 12 * 5120);
  bf16x8 wq[2][5];
  if (w < 2) wait_vm<16>(); else wait_vm<14>();
  lds_arrive(lds, SY_WQ0);
  lds_wait(lds, SY_WQ0);
  STAMP(4);
#pragma unroll
  for (int n = 0; n < 5; ++n) wq[0][n] = lds_tr(lds, wa2 + n * 32, 16 * 160);
#pragma unroll
  for (int ks = 0; ks < 18; ++ks) {
    cwait_lgkm<4>();
    if (ks == 5) { if (w < 2) wait_vm<8>(); else wait_vm<7>(); lds_arrive(lds, SY_WQ1); lds_wait(lds, SY_WQ1); STAMP(5); }
    if (ks == 11) { wait_vm<0>(); lds_arrive(lds, SY_WQ2); lds_wait(lds, SY_WQ2); STAMP(6); }
    SBAR();
    step5(lds, qa, wq[ks & 1], wq[(ks + 1) & 1], X[ks][0], X[ks][1], ks + 1 < 12 ? wa2 + (ks + 1) * 5120 : wa3 + (ks + 1 - 12) * 5120, ks < 17);
  }
  WF wf;
  {
    const int b0 = opaque(L_WF + lane * 16), b1 = opaque(L_WF + 10240 + lane * 8);
#pragma unroll
    for (int n = 0; n < 5; ++n) load_wf_tile(lds, b0, b1, wf, n);
  }
  mfma_fence(qa);
  lds_arrive(lds, SY_QDONE);
  STAMP(7);
  const unsigned T = *(LAS unsigned*)(lds + L_SYNC + SY_TAG * 4);
  unsigned ph[2][ISB ? 18 : 10];
  {
    f32x4 P[2][5];
    logits2(wf, qa[0], qa[1], qa[2], qa[3], qa[4], P);
    feature_map<ISB>(P[0], ph[0]);
    feature_map<ISB>(P[1], ph[1]);
#pragma unroll
    for (int j = 0; j < (ISB ? 18 : 10); ++j) asm volatile("" : "+v"(ph[0][j]), "+v"(ph[1][j]));
  }
  int a_kv[5], a_st[9];
  {
    const bool own = (i >> 2) == gg;
#pragma unroll
    for (int n = 0; n < 5; ++n) {
      const int e = 4 * n + (i & 3);
      a_kv[n] = opaque((own && e < 19) ? L_PHI + gg * KVHEAD + e * KVROW : L_ZERO);
    }
#pragma unroll
    for (int pp = 0; pp < 9; ++pp) {
      const int cp = 9 * gg + pp;
      a_st[pp] = opaque(L_V + w * 4608 + (cp >> 2) * 256 + i * 16 + (cp & 3) * 4);
    }
  }
  const rsrc_t ro = mk_rsrc(a.ws + WS_O + (prob * 16 + t) * O_STRIDE, O_STRIDE);
  const unsigned o_dst = (2 * w * 72 + 9 * g) * 256 + lane * 16;
  const rsrc_t rwo = mk_rsrc(ISB ? a.woB : a.woA, 576 * XROW);
  unsigned vo[5];
#pragma unroll
  for (int n = 0; n < 5; ++n) {
    const int nn = ((w + 4 * n) % 5) * 64 + lane, rho = nn / 10, cp = nn - 10 * rho;
    vo[n] = (unsigned)opaque(krow(rho) * XROW + 144 * g + 16 * min(cp, 8));
  }
  lds_wait(lds, SY_QDONE);
#pragma unroll
  for (int n = 0; n < WO_FIRST; ++n) { const int v = w + 4 * n; dma16(rwo, vo[n % 5], (v / 5) * 32 * XROW, lb + L_WQ + v * 1024); }
  STAMP(8);
  lds_wait(lds, SY_KVIMG);
  __builtin_amdgcn_s_setprio(3);
  STAMP(9);
  f32x4 O[5][2];
  {
    bf16x8 af[5][C5];
#pragma unroll
    for (int s = 0; s < C5; ++s) {
#pragma unroll
      for (int n = 0; n < 5; ++n) af[n][s] = lds128(lds, a_kv[n] + s * 16);
    }
    SBAR();
    constexpr int NP = ISB ? 18 : 10;
#pragma unroll
    for (int s = 0; s < C5; ++s) {
      bf16x8 bq[2];
#pragma unroll
      for (int m = 0; m < 2; ++m) {
        unsigned u[4];
#pragma unroll
        for (int j = 0; j < 4; ++j) u[j] = 4 * s + j < NP ? ph[m][4 * s + j < NP ? 4 * s + j : 0] : 0u;
        bq[m] = frag_of(u[0], u[1], u[2], u[3]);
      }
#pragma unroll
      for (int n = 4; n >= 0; --n) {
#pragma unroll
        for (int m = 0; m < 2; ++m) O[n][m] = mfma(af[n][s], bq[m], s == 0 ? zero4() : O[n][m]);
      }
    }
  }
  STAMP(10);
#pragma unroll
  for (int m = 0; m < 2; ++m) {
    const float rc = __builtin_amdgcn_rcpf(O[4][m][2] + 1e-6f);
    const f32x2 rc2 = {rc, rc};
#pragma unroll
    for (int pp = 0; pp < 9; ++pp) {
      const int e = 2 * pp;
      const f32x2 y = f32x2{O[e >> 2][m][e & 3], O[(e + 1) >> 2][m][(e + 1) & 3]} * rc2;
      *(LAS unsigned*)(lds + a_st[pp] + m * 2304) = nosent(pk2(y[0], y[1]));
    }
  }
  lds_wait(lds, SY_PRE);
  {
    const int sb = L_V + w * 4608 + lane * 16;
    u32x4 v[5];
#pragma unroll
    for (int k = 0; k < 5; ++k) v[k] = *(LAS u32x4*)(lds + sb + (k < 4 || lane < 32 ? k * 1024 : 0));
#pragma unroll
    for (int k = 0; k < 5; ++k) {
      const int n = lane + 64 * k;
      st_sc1(v[k], ro, n < 288 ? o_dst + k * 1024 + (n >= 144 ? (72 - 9) * 256 : 0) : OOB);
    }
    st_tag(mk_rsrc(a.ws, WS_USED), WS_H2 + (((prob * 16 + t) * 8 + g) * 4 + w) * 128, T, lane);
  }
  STAMP(11);
#pragma unroll
  for (int n = WO_FIRST; n < 23; ++n) {
    const int v = w + 4 * n;
    if (n < 22 || w < 2) dma16(rwo, vo[n % 5], (v / 5) * 32 * XROW, lb + L_WQ + v * 1024);
  }
  wait_vm<0>();
  lds_arrive(lds, SY_WO);
  STAMP(12);
  STAMP_OUT(4 + w);
}

__global__ __launch_bounds__(512, 2) void hedgehog_fused(Args a) {
  extern __shared__ __attribute__((aligned(16))) char smem[];
  LAS char* lds = (LAS char*)smem;
  const int b = blockIdx.x, xcd = b & 7, r = b >> 3;
  const int prob = xcd >> 2, t = (xcd & 3) * 4 + (r >> 3), g = r & 7;
  const int wave = __builtin_amdgcn_readfirstlane(threadIdx.x >> 6), lane = threadIdx.x & 63;
  if (wave < 4) {
    if (prob) c_path<true>(a, lds, wave, lane, t, g); else c_path<false>(a, lds, wave, lane, t, g);
  } else {
    if (prob) h_path<true>(a, lds, wave - 4, lane, t, g); else h_path<false>(a, lds, wave - 4, lane, t, g);
  }
}

}

extern "C" void kernel_launch(void* const* d_in, const int* in_sizes, int n_in, void* d_out, int out_size, void* d_ws,
                              size_t ws_size, hipStream_t stream) {
  (void)in_sizes; (void)n_in; (void)out_size; (void)ws_size;
  Args a;
  a.xA = d_in[0]; a.wA = d_in[1]; a.woA = d_in[2]; a.xB = d_in[3]; a.wB = d_in[4]; a.wphi = d_in[5]; a.woB = d_in[6];
  a.out = d_out; a.ws = (char*)d_ws;
  (void)hipFuncSetAttribute((const void*)hedgehog_fused, hipFuncAttributeMaxDynamicSharedMemorySize, LDS_BYTES);
  hedgehog_fused<<<256, 512, LDS_BYTES, stream>>>(a);
}
```

```cpp
#include <hip/hip_runtime.h>
#include <hip/hip_bf16.h>
#include <stdint.h>
#include <type_traits>

typedef __bf16 bf16x8 __attribute__((ext_vector_type(8)));
typedef __bf16 bf16x4 __attribute__((ext_vector_type(4)));
typedef __bf16 bf16x2 __attribute__((ext_vector_type(2)));
typedef short s16x4 __attribute__((ext_vector_type(4)));
typedef float f32x4 __attribute__((ext_vector_type(4)));
typedef float f32x2 __attribute__((ext_vector_type(2)));
typedef unsigned u32x4 __attribute__((ext_vector_type(4)));
typedef unsigned u32x2 __attribute__((ext_vector_type(2)));
#define LAS __attribute__((address_space(3)))
#define GAS __attribute__((address_space(1)))
#define DI __device__ __forceinline__
#define RLX_AGENT __ATOMIC_RELAXED, __HIP_MEMORY_SCOPE_AGENT
#define SBAR() __builtin_amdgcn_sched_barrier(0)
typedef __amdgpu_buffer_rsrc_t rsrc_t;

#define DBG 0
#define PROF 0
#define STAMP(n)
#define CSTAMP(n)
#define STAMP_DECL
#define STAMP_OUT(wv)
#define PCOUNT(n)
#define LSTAMP(n)
#define FSTAMP(n)
#define PFQ 0
#define PFO 1
#define WO_FIRST 11
#define TLBW 0
#define PRO 0
#define CPF 0
#define HFIRST 0
#define KV_UNROLL 1
#define ROT 1

namespace {

constexpr int XROW = 1152;
constexpr int WROW = 3456;
constexpr int OUTROW = 2304;
constexpr int SLOT = 34816, WPART = 16384;
constexpr int L_WQ = 0;
constexpr int L_PHI = 92160;
constexpr int L_V = 129024;
constexpr int L_WF = 149504;
constexpr int L_SYNC = 162304;
constexpr int L_ZERO = 162560;
constexpr int LDS_BYTES = 163840;
enum { SY_WQ0 = 0, SY_WQ1, SY_WQ2, SY_QDONE, SY_PHI, SY_KVDONE, SY_KVIMG, SY_WO, SY_KV8, SY_PRE, SY_TAG = 63 };
constexpr int WS_E = 0, WS_KVP = 65536, KVP_STRIDE = 6144, WS_O = 2097152, O_STRIDE = 147456;
constexpr int WS_H1 = WS_O + 32 * O_STRIDE, WS_H2 = WS_H1 + 131072;
constexpr int WS_USED = WS_H2 + 131072;
constexpr unsigned SENT = 0xFFFFFFFFu;
constexpr unsigned OOB = 0x7FFF0000u;
constexpr int SPIN_G = 1 << 18, SPIN_L = 1 << 22;
constexpr float LOG2E = 1.4426950408889634f;

struct Args {
  const void* xA; const void* wA; const void* woA; const void* xB; const void* wB; const void* wphi; const void* woB;
  void* out; char* ws;
};

DI f32x4 mfma(bf16x8 a, bf16x8 b, f32x4 c) { return __builtin_amdgcn_mfma_f32_16x16x32_bf16(a, b, c, 0, 0, 0); }
DI f32x4 mfma16(u32x2 a, u32x2 b, f32x4 c) {
  return __builtin_amdgcn_mfma_f32_16x16x16bf16_1k(__builtin_bit_cast(s16x4, a), __builtin_bit_cast(s16x4, b), c, 0, 0, 0);
}
DI void mfma_acc(f32x4& c, bf16x8 a, bf16x8 b) { asm volatile("v_mfma_f32_16x16x32_bf16 %0, %1, %2, %0" : "+v"(c) : "v"(a), "v"(b)); }
template <int N> DI void mfma_fence(f32x4 (&acc)[N][2]) {
  asm volatile("s_nop 7\n\ts_nop 3" : "+v"(acc[N - 1][0]), "+v"(acc[N - 1][1]) :: "memory");
#pragma unroll
  for (int n = 0; n < N - 1; ++n) asm volatile("" : "+v"(acc[n][0]), "+v"(acc[n][1]));
}
DI unsigned pk2(float a, float b) { f32x2 p = {a, b}; return __builtin_bit_cast(unsigned, __builtin_convertvector(p, bf16x2)); }
DI bf16x8 frag_of(unsigned a, unsigned b, unsigned c, unsigned d) { u32x4 v = {a, b, c, d}; return __builtin_bit_cast(bf16x8, v); }
DI bf16x8 pack_pair(f32x4 lo, f32x4 hi) { return frag_of(pk2(lo[0], lo[1]), pk2(lo[2], lo[3]), pk2(hi[0], hi[1]), pk2(hi[2], hi[3])); }
DI bf16x8 lds128(LAS char* l, int off) { return *(LAS bf16x8*)(l + off); }
DI bf16x8 lds_tr(LAS char* l, int off, int second) {
  bf16x4 a = __builtin_amdgcn_ds_read_tr16_b64_v4bf16((LAS bf16x4*)(l + off));
  bf16x4 b = __builtin_amdgcn_ds_read_tr16_b64_v4bf16((LAS bf16x4*)(l + off + second));
  return __builtin_shufflevector(a, b, 0, 1, 2, 3, 4, 5, 6, 7);
}
DI void dma16(rsrc_t r, unsigned voff, unsigned soff, unsigned ldsaddr) {
  asm volatile("s_mov_b32 m0, %0\n\ts_nop 0\n\tbuffer_load_dwordx4 %1, %2, %3 offen lds" ::"s"(ldsaddr), "v"(voff), "s"(r), "s"(soff) : "memory");
}
template <int N> DI void wait_vm() { asm volatile("s_waitcnt vmcnt(%0)" ::"n"(N) : "memory"); }
DI void cwait_lgkm0() { __builtin_amdgcn_s_waitcnt(0xC07F); }
template <int N> DI void cwait_vm_lgkm4() { __builtin_amdgcn_s_waitcnt((N & 15) | 0x70 | (4 << 8) | ((N >> 4) << 14)); }
DI void barrier() { asm volatile("s_waitcnt lgkmcnt(0)\n\ts_barrier" ::: "memory"); }
DI void lds_arrive(LAS char* l, int id) {
  const unsigned addr = (unsigned)(size_t)l + L_SYNC + id * 4, one = 1u;
  unsigned long long save;
  asm volatile("s_waitcnt lgkmcnt(0)\n\ts_mov_b64 %0, exec\n\ts_mov_b64 exec, 1\n\tds_add_u32 %1, %2\n\ts_mov_b64 exec, %0"
               : "=&s"(save) : "v"(addr), "v"(one) : "memory");
}
DI void lds_wait(LAS char* l, int id, unsigned n = 4u) {
  const unsigned addr = (unsigned)(size_t)l + L_SYNC + id * 4;
#pragma unroll 1
  for (int sp = 0; sp < SPIN_L; ++sp) {
    unsigned v;
    asm volatile("ds_read_b32 %0, %1\n\ts_waitcnt lgkmcnt(0)" : "=v"(v) : "v"(addr) : "memory");
    if ((unsigned)__builtin_amdgcn_readfirstlane(v) >= n) break;
    __builtin_amdgcn_s_sleep(1);
  }
}
DI rsrc_t mk_rsrc(const void* p, int bytes) { return __builtin_amdgcn_make_buffer_rsrc((void*)p, 0, bytes, 0x00020000); }
DI void st_sc1(u32x4 v, rsrc_t r, unsigned off) { __builtin_amdgcn_raw_buffer_store_b128(v, r, off, 0, 16); }
DI u32x4 ld_sc1(rsrc_t r, unsigned off, unsigned soff) { return __builtin_amdgcn_raw_buffer_load_b128(r, off, soff, 16); }
DI int opaque(int x) { asm volatile("" : "+v"(x)); return x; }
DI unsigned umax3(unsigned a, unsigned b, unsigned c) { return max(max(a, b), c); }
DI unsigned nosent(unsigned x) { return min(x, 0xFFFFFFFEu); }
DI void swap16(unsigned& a, unsigned& b) { auto r = __builtin_amdgcn_permlane16_swap(a, b, false, false); a = r[0]; b = r[1]; }
DI f32x4 zero4() { return f32x4{0.f, 0.f, 0.f, 0.f}; }
DI float fmax3(float a, float b, float c) { return fmaxf(fmaxf(a, b), c); }
DI float fmin3(float a, float b, float c) { return fminf(fminf(a, b), c); }
DI void poll_tag(rsrc_t r, unsigned off, unsigned tag, unsigned long long mask, unsigned& p0, unsigned& p1) {
  unsigned cnt = SPIN_G;
  asm volatile(
      "buffer_load_dword %0, %3, %4, 0 offen sc1\n"
      "L_poll_%=:\n\t"
      "buffer_load_dword %1, %3, %4, 0 offen sc1\n\t"
      "s_waitcnt vmcnt(1)\n\t"
      "v_cmp_ne_u32 vcc, %5, %0\n\t"
      "s_nop 1\n\t"
      "s_and_b64 vcc, vcc, %6\n\t"
      "s_cbranch_vccz L_done_%=\n\t"
      "buffer_load_dword %0, %3, %4, 0 offen sc1\n\t"
      "s_waitcnt vmcnt(1)\n\t"
      "v_cmp_ne_u32 vcc, %5, %1\n\t"
      "s_nop 1\n\t"
      "s_and_b64 vcc, vcc, %6\n\t"
      "s_cbranch_vccz L_done_%=\n\t"
      "s_sub_u32 %2, %2, 1\n\t"
      "s_cmp_lg_u32 %2, 0\n\t"
      "s_cbranch_scc1 L_poll_%=\n"
      "L_done_%=:"
      : "=&v"(p0), "=&v"(p1), "+s"(cnt) : "v"(off), "s"(r), "s"(tag), "s"(mask) : "vcc", "scc", "memory");
}
DI void st_tag(rsrc_t r, unsigned line, unsigned tag, int lane) { st_sc1(u32x4{tag, tag, tag, tag}, r, lane < 8 ? line + lane * 16 : OOB); }

DI int krow(int rho) { return 8 * ((rho >> 2) & 3) + 4 * (rho >> 4) + (rho & 3); }

template <bool ISB> DI void feature_map(const f32x4 (&P)[5], unsigned (&ph)[ISB ? 18 : 10]) {
  float p[18];
#pragma unroll
  for (int f = 0; f < 18; ++f) p[f] = P[f >> 2][f & 3];
  if constexpr (ISB) {
    float mx = fmax3(p[0], p[1], p[2]), mn = fmin3(p[0], p[1], p[2]);
#pragma unroll
    for (int f = 3; f < 17; f += 2) { mx = fmax3(mx, p[f], p[f + 1]); mn = fmin3(mn, p[f], p[f + 1]); }
    mx = fmax3(mx, p[17], p[17]); mn = fmin3(mn, p[17], p[17]);
    const f32x2 cp = {-mx * LOG2E, -mx * LOG2E}, cn = {mn * LOG2E, mn * LOG2E};
    f32x2 lp = {LOG2E, LOG2E}, ln = {-LOG2E, -LOG2E};
    asm("" : "+v"(lp), "+v"(ln));
    f32x2 ep[9], en[9];
#pragma unroll
    for (int f = 0; f < 9; ++f) {
      const f32x2 x = {p[2 * f], p[2 * f + 1]};
      const f32x2 a = __builtin_elementwise_fma(x, lp, cp), b = __builtin_elementwise_fma(x, ln, cn);
      ep[f] = f32x2{__builtin_amdgcn_exp2f(a[0]), __builtin_amdgcn_exp2f(a[1])};
      en[f] = f32x2{__builtin_amdgcn_exp2f(b[0]), __builtin_amdgcn_exp2f(b[1])};
    }
    f32x2 sp = ep[0], sn = en[0];
#pragma unroll
    for (int f = 1; f < 9; ++f) { sp += ep[f]; sn += en[f]; }
    const float rp = __builtin_amdgcn_rcpf(sp[0] + sp[1]), rn = __builtin_amdgcn_rcpf(sn[0] + sn[1]);
#pragma unroll
    for (int f = 0; f < 9; ++f) {
      const f32x2 a = ep[f] * rp, b = en[f] * rn;
      ph[f] = pk2(a[0], a[1]);
      ph[9 + f] = pk2(b[0], b[1]);
    }
  } else {
    f32x2 l2 = {LOG2E, LOG2E}, eps = {1e-6f, 1e-6f};
    asm("" : "+v"(l2), "+v"(eps));
#pragma unroll
    for (int f = 0; f < 9; ++f) {
      const f32x2 lo = f32x2{fminf(p[2 * f], 0.f), fminf(p[2 * f + 1], 0.f)} * l2;
      const f32x2 hi = f32x2{fmaxf(p[2 * f], 0.f), fmaxf(p[2 * f + 1], 0.f)};
      const f32x2 y = (hi + f32x2{__builtin_amdgcn_exp2f(lo[0]), __builtin_amdgcn_exp2f(lo[1])}) + eps;
      ph[f] = pk2(y[0], y[1]);
    }
    ph[9] = 0u;
  }
}

struct WF { bf16x8 a[5][2]; u32x2 h[5]; };
DI void load_wf_tile(LAS char* lds, int b0, int b1, WF& f, int t) {
  f.a[t][0] = lds128(lds, b0 + (2 * t) * 1024);
  f.a[t][1] = lds128(lds, b0 + (2 * t + 1) * 1024);
  f.h[t] = *(LAS u32x2*)(lds + b1 + t * 512);
}
DI void logits2(const WF& f, const f32x4 (&t0)[2], const f32x4 (&t1)[2], const f32x4 (&t2)[2], const f32x4 (&t3)[2],
                const f32x4 (&t4)[2], f32x4 (&P)[2][5]) {
#pragma unroll
  for (int m = 0; m < 2; ++m) {
    const bf16x8 b0 = pack_pair(t0[m], t1[m]), b1 = pack_pair(t2[m], t3[m]);
    const u32x2 b2 = u32x2{pk2(t4[m][0], t4[m][1]), pk2(t4[m][2], t4[m][3])};
#pragma unroll
    for (int t = 0; t < 5; ++t) P[m][t] = mfma(f.a[t][0], b0, zero4());
#pragma unroll
    for (int t = 0; t < 5; ++t) P[m][t] = mfma(f.a[t][1], b1, P[m][t]);
#pragma unroll
    for (int t = 0; t < 5; ++t) P[m][t] = mfma16(f.h[t], b2, P[m][t]);
  }
}
DI int wf_offset(int h, int d, int f) {
  const int col = 18 * h + d, s = col >> 5, ln = 16 * ((col >> 2) & 3) + 4 * h + (f & 3), tt = f >> 2;
  return s < 2 ? (tt * 2 + s) * 1024 + ln * 16 + (((col >> 4) & 1) * 4 + (col & 3)) * 2 : 10240 + tt * 512 + ln * 8 + (col & 3) * 2;
}
template <int N> DI void cwait_lgkm() { __builtin_amdgcn_s_waitcnt(0xC07F | (N << 8)); }
DI void step5(LAS char* lds, f32x4 (&acc)[5][2], const bf16x8 (&wc)[5], bf16x8 (&wn)[5], bf16x8 x0, bf16x8 x1, int next, bool more) {
#pragma unroll
  for (int j = 0; j < 10; ++j) {
    if (j == 6) { if (more) cwait_lgkm<10>(); else cwait_lgkm<0>(); }
    mfma_acc(acc[j >> 1][j & 1], wc[j >> 1], (j & 1) ? x1 : x0);
    if (more && j < 5) wn[j] = lds_tr(lds, next + j * 32, 16 * 160);
    SBAR();
  }
}

template <bool ISB> DI void c_path(const Args& a, LAS char* lds, const int w, const int lane0, const int t, const int g) {
  constexpr int prob = ISB ? 1 : 0;
  constexpr int RP = ISB ? 288 : 160;
  constexpr int C5 = ISB ? 5 : 3;
  constexpr int KVROW = C5 * 16, KVHEAD = ISB ? 1536 : 1024, NCH = 19 * C5;
  char* ws = a.ws;
  const rsrc_t rws = mk_rsrc(ws, WS_USED);
  const unsigned kvp_grp = WS_KVP + (prob * 8 + g) * 16 * KVP_STRIDE;
  const unsigned obuf = WS_O + (prob * 16 + t) * O_STRIDE;
  constexpr int NU = ISB ? 9 : 5, UMAIN = NU - 1;
  f32x4 acc[9][2];
  WF wf;
  unsigned T;
  int a_v[5][2], a_one, a_prow, a_pm, a_pl, a_b0, a_b1, a_mu, a_al, a_la;
  unsigned o_p0, o_p1, o_mu, o_ml;
  STAMP_DECL
  {
    const int lane = lane0, tid = w * 64 + lane, i = lane & 15, gg = lane >> 4;
    STAMP(0); CSTAMP(43);
    T = __hip_atomic_load((GAS unsigned*)(ws + WS_E + prob * 256), RLX_AGENT) + 1u;
    unsigned tl0 = 0u, tl1 = 0u, tl2 = 0u;
    if (TLBW && w == 3) {
      const unsigned wsoff = lane < 3 ? obuf + lane * 65536 : lane < 5 ? kvp_grp + (lane - 3) * 65536
                           : lane == 5 ? WS_H1 + (prob * 8 + g) * 8192 : lane == 6 ? WS_H2 + (prob * 16 + t) * 4096 : OOB;
      tl0 = __builtin_amdgcn_raw_buffer_load_b32(rws, wsoff, 0, 0);
      tl1 = __builtin_amdgcn_raw_buffer_load_b32(mk_rsrc(a.out, 2048 * OUTROW), lane < 6 ? t * 128 * OUTROW + min(lane * 65536, 128 * OUTROW - 4) : OOB, 0, 0);
      tl2 = lane < 32 ? __builtin_amdgcn_raw_buffer_load_b32(mk_rsrc(ISB ? a.wB : a.wA, 576 * WROW), lane * 65536, 0, 0)
                      : __builtin_amdgcn_raw_buffer_load_b32(mk_rsrc(ISB ? a.woB : a.woA, 576 * XROW), (lane - 32) * 65536, 0, 0);
    }
    unsigned short wel[6];
    if constexpr (ISB) {
      const rsrc_t rp = mk_rsrc(a.wphi, 32 * 324 * 2);
#pragma unroll
      for (int k = 0; k < 6; ++k) {
        const int idx = lane + 64 * k;
        wel[k] = __builtin_amdgcn_raw_buffer_load_b16(rp, idx < 324 ? ((4 * g + w) * 324 + idx) * 2 : OOB, 0, 0);
      }
    }
    {
      const u32x4 s = {SENT, SENT, SENT, SENT};
#pragma unroll
      for (int k = 0; k < (KVHEAD / 16 + 63) / 64; ++k) {
        const int n = lane + 64 * k;
        st_sc1(s, rws, n < KVHEAD / 16 ? kvp_grp + t * KVP_STRIDE + w * KVHEAD + n * 16 : OOB);
      }
    }
    if (tid < 64) *(LAS unsigned*)(lds + L_SYNC + tid * 4) = tid == SY_TAG ? T : 0u;
    if (tid < 32) *(LAS unsigned*)(lds + L_ZERO + tid * 4) = 0u;
#pragma unroll
    for (int n = 0; n < 9; ++n) { acc[n][0] = zero4(); acc[n][1] = zero4(); asm volatile("" : "+v"(acc[n][0]), "+v"(acc[n][1])); }
    const int xa0 = (32 * w + i) * 128 + ((gg ^ ((i >> 1) & 7)) * 16), xa1 = xa0 ^ 64;
    const int wa = WPART + (4 * gg + (i >> 2)) * 288 + (i & 3) * 8;
    const int wm = wa + ((i & 2) ? 256 : 128);
    {
      const int xs = ((i >> 2) & 3) << 3;
      const int vrow = L_V + (32 * w + i) * 160;
#pragma unroll
      for (int n = 0; n < 5; ++n) {
#pragma unroll
        for (int pp = 0; pp < 2; ++pp) {
          const int c0 = (n < 4 ? 16 * n : 56) + 2 * pp, h0 = c0 / 18;
          const int thr = (18 * (h0 + 1) - c0 + 3) / 4;
          a_v[n][pp] = opaque(vrow + ((2 * c0 + 4 * h0 + 8 * gg + ((thr < 4 && gg >= thr) ? 4 : 0)) ^ xs));
        }
      }
      a_one = opaque(vrow + ((40 * gg + 36) ^ xs));
      a_prow = opaque(L_PHI + (32 * w + i) * RP);
      a_pm = opaque((UMAIN * gg * 8) ^ xs);
      a_pl = opaque(L_PHI + (32 * w + i) * RP + (((4 * UMAIN + gg) * 8) ^ xs));
      const int q = i >> 2, p = i & 3;
      const int pa = L_PHI + (4 * gg + q) * RP, va = L_V + (4 * gg + q) * 160;
      a_b0 = opaque(pa + ((UMAIN * w + p) ^ gg) * 8); a_b1 = opaque(pa + ((UMAIN * w + 4 + p) ^ gg) * 8);
      a_mu = opaque(pa + ((4 * UMAIN + p) ^ gg) * 8);
      a_al = opaque(va + ((5 * w + p) ^ gg) * 8); a_la = opaque(va + ((5 * p + 4) ^ gg) * 8);
      const unsigned mine = kvp_grp + t * KVP_STRIDE + w * KVHEAD;
      const bool lam = (i >> 2) == w && (i & 3) < 3;
      const int el = 16 + (i & 3), ch = ISB ? ((gg & 1) << 1) | (gg >> 1) : gg >> 1;
      const unsigned o_main = mine + i * KVROW + ch * 16, o_lam = lam ? mine + el * KVROW + ch * 16 : OOB;
      o_p0 = opaque(ISB || !(gg & 1) ? o_main : o_lam); o_p1 = opaque(o_lam);
      o_mu = opaque(gg == w ? mine + i * KVROW + (C5 - 1) * 16 : OOB);
      o_ml = opaque((gg == w && lam) ? mine + el * KVROW + (C5 - 1) * 16 : OOB);
    }
    int a_wf[6];
    {
      const u32x4 z = {0u, 0u, 0u, 0u};
#pragma unroll
      for (int k = 0; k < 3; ++k) {
        const int n = lane + 64 * k, fr = n >> 4, sl = n & 15;
        if (n < 160) *(LAS u32x4*)(lds + L_WF + fr * 1024 + (16 * (sl >> 2) + 4 * w + (sl & 3)) * 16) = z;
      }
#pragma unroll
      for (int k = 0; k < 2; ++k) {
        const int n = lane + 64 * k, fr = n >> 4, sl = n & 15;
        if (n < 80) *(LAS u32x2*)(lds + L_WF + 10240 + fr * 512 + (16 * (sl >> 2) + 4 * w + (sl & 3)) * 8) = u32x2{0u, 0u};
      }
      if constexpr (ISB) {
#pragma unroll
        for (int k = 0; k < 6; ++k) {
          const int idx = lane + 64 * k, d = idx / 18, f = idx - d * 18;
          a_wf[k] = opaque(idx < 324 ? L_WF + wf_offset(w, d, f) : L_ZERO + 124);
        }
      } else if (lane < 18) {
        *(LAS unsigned short*)(lds + L_WF + wf_offset(w, lane, lane)) = (unsigned short)0x3F80;
      }
    }
    bf16x8 wb[2][9], xb[2][2];
    auto kstep = [&](auto SC, int xo, int wo, int wmo) {
      constexpr int S = decltype(SC)::value, N = S ^ 1;
#pragma unroll
      for (int n = 0; n < 9; ++n) {
        mfma_acc(acc[n][0], wb[S][n], xb[S][0]);
        if (n == 0) { xb[N][0] = lds128(lds, xo); xb[N][1] = lds128(lds, xo + 2048); }
        else if (n <= 4) wb[N][2 * n - 1] = lds_tr(lds, 2 * n - 1 < 8 ? wo + (2 * n - 1) * 32 + (2 * n - 1 >= 4 ? 16 : 0) : wmo, 4608);
        mfma_acc(acc[n][1], wb[S][n], xb[S][1]);
        if (n <= 4) wb[N][2 * n] = lds_tr(lds, 2 * n < 8 ? wo + (2 * n) * 32 + (2 * n >= 4 ? 16 : 0) : wmo, 4608);
        SBAR();
      }
    };
    using I0 = std::integral_constant<int, 0>;
    using I1 = std::integral_constant<int, 1>;
    STAMP(1);
    barrier();
    STAMP(2); CSTAMP(44);
    {
      const int xo = opaque(xa0 + 3 * SLOT), wo = opaque(wa + 3 * SLOT), wmo = opaque(wm + 3 * SLOT);
      xb[0][0] = lds128(lds, xo); xb[0][1] = lds128(lds, xo + 2048);
#pragma unroll
      for (int n = 0; n < 9; ++n) wb[0][n] = lds_tr(lds, n < 8 ? wo + n * 32 + (n >= 4 ? 16 : 0) : wmo, 4608);
    }
    int base = 3 * SLOT;
    unsigned pfq = 0u;
#pragma unroll
    for (int c = 0; c < 8; ++c) {
      {
        const int xo = opaque(xa1 + base), wo = opaque(wa + base + 9216), wmo = opaque(wm + base + 9216);
        cwait_lgkm0();
        SBAR();
        kstep(I0{}, xo, wo, wmo);
      }
      if (PFQ && c == 1) {
        const int row = 18 * (blockIdx.x >> 3) + tid / 9, ln = tid - 9 * (tid / 9);
        pfq = __builtin_amdgcn_raw_buffer_load_b32(mk_rsrc(ISB ? a.wB : a.wA, 576 * WROW), tid < 162 ? row * WROW + ln * 128 : OOB, 0, 0);
      }
      if constexpr (ISB) {
        if (c == 3) {
#pragma unroll
          for (int k = 0; k < 6; ++k) *(LAS unsigned short*)(lds + a_wf[k]) = wel[k];
        }
      }
      if (c == 5) { asm volatile("" :: "v"(pfq), "v"(tl0), "v"(tl1), "v"(tl2)); wait_vm<0>(); }
      cwait_lgkm0();
      barrier();
      base = base == 3 * SLOT ? 0 : base + SLOT;
      {
        const int xo = opaque(xa0 + base), wo = opaque(wa + base), wmo = opaque(wm + base);
        SBAR();
        kstep(I1{}, xo, wo, wmo);
      }
    }
    {
      const int xo = opaque(xa1 + base), wo = opaque(wa + base + 9216), wmo = opaque(wm + base + 9216);
      cwait_lgkm0();
      SBAR();
      kstep(I0{}, xo, wo, wmo);
      cwait_lgkm0();
      lds_arrive(lds, SY_KV8);
      const int b0 = opaque(L_WF + lane * 16), b1 = opaque(L_WF + 10240 + lane * 8);
      SBAR();
#pragma unroll
      for (int n = 0; n < 9; ++n) {
        mfma_acc(acc[n][0], wb[1][n], xb[1][0]);
        if (n < 5) load_wf_tile(lds, b0, b1, wf, n);
        mfma_acc(acc[n][1], wb[1][n], xb[1][1]);
        SBAR();
      }
      lds_wait(lds, SY_KV8, 8u);
      mfma_fence(acc);
    }
  }
  const int lane = opaque(lane0), tid = w * 64 + lane, i = lane & 15, gg = lane >> 4;
  const unsigned Ts = __builtin_amdgcn_readfirstlane(T);
  __builtin_amdgcn_s_setprio(2);
  STAMP(3); CSTAMP(45);

  {
    f32x4 P[2][5];
    bf16x8 kb[2][2];
    u32x2 kb2[2];
#pragma unroll
    for (int m = 0; m < 2; ++m) {
      kb[m][0] = pack_pair(acc[0][m], acc[1][m]); kb[m][1] = pack_pair(acc[2][m], acc[3][m]);
      kb2[m] = u32x2{pk2(acc[8][m][0], acc[8][m][1]), pk2(acc[8][m][2], acc[8][m][3])};
    }
    SBAR();
#pragma unroll
    for (int st = 0; st < 30; ++st) {
      const int m = st / 15, s = (st % 15) / 5, tt = st % 5;
      if (s == 0) P[m][tt] = mfma(wf.a[tt][0], kb[m][0], zero4());
      if (s == 1) P[m][tt] = mfma(wf.a[tt][1], kb[m][1], P[m][tt]);
      if (s == 2) P[m][tt] = mfma16(wf.h[tt], kb2[m], P[m][tt]);
      if (st < 20) {
        const int n = st >> 2, pp = (st >> 1) & 1, mv = st & 1;
        const f32x4 v = n < 4 ? acc[4 + n][mv] : acc[8][mv];
        const int am = n < 4 ? a_v[n][pp] + mv * 2560 : (gg >= 2 ? a_v[n][pp] + mv * 2560 : L_ZERO + 124);
        *(LAS unsigned*)(lds + am) = pk2(v[2 * pp], v[2 * pp + 1]);
      }
      SBAR();
    }
    *(LAS unsigned*)(lds + a_one) = 0x00003F80u; *(LAS unsigned*)(lds + a_one + 2560) = 0x00003F80u;
    FSTAMP(18);
#pragma unroll
    for (int m = 0; m < 2; ++m) {
      unsigned ph[ISB ? 18 : 10];
      feature_map<ISB>(P[m], ph);
#pragma unroll
      for (int j = 0; j < NU; ++j)
        *(LAS u32x2*)(lds + (j < UMAIN ? a_prow + (a_pm ^ (j * 8)) : a_pl) + m * 16 * RP) = u32x2{ph[2 * j], ph[2 * j + 1]};
      FSTAMP(19 + m);
    }
  }
  STAMP(4);
  lds_arrive(lds, SY_PHI);
  lds_wait(lds, SY_PHI);
  STAMP(5);

  {
    bf16x8 al[4], la[4], b0[4], mu[4], b1[4];
    f32x4 d0 = zero4(), d1 = zero4(), d2 = zero4(), d3 = zero4(), d4 = zero4(), d5 = zero4();
    asm volatile("" : "+v"(d0), "+v"(d1), "+v"(d2), "+v"(d3), "+v"(d4), "+v"(d5));
    auto rd = [&](int ks) {
      al[ks] = lds_tr(lds, a_al + ks * 32 * 160, 16 * 160);
      b0[ks] = lds_tr(lds, a_b0 + ks * 32 * RP, 16 * RP);
      if constexpr (ISB) b1[ks] = lds_tr(lds, a_b1 + ks * 32 * RP, 16 * RP);
      la[ks] = lds_tr(lds, a_la + ks * 32 * 160, 16 * 160);
      mu[ks] = lds_tr(lds, a_mu + ks * 32 * RP, 16 * RP);
      SBAR();
    };
    auto mm = [&](int ks) {
      mfma_acc(d0, b0[ks], al[ks]);
      if constexpr (ISB) mfma_acc(d1, b1[ks], al[ks]);
      mfma_acc(d2, b0[ks], la[ks]);
      if constexpr (ISB) mfma_acc(d3, b1[ks], la[ks]);
      mfma_acc(d4, mu[ks], al[ks]); mfma_acc(d5, mu[ks], la[ks]);
      SBAR();
    };
    constexpr int RD = ISB ? 10 : 8;
    rd(0); rd(1);
    __builtin_amdgcn_s_waitcnt(0xC07F & ~0xF00 | (RD << 8)); SBAR(); mm(0); rd(2);
    __builtin_amdgcn_s_waitcnt(0xC07F & ~0xF00 | (RD << 8)); SBAR(); mm(1); rd(3);
    __builtin_amdgcn_s_waitcnt(0xC07F & ~0xF00 | (RD << 8)); SBAR(); mm(2);
    cwait_lgkm0(); SBAR(); mm(3);
    asm volatile("s_nop 7\n\ts_nop 3" : "+v"(d0), "+v"(d1), "+v"(d2), "+v"(d3), "+v"(d4), "+v"(d5));
    if constexpr (ISB) {
      unsigned x0 = pk2(d0[0], d0[1]), x1 = pk2(d0[2], d0[3]), y0 = pk2(d1[0], d1[1]), y1 = pk2(d1[2], d1[3]);
      swap16(x0, y0); swap16(x1, y1);
      st_sc1(u32x4{nosent(x0), nosent(x1), nosent(y0), nosent(y1)}, rws, o_p0);
      x0 = pk2(d2[0], d2[1]); x1 = pk2(d2[2], d2[3]); y0 = pk2(d3[0], d3[1]); y1 = pk2(d3[2], d3[3]);
      swap16(x0, y0); swap16(x1, y1);
      st_sc1(u32x4{nosent(x0), nosent(x1), nosent(y0), nosent(y1)}, rws, o_p1);
    } else {
      unsigned x0 = pk2(d0[0], d0[1]), x1 = pk2(d0[2], d0[3]), y0 = pk2(d2[0], d2[1]), y1 = pk2(d2[2], d2[3]);
      swap16(x0, y0); swap16(x1, y1);
      st_sc1(u32x4{nosent(x0), nosent(x1), nosent(y0), nosent(y1)}, rws, o_p0);
    }
    st_sc1(u32x4{nosent(pk2(d4[0], d4[1])), nosent(pk2(d4[2], d4[3])), 0u, 0u}, rws, o_mu);
    st_sc1(u32x4{nosent(pk2(d5[0], d5[1])), nosent(pk2(d5[2], d5[3])), 0u, 0u}, rws, o_ml);
    st_tag(rws, WS_H1 + (((prob * 8 + g) * 16 + t) * 4 + w) * 128, T, lane);
    lds_arrive(lds, SY_KVDONE);
  }
  __builtin_amdgcn_s_setprio(0);
  STAMP(6); CSTAMP(46);
  const rsrc_t rwo = mk_rsrc(ISB ? a.woB : a.woA, 576 * XROW);
  {
    STAMP(7);
    const rsrc_t rkv = mk_rsrc(ws + kvp_grp, 16 * KVP_STRIDE);
    constexpr int NG = (NCH + 15) / 16;
    const unsigned one = (i & 1) ? 0x3F800000u : 0x3F80u;
    const bf16x8 sel = frag_of((i >> 1) == 0 ? one : 0u, (i >> 1) == 1 ? one : 0u, (i >> 1) == 2 ? one : 0u, (i >> 1) == 3 ? one : 0u);
    const unsigned off0 = gg * KVP_STRIDE + w * KVHEAD + i * 16;
    const unsigned offl = 16 * (NG - 1) + i < NCH ? off0 + (NG - 1) * 256 : OOB;
    const int kvw = L_PHI + w * KVHEAD + i * 16 + gg * 8;
    unsigned pfo = 0u;
    if (PFO) {
      const int row = 18 * (blockIdx.x >> 3) + tid / 9, ln = tid - 9 * (tid / 9);
      asm volatile("buffer_load_dword %0, %1, %2, 0 offen" : "=v"(pfo) : "v"(tid < 162 ? row * XROW + ln * 128 : OOB), "s"(rwo) : "memory");
    }
    unsigned p0, p1;
    poll_tag(rws, lane < 16 ? WS_H1 + (((prob * 8 + g) * 16 + lane) * 4 + w) * 128 : OOB, Ts, 0xFFFFull, p0, p1);
    __builtin_amdgcn_s_setprio(2);
    STAMP(8);
    f32x4 sum[NG];
    {
      u32x4 v[NG][4];
#pragma unroll
      for (int cg = 0; cg < NG; ++cg) {
        const unsigned off = cg < NG - 1 ? off0 + cg * 256 : offl;
#pragma unroll
        for (int u = 0; u < 4; ++u)
          asm volatile("buffer_load_dwordx4 %0, %1, %2, %3 offen sc1" : "=v"(v[cg][u]) : "v"(off), "s"(rkv), "s"(4 * u * KVP_STRIDE) : "memory");
      }
      {
        const u32x4 s = {SENT, SENT, SENT, SENT};
#pragma unroll
        for (int k = 0; k < 5; ++k) {
          const int n = lane + 64 * k;
          st_sc1(s, rws, n < 288 ? obuf + (2 * w * 72 + 9 * g) * 256 + n * 16 + (n >= 144 ? (72 - 9) * 256 : 0) : OOB);
        }
        SBAR();
      }
      float chk = 0.f;
#pragma unroll
      for (int cg = 0; cg < NG; ++cg) {
        asm volatile("s_waitcnt vmcnt(%7)" : "+v"(v[cg][0]), "+v"(v[cg][1]), "+v"(v[cg][2]), "+v"(v[cg][3]), "+v"(p0), "+v"(p1), "+v"(pfo)
                     : "n"((NG - 1 - cg) * 4 + 5));
        SBAR();
        f32x4 d = zero4();
#pragma unroll
        for (int u = 0; u < 4; ++u) d = mfma(sel, __builtin_bit_cast(bf16x8, v[cg][u]), d);
        sum[cg] = d;
        chk += d[0];
      }
      FSTAMP(21);
      if (!__all(chk == chk)) {
        PCOUNT(22);
#pragma unroll 1
        for (int sp = 0; sp < SPIN_G; ++sp) {
          unsigned mx = 0u;
#pragma unroll 1
          for (int n = 0; n < NG * 4; ++n) {
            asm volatile("" ::: "memory");
            const u32x4 x = ld_sc1(rkv, (n >> 2) < NG - 1 ? off0 + (n >> 2) * 256 : offl, 4 * (n & 3) * KVP_STRIDE);
            mx = umax3(mx, x[0], x[1]); mx = umax3(mx, x[2], x[3]);
          }
          if (__all(mx != SENT)) break;
        }
#pragma unroll
        for (int cg = 0; cg < NG; ++cg) {
          f32x4 d = zero4();
#pragma unroll
          for (int u = 0; u < 4; ++u)
            d = mfma(sel, __builtin_bit_cast(bf16x8, ld_sc1(rkv, cg < NG - 1 ? off0 + cg * 256 : offl, 4 * u * KVP_STRIDE)), d);
          sum[cg] = d;
        }
      }
    }
    lds_wait(lds, SY_KVDONE);
    if (gg < 2) {
#pragma unroll
      for (int cg = 0; cg < NG; ++cg)
        if (cg < NG - 1 || 16 * cg + i < NCH)
          *(LAS u32x2*)(lds + kvw + cg * 256) = u32x2{pk2(sum[cg][0], sum[cg][1]), pk2(sum[cg][2], sum[cg][3])};
    }
    lds_arrive(lds, SY_KVIMG);
    wait_vm<0>();
    lds_arrive(lds, SY_PRE);
  }
  STAMP(9);
  __builtin_amdgcn_s_setprio(0);
  STAMP(10);

  {
    const rsrc_t ro = mk_rsrc(ws + obuf, O_STRIDE);
    const rsrc_t rout = mk_rsrc(a.out, 2048 * OUTROW);
    const unsigned orow = (t * 128 + 32 * w + i) * OUTROW + prob * 1152 + 144 * g;
    const unsigned o_pair = orow + (2 * (gg & 1) + (gg >> 1)) * 16, o_last = gg < 2 ? orow + gg * 16 * OUTROW + 128 : OOB;
    const int wl = (4 * gg + (i >> 2)) * 160 + (i & 3) * 8;
    const int wa2 = opaque(L_WQ + wl), wa3 = opaque(L_WQ + wl + 12 * 5120);
    lds_wait(lds, SY_WO);
    unsigned p0, p1;
    poll_tag(rws, lane < 8 ? WS_H2 + (((prob * 16 + t) * 8 + lane) * 4 + w) * 128 : OOB, Ts, 0xFFull, p0, p1);
    __builtin_amdgcn_s_setprio(2);
    STAMP(11);
    f32x4 oc[5][2];
#pragma unroll 1
    for (int pass = 0; pass < 2; ++pass) {
      asm volatile("" ::: "memory");
      u32x4 of[18][2];
#pragma unroll
      for (int ks = 0; ks < 18; ++ks) {
#pragma unroll
        for (int m = 0; m < 2; ++m) { of[ks][m] = ld_sc1(ro, lane * 16, ((2 * w + m) * 72 + 4 * ks) * 256); SBAR(); }
      }
      bf16x8 wq[2][5];
#pragma unroll
      for (int n = 0; n < 5; ++n) {
        wq[0][n] = lds_tr(lds, wa2 + n * 32, 16 * 160);
        oc[n][0] = zero4(); oc[n][1] = zero4();
        asm volatile("" : "+v"(oc[n][0]), "+v"(oc[n][1]));
      }
      SBAR();
#define OUT_STEP(ks) do { cwait_vm_lgkm4<34 - 2 * (ks)>(); SBAR(); \
      step5(lds, oc, wq[(ks) & 1], wq[((ks) + 1) & 1], __builtin_bit_cast(bf16x8, of[ks][0]), __builtin_bit_cast(bf16x8, of[ks][1]), \
            (ks) + 1 < 12 ? wa2 + ((ks) + 1) * 5120 : wa3 + ((ks) + 1 - 12) * 5120, (ks) < 17); } while (0)
      OUT_STEP(0);
      asm volatile("" :: "v"(p0), "v"(p1));
      OUT_STEP(1); OUT_STEP(2); OUT_STEP(3); OUT_STEP(4); OUT_STEP(5); OUT_STEP(6); OUT_STEP(7); OUT_STEP(8);
      OUT_STEP(9); OUT_STEP(10); OUT_STEP(11); OUT_STEP(12); OUT_STEP(13); OUT_STEP(14); OUT_STEP(15); OUT_STEP(16); OUT_STEP(17);
#undef OUT_STEP
      mfma_fence(oc);
      const float chk = oc[0][0][0] + oc[0][1][0];
      if (pass == 1 || __all(chk == chk)) break;
      PCOUNT(23);
#pragma unroll 1
      for (int sp = 0; sp < SPIN_G; ++sp) {
        unsigned mx = 0u;
#pragma unroll 1
        for (int n = 0; n < 36; ++n) {
          asm volatile("" ::: "memory");
          const u32x4 x = ld_sc1(ro, lane * 16, ((2 * w + (n & 1)) * 72 + 4 * (n >> 1)) * 256);
          mx = umax3(mx, x[0], x[1]); mx = umax3(mx, x[2], x[3]);
        }
        if (__all(mx != SENT)) break;
      }
    }
    STAMP(12);
#pragma unroll
    for (int m = 0; m < 2; ++m) {
#pragma unroll
      for (int n = 0; n < 4; n += 2) {
        unsigned x0 = pk2(oc[n][m][0], oc[n][m][1]), x1 = pk2(oc[n][m][2], oc[n][m][3]);
        unsigned y0 = pk2(oc[n + 1][m][0], oc[n + 1][m][1]), y1 = pk2(oc[n + 1][m][2], oc[n + 1][m][3]);
        swap16(x0, y0); swap16(x1, y1);
        __builtin_amdgcn_raw_buffer_store_b128(u32x4{x0, x1, y0, y1}, rout, o_pair + m * 16 * OUTROW + n * 32, 0, 0);
      }
    }
    unsigned x0 = pk2(oc[4][0][0], oc[4][0][1]), x1 = pk2(oc[4][0][2], oc[4][0][3]);
    unsigned y0 = pk2(oc[4][1][0], oc[4][1][1]), y1 = pk2(oc[4][1][2], oc[4][1][3]);
    swap16(x0, y0); swap16(x1, y1);
    __builtin_amdgcn_raw_buffer_store_b128(u32x4{x0, x1, y0, y1}, rout, o_last, 0, 0);
    if (t == 0 && g == 0 && tid == 0) __hip_atomic_store((GAS unsigned*)(ws + WS_E + prob * 256), T, RLX_AGENT);
  }
  STAMP(13); CSTAMP(47);
  STAMP_OUT(w);
}

template <bool ISB> DI void h_path(const Args& a, LAS char* lds, const int w, const int lane, const int t, const int g) {
  constexpr int prob = ISB ? 1 : 0;
  constexpr int C5 = ISB ? 5 : 3;
  constexpr int KVROW = C5 * 16, KVHEAD = ISB ? 1536 : 1024;
  const int i = lane & 15, gg = lane >> 4;
  const rsrc_t rx = mk_rsrc((const char*)(ISB ? a.xB : a.xA) + (size_t)t * 128 * XROW, 128 * XROW);
  const rsrc_t rw = mk_rsrc(ISB ? a.wB : a.wA, 576 * WROW);
  const unsigned lb = (unsigned)(size_t)lds;
  STAMP_DECL
  STAMP(0);
  const int rot = ROT == 1 ? (g + 2 * (t & 3) + ((t >> 1) & 1)) % 9 : ROT == 2 ? (g + 8 * (t & 3)) % 9 : 0;
  auto kchunk = [&](int c) { const int k = c + rot; return k >= 9 ? k - 9 : k; };
  unsigned vx[2], vw[5], vq[8];
#pragma unroll
  for (int par = 0; par < 2; ++par) vx[par] = (lane >> 3) * XROW + (((lane & 7) ^ ((4 * par + (lane >> 4)) & 7)) * 16);
  auto issue_x = [&](int c) {
    const unsigned sb = lb + ((c + 3) & 3) * SLOT;
#pragma unroll
    for (int jj = 0; jj < 4; ++jj) dma16(rx, vx[jj & 1], (4 * w + jj) * 8 * XROW + kchunk(c) * 128, sb + (4 * w + jj) * 1024);
  };
  issue_x(0);
#pragma unroll
  for (int n = 0; n < 5; ++n) {
    const int nn = (w + 4 * n) * 64 + lane, r64 = nn / 18, cp = nn - 18 * r64;
    const int co = cp < 9 ? 1152 + 16 * cp : 2304 + 16 * (cp - 9);
    vw[n] = ((r64 & 32) + krow(r64 & 31)) * WROW + 144 * g + co;
  }
  auto issue_w = [&](int c) {
    const unsigned sb = lb + ((c + 3) & 3) * SLOT;
#pragma unroll
    for (int n = 0; n < 5; ++n)
      if (n < 4 || w < 2) dma16(rw, vw[n], kchunk(c) * 64 * WROW, sb + WPART + (w + 4 * n) * 1024);
  };
  issue_w(0); issue_x(1); issue_w(1);
  if (PRO >= 2) { issue_x(2); issue_w(2); }
  if (PRO >= 3) { issue_x(3); issue_w(3); }
  STAMP(1);
  if (PRO == 0) { if (w < 2) wait_vm<9>(); else wait_vm<8>(); }
  if (PRO == 2) { if (w < 2) wait_vm<18>(); else wait_vm<16>(); }
  if (PRO == 3) { if (w < 2) wait_vm<27>(); else wait_vm<24>(); }
  STAMP(2);
  barrier();
  if (PRO < 2) { issue_x(2); issue_w(2); }
#pragma unroll
  for (int n = 0; n < 8; ++n) {
    const int v = w + 4 * n, ks = v / 5, nn = (v - 5 * ks) * 64 + lane, rho = nn / 10, cp = nn - 10 * rho;
    vq[n] = ((ks & 1) * 32 + krow(rho)) * WROW + 144 * g + 16 * min(cp, 8);
  }
  auto issue_wq = [&](int grp) {
#pragma unroll
    for (int n = 0; n < 8; ++n)
      if (n < 7 || w < 2)
        dma16(rw, vq[n], kchunk(3 * grp + ((w + 4 * n) / 5 >> 1)) * 64 * WROW, lb + L_WQ + grp * 30720 + (w + 4 * n) * 1024);
  };
  bf16x8 X[18][2];
  const int xa0 = (32 * w + i) * 128 + ((gg ^ ((i >> 1) & 7)) * 16), xa1 = xa0 ^ 64;
#pragma unroll
  for (int c = 0; c < 9; ++c) {
    const int base = ((c + 3) & 3) * SLOT;
    const int xo0 = opaque(xa0 + base), xo1 = opaque(xa1 + base);
    X[2 * c][0] = lds128(lds, xo0); X[2 * c][1] = lds128(lds, xo0 + 2048);
    X[2 * c + 1][0] = lds128(lds, xo1); X[2 * c + 1][1] = lds128(lds, xo1 + 2048);
    if (c <= 5 && (c > 0 || PRO < 3)) { issue_x(c + 3); issue_w(c + 3); }
    LSTAMP(16 + c);
    if (c <= 5) { if (w < 2) wait_vm<18>(); else wait_vm<16>(); }
    if (c == 6) { if (w < 2) wait_vm<9>(); else wait_vm<8>(); }
    if (c == 7) wait_vm<0>();
    LSTAMP(25 + c);
    if (c < 8) barrier(); else lds_arrive(lds, SY_KV8);
    LSTAMP(34 + c);
  }
  issue_wq(0); issue_wq(1); issue_wq(2);
  __builtin_amdgcn_s_setprio(1);
  STAMP(3);
  f32x4 qa[5][2];
#pragma unroll
  for (int n = 0; n < 5; ++n) { qa[n][0] = zero4(); qa[n][1] = zero4(); asm volatile("" : "+v"(qa[n][0]), "+v"(qa[n][1])); }
  const int wa2 = opaque(L_WQ + (4 * gg + (i >> 2)) * 160 + (i & 3) * 8), wa3 = opaque(wa2 + 12 * 5120);
  bf16x8 wq[2][5];
  if (w < 2) wait_vm<16>(); else wait_vm<14>();
  lds_arrive(lds, SY_WQ0);
  lds_wait(lds, SY_WQ0);
  STAMP(4);
#pragma unroll
  for (int n = 0; n < 5; ++n) wq[0][n] = lds_tr(lds, wa2 + n * 32, 16 * 160);
#pragma unroll
  for (int ks = 0; ks < 18; ++ks) {
    cwait_lgkm<4>();
    if (ks == 5) { if (w < 2) wait_vm<8>(); else wait_vm<7>(); lds_arrive(lds, SY_WQ1); lds_wait(lds, SY_WQ1); STAMP(5); }
    if (ks == 11) { wait_vm<0>(); lds_arrive(lds, SY_WQ2); lds_wait(lds, SY_WQ2); STAMP(6); }
    SBAR();
    step5(lds, qa, wq[ks & 1], wq[(ks + 1) & 1], X[ks][0], X[ks][1], ks + 1 < 12 ? wa2 + (ks + 1) * 5120 : wa3 + (ks + 1 - 12) * 5120, ks < 17);
  }
  WF wf;
  {
    const int b0 = opaque(L_WF + lane * 16), b1 = opaque(L_WF + 10240 + lane * 8);
#pragma unroll
    for (int n = 0; n < 5; ++n) load_wf_tile(lds, b0, b1, wf, n);
  }
  mfma_fence(qa);
  lds_arrive(lds, SY_QDONE);
  STAMP(7);
  const unsigned T = *(LAS unsigned*)(lds + L_SYNC + SY_TAG * 4);
  unsigned ph[2][ISB ? 18 : 10];
  {
    f32x4 P[2][5];
    logits2(wf, qa[0], qa[1], qa[2], qa[3], qa[4], P);
    feature_map<ISB>(P[0], ph[0]);
    feature_map<ISB>(P[1], ph[1]);
#pragma unroll
    for (int j = 0; j < (ISB ? 18 : 10); ++j) asm volatile("" : "+v"(ph[0][j]), "+v"(ph[1][j]));
  }
  int a_kv[5], a_st[9];
  {
    const bool own = (i >> 2) == gg;
#pragma unroll
    for (int n = 0; n < 5; ++n) {
      const int e = 4 * n + (i & 3);
      a_kv[n] = opaque((own && e < 19) ? L_PHI + gg * KVHEAD + e * KVROW : L_ZERO);
    }
#pragma unroll
    for (int pp = 0; pp < 9; ++pp) {
      const int cp = 9 * gg + pp;
      a_st[pp] = opaque(L_V + w * 4608 + (cp >> 2) * 256 + i * 16 + (cp & 3) * 4);
    }
  }
  const rsrc_t ro = mk_rsrc(a.ws + WS_O + (prob * 16 + t) * O_STRIDE, O_STRIDE);
  const unsigned o_dst = (2 * w * 72 + 9 * g) * 256 + lane * 16;
  const rsrc_t rwo = mk_rsrc(ISB ? a.woB : a.woA, 576 * XROW);
  unsigned vo[5];
#pragma unroll
  for (int n = 0; n < 5; ++n) {
    const int nn = ((w + 4 * n) % 5) * 64 + lane, rho = nn / 10, cp = nn - 10 * rho;
    vo[n] = (unsigned)opaque(krow(rho) * XROW + 144 * g + 16 * min(cp, 8));
  }
  lds_wait(lds, SY_QDONE);
#pragma unroll
  for (int n = 0; n < WO_FIRST; ++n) { const int v = w + 4 * n; dma16(rwo, vo[n % 5], (v / 5) * 32 * XROW, lb + L_WQ + v * 1024); }
  STAMP(8);
  lds_wait(lds, SY_KVIMG);
  __builtin_amdgcn_s_setprio(3);
  STAMP(9);
  f32x4 O[5][2];
  {
    bf16x8 af[5][C5];
#pragma unroll
    for (int s = 0; s < C5; ++s) {
#pragma unroll
      for (int n = 0; n < 5; ++n) af[n][s] = lds128(lds, a_kv[n] + s * 16);
    }
    SBAR();
    constexpr int NP = ISB ? 18 : 10;
#pragma unroll
    for (int s = 0; s < C5; ++s) {
      bf16x8 bq[2];
#pragma unroll
      for (int m = 0; m < 2; ++m) {
        unsigned u[4];
#pragma unroll
        for (int j = 0; j < 4; ++j) u[j] = 4 * s + j < NP ? ph[m][4 * s + j < NP ? 4 * s + j : 0] : 0u;
        bq[m] = frag_of(u[0], u[1], u[2], u[3]);
      }
#pragma unroll
      for (int n = 4; n >= 0; --n) {
#pragma unroll
        for (int m = 0; m < 2; ++m) O[n][m] = mfma(af[n][s], bq[m], s == 0 ? zero4() : O[n][m]);
      }
    }
  }
  STAMP(10);
#pragma unroll
  for (int m = 0; m < 2; ++m) {
    const float rc = __builtin_amdgcn_rcpf(O[4][m][2] + 1e-6f);
    const f32x2 rc2 = {rc, rc};
#pragma unroll
    for (int pp = 0; pp < 9; ++pp) {
      const int e = 2 * pp;
      const f32x2 y = f32x2{O[e >> 2][m][e & 3], O[(e + 1) >> 2][m][(e + 1) & 3]} * rc2;
      *(LAS unsigned*)(lds + a_st[pp] + m * 2304) = nosent(pk2(y[0], y[1]));
    }
  }
  lds_wait(lds, SY_PRE);
  {
    const int sb = L_V + w * 4608 + lane * 16;
    u32x4 v[5];
#pragma unroll
    for (int k = 0; k < 5; ++k) v[k] = *(LAS u32x4*)(lds + sb + (k < 4 || lane < 32 ? k * 1024 : 0));
#pragma unroll
    for (int k = 0; k < 5; ++k) {
      const int n = lane + 64 * k;
      st_sc1(v[k], ro, n < 288 ? o_dst + k * 1024 + (n >= 144 ? (72 - 9) * 256 : 0) : OOB);
    }
    st_tag(mk_rsrc(a.ws, WS_USED), WS_H2 + (((prob * 16 + t) * 8 + g) * 4 + w) * 128, T, lane);
  }
  STAMP(11);
#pragma unroll
  for (int n = WO_FIRST; n < 23; ++n) {
    const int v = w + 4 * n;
    if (n < 22 || w < 2) dma16(rwo, vo[n % 5], (v / 5) * 32 * XROW, lb + L_WQ + v * 1024);
  }
  wait_vm<0>();
  lds_arrive(lds, SY_WO);
  STAMP(12);
  STAMP_OUT(4 + w);
}

__global__ __launch_bounds__(512, 2) void hedgehog_fused(Args a) {
  extern __shared__ __attribute__((aligned(16))) char smem[];
  LAS char* lds = (LAS char*)smem;
  const int b = blockIdx.x, xcd = b & 7, r = b >> 3;
  const int prob = xcd >> 2, t = (xcd & 3) * 4 + (r >> 3), g = r & 7;
  const int wave = __builtin_amdgcn_readfirstlane(threadIdx.x >> 6), lane = threadIdx.x & 63;
  unsigned cpf = 0u;
  if (CPF) {
    unsigned long long pc;
    asm volatile("s_getpc_b64 %0" : "=s"(pc));
    cpf = *(const GAS unsigned*)((pc & ~127ull) + threadIdx.x * 128u);
  }
  if ((wave < 4) != (HFIRST != 0)) {
    if (prob) c_path<true>(a, lds, wave & 3, lane, t, g); else c_path<false>(a, lds, wave & 3, lane, t, g);
  } else {
    if (prob) h_path<true>(a, lds, wave & 3, lane, t, g); else h_path<false>(a, lds, wave & 3, lane, t, g);
  }
  asm volatile("" :: "v"(cpf));
}

}

extern "C" void kernel_launch(void* const* d_in, const int* in_sizes, int n_in, void* d_out, int out_size, void* d_ws,
                              size_t ws_size, hipStream_t stream) {
  (void)in_sizes; (void)n_in; (void)out_size; (void)ws_size;
  Args a;
  a.xA = d_in[0]; a.wA = d_in[1]; a.woA = d_in[2]; a.xB = d_in[3]; a.wB = d_in[4]; a.wphi = d_in[5]; a.woB = d_in[6];
  a.out = d_out; a.ws = (char*)d_ws;
  (void)hipFuncSetAttribute((const void*)hedgehog_fused, hipFuncAttributeMaxDynamicSharedMemorySize, LDS_BYTES);
  hedgehog_fused<<<256, 512, LDS_BYTES, stream>>>(a);
}
```

```cpp
#include <hip/hip_runtime.h>
#include <hip/hip_bf16.h>
#include <stdint.h>
#include <type_traits>

typedef __bf16 bf16x8 __attribute__((ext_vector_type(8)));
typedef __bf16 bf16x4 __attribute__((ext_vector_type(4)));
typedef __bf16 bf16x2 __attribute__((ext_vector_type(2)));
typedef short s16x4 __attribute__((ext_vector_type(4)));
typedef float f32x4 __attribute__((ext_vector_type(4)));
typedef float f32x2 __attribute__((ext_vector_type(2)));
typedef unsigned u32x4 __attribute__((ext_vector_type(4)));
typedef unsigned u32x2 __attribute__((ext_vector_type(2)));
#define LAS __attribute__((address_space(3)))
#define GAS __attribute__((address_space(1)))
#define DI __device__ __forceinline__
#define RLX_AGENT __ATOMIC_RELAXED, __HIP_MEMORY_SCOPE_AGENT
#define SBAR() __builtin_amdgcn_sched_barrier(0)
typedef __amdgpu_buffer_rsrc_t rsrc_t;

#define DBG 0
#define PROF 0
#define STAMP(n)
#define CSTAMP(n)
#define STAMP_DECL
#define STAMP_OUT(wv)
#define PCOUNT(n)
#define LSTAMP(n)
#define FSTAMP(n)
#define PFQ 0
#define PFO 1
#define WO_FIRST 11
#define TLBW 0
#define PRO 0
#define CPF 0
#define HFIRST 0
#define OLOOK 4
#define KV_UNROLL 1
#define ROT 1

namespace {

constexpr int XROW = 1152;
constexpr int WROW = 3456;
constexpr int OUTROW = 2304;
constexpr int SLOT = 34816, WPART = 16384;
constexpr int L_WQ = 0;
constexpr int L_PHI = 92160;
constexpr int L_V = 129024;
constexpr int L_WF = 149504;
constexpr int L_SYNC = 162304;
constexpr int L_ZERO = 162560;
constexpr int LDS_BYTES = 163840;
enum { SY_WQ0 = 0, SY_WQ1, SY_WQ2, SY_QDONE, SY_PHI, SY_KVDONE, SY_KVIMG, SY_WO, SY_KV8, SY_PRE, SY_TAG = 63 };
constexpr int WS_E = 0, WS_KVP = 65536, KVP_STRIDE = 6144, WS_O = 2097152, O_STRIDE = 147456;
constexpr int WS_H1 = WS_O + 32 * O_STRIDE, WS_H2 = WS_H1 + 131072;
constexpr int WS_USED = WS_H2 + 131072;
constexpr unsigned SENT = 0xFFFFFFFFu;
constexpr unsigned OOB = 0x7FFF0000u;
constexpr int SPIN_G = 1 << 18, SPIN_L = 1 << 22;
constexpr float LOG2E = 1.4426950408889634f;

struct Args {
  const void* xA; const void* wA; const void* woA; const void* xB; const void* wB; const void* wphi; const void* woB;
  void* out; char* ws;
};

DI f32x4 mfma(bf16x8 a, bf16x8 b, f32x4 c) { return __builtin_amdgcn_mfma_f32_16x16x32_bf16(a, b, c, 0, 0, 0); }
DI f32x4 mfma16(u32x2 a, u32x2 b, f32x4 c) {
  return __builtin_amdgcn_mfma_f32_16x16x16bf16_1k(__builtin_bit_cast(s16x4, a), __builtin_bit_cast(s16x4, b), c, 0, 0, 0);
}
DI void mfma_acc(f32x4& c, bf16x8 a, bf16x8 b) { asm volatile("v_mfma_f32_16x16x32_bf16 %0, %1, %2, %0" : "+v"(c) : "v"(a), "v"(b)); }
template <int N> DI void mfma_fence(f32x4 (&acc)[N][2]) {
  asm volatile("s_nop 7\n\ts_nop 3" : "+v"(acc[N - 1][0]), "+v"(acc[N - 1][1]) :: "memory");
#pragma unroll
  for (int n = 0; n < N - 1; ++n) asm volatile("" : "+v"(acc[n][0]), "+v"(acc[n][1]));
}
DI unsigned pk2(float a, float b) { f32x2 p = {a, b}; return __builtin_bit_cast(unsigned, __builtin_convertvector(p, bf16x2)); }
DI bf16x8 frag_of(unsigned a, unsigned b, unsigned c, unsigned d) { u32x4 v = {a, b, c, d}; return __builtin_bit_cast(bf16x8, v); }
DI bf16x8 pack_pair(f32x4 lo, f32x4 hi) { return frag_of(pk2(lo[0], lo[1]), pk2(lo[2], lo[3]), pk2(hi[0], hi[1]), pk2(hi[2], hi[3])); }
DI bf16x8 lds128(LAS char* l, int off) { return *(LAS bf16x8*)(l + off); }
DI bf16x8 lds_tr(LAS char* l, int off, int second) {
  bf16x4 a = __builtin_amdgcn_ds_read_tr16_b64_v4bf16((LAS bf16x4*)(l + off));
  bf16x4 b = __builtin_amdgcn_ds_read_tr16_b64_v4bf16((LAS bf16x4*)(l + off + second));
  return __builtin_shufflevector(a, b, 0, 1, 2, 3, 4, 5, 6, 7);
}
DI void dma16(rsrc_t r, unsigned voff, unsigned soff, unsigned ldsaddr) {
  asm volatile("s_mov_b32 m0, %0\n\ts_nop 0\n\tbuffer_load_dwordx4 %1, %2, %3 offen lds" ::"s"(ldsaddr), "v"(voff), "s"(r), "s"(soff) : "memory");
}
template <int N> DI void wait_vm() { asm volatile("s_waitcnt vmcnt(%0)" ::"n"(N) : "memory"); }
DI void cwait_lgkm0() { __builtin_amdgcn_s_waitcnt(0xC07F); }
template <int N> DI void cwait_vm_lgkm4() { __builtin_amdgcn_s_waitcnt((N & 15) | 0x70 | (4 << 8) | ((N >> 4) << 14)); }
DI void barrier() { asm volatile("s_waitcnt lgkmcnt(0)\n\ts_barrier" ::: "memory"); }
DI void lds_arrive(LAS char* l, int id) {
  const unsigned addr = (unsigned)(size_t)l + L_SYNC + id * 4, one = 1u;
  unsigned long long save;
  asm volatile("s_waitcnt lgkmcnt(0)\n\ts_mov_b64 %0, exec\n\ts_mov_b64 exec, 1\n\tds_add_u32 %1, %2\n\ts_mov_b64 exec, %0"
               : "=&s"(save) : "v"(addr), "v"(one) : "memory");
}
DI void lds_wait(LAS char* l, int id, unsigned n = 4u) {
  const unsigned addr = (unsigned)(size_t)l + L_SYNC + id * 4;
#pragma unroll 1
  for (int sp = 0; sp < SPIN_L; ++sp) {
    unsigned v;
    asm volatile("ds_read_b32 %0, %1\n\ts_waitcnt lgkmcnt(0)" : "=v"(v) : "v"(addr) : "memory");
    if ((unsigned)__builtin_amdgcn_readfirstlane(v) >= n) break;
    __builtin_amdgcn_s_sleep(1);
  }
}
DI rsrc_t mk_rsrc(const void* p, int bytes) { return __builtin_amdgcn_make_buffer_rsrc((void*)p, 0, bytes, 0x00020000); }
DI void st_sc1(u32x4 v, rsrc_t r, unsigned off) { __builtin_amdgcn_raw_buffer_store_b128(v, r, off, 0, 16); }
DI u32x4 ld_sc1(rsrc_t r, unsigned off, unsigned soff) { return __builtin_amdgcn_raw_buffer_load_b128(r, off, soff, 16); }
DI int opaque(int x) { asm volatile("" : "+v"(x)); return x; }
DI unsigned umax3(unsigned a, unsigned b, unsigned c) { return max(max(a, b), c); }
DI unsigned nosent(unsigned x) { return min(x, 0xFFFFFFFEu); }
DI void swap16(unsigned& a, unsigned& b) { auto r = __builtin_amdgcn_permlane16_swap(a, b, false, false); a = r[0]; b = r[1]; }
DI f32x4 zero4() { return f32x4{0.f, 0.f, 0.f, 0.f}; }
DI float fmax3(float a, float b, float c) { return fmaxf(fmaxf(a, b), c); }
DI float fmin3(float a, float b, float c) { return fminf(fminf(a, b), c); }
DI void poll_tag(rsrc_t r, unsigned off, unsigned tag, unsigned long long mask, unsigned& p0, unsigned& p1) {
  unsigned cnt = SPIN_G;
  asm volatile(
      "buffer_load_dword %0, %3, %4, 0 offen sc1\n"
      "L_poll_%=:\n\t"
      "buffer_load_dword %1, %3, %4, 0 offen sc1\n\t"
      "s_waitcnt vmcnt(1)\n\t"
      "v_cmp_ne_u32 vcc, %5, %0\n\t"
      "s_nop 1\n\t"
      "s_and_b64 vcc, vcc, %6\n\t"
      "s_cbranch_vccz L_done_%=\n\t"
      "buffer_load_dword %0, %3, %4, 0 offen sc1\n\t"
      "s_waitcnt vmcnt(1)\n\t"
      "v_cmp_ne_u32 vcc, %5, %1\n\t"
      "s_nop 1\n\t"
      "s_and_b64 vcc, vcc, %6\n\t"
      "s_cbranch_vccz L_done_%=\n\t"
      "s_sub_u32 %2, %2, 1\n\t"
      "s_cmp_lg_u32 %2, 0\n\t"
      "s_cbranch_scc1 L_poll_%=\n"
      "L_done_%=:"
      : "=&v"(p0), "=&v"(p1), "+s"(cnt) : "v"(off), "s"(r), "s"(tag), "s"(mask) : "vcc", "scc", "memory");
}
DI void st_tag(rsrc_t r, unsigned line, unsigned tag, int lane) { st_sc1(u32x4{tag, tag, tag, tag}, r, lane < 8 ? line + lane * 16 : OOB); }

DI int krow(int rho) { return 8 * ((rho >> 2) & 3) + 4 * (rho >> 4) + (rho & 3); }

template <bool ISB> DI void feature_map(const f32x4 (&P)[5], unsigned (&ph)[ISB ? 18 : 10]) {
  float p[18];
#pragma unroll
  for (int f = 0; f < 18; ++f) p[f] = P[f >> 2][f & 3];
  if constexpr (ISB) {
    float mx = fmax3(p[0], p[1], p[2]), mn = fmin3(p[0], p[1], p[2]);
#pragma unroll
    for (int f = 3; f < 17; f += 2) { mx = fmax3(mx, p[f], p[f + 1]); mn = fmin3(mn, p[f], p[f + 1]); }
    mx = fmax3(mx, p[17], p[17]); mn = fmin3(mn, p[17], p[17]);
    const f32x2 cp = {-mx * LOG2E, -mx * LOG2E}, cn = {mn * LOG2E, mn * LOG2E};
    f32x2 lp = {LOG2E, LOG2E}, ln = {-LOG2E, -LOG2E};
    asm("" : "+v"(lp), "+v"(ln));
    f32x2 ep[9], en[9];
#pragma unroll
    for (int f = 0; f < 9; ++f) {
      const f32x2 x = {p[2 * f], p[2 * f + 1]};
      const f32x2 a = __builtin_elementwise_fma(x, lp, cp), b = __builtin_elementwise_fma(x, ln, cn);
      ep[f] = f32x2{__builtin_amdgcn_exp2f(a[0]), __builtin_amdgcn_exp2f(a[1])};
      en[f] = f32x2{__builtin_amdgcn_exp2f(b[0]), __builtin_amdgcn_exp2f(b[1])};
    }
    f32x2 sp = ep[0], sn = en[0];
#pragma unroll
    for (int f = 1; f < 9; ++f) { sp += ep[f]; sn += en[f]; }
    const float rp = __builtin_amdgcn_rcpf(sp[0] + sp[1]), rn = __builtin_amdgcn_rcpf(sn[0] + sn[1]);
#pragma unroll
    for (int f = 0; f < 9; ++f) {
      const f32x2 a = ep[f] * rp, b = en[f] * rn;
      ph[f] = pk2(a[0], a[1]);
      ph[9 + f] = pk2(b[0], b[1]);
    }
  } else {
    f32x2 l2 = {LOG2E, LOG2E}, eps = {1e-6f, 1e-6f};
    asm("" : "+v"(l2), "+v"(eps));
#pragma unroll
    for (int f = 0; f < 9; ++f) {
      const f32x2 lo = f32x2{fminf(p[2 * f], 0.f), fminf(p[2 * f + 1], 0.f)} * l2;
      const f32x2 hi = f32x2{fmaxf(p[2 * f], 0.f), fmaxf(p[2 * f + 1], 0.f)};
      const f32x2 y = (hi + f32x2{__builtin_amdgcn_exp2f(lo[0]), __builtin_amdgcn_exp2f(lo[1])}) + eps;
      ph[f] = pk2(y[0], y[1]);
    }
    ph[9] = 0u;
  }
}

struct WF { bf16x8 a[5][2]; u32x2 h[5]; };
DI void load_wf_tile(LAS char* lds, int b0, int b1, WF& f, int t) {
  f.a[t][0] = lds128(lds, b0 + (2 * t) * 1024);
  f.a[t][1] = lds128(lds, b0 + (2 * t + 1) * 1024);
  f.h[t] = *(LAS u32x2*)(lds + b1 + t * 512);
}
DI void logits2(const WF& f, const f32x4 (&t0)[2], const f32x4 (&t1)[2], const f32x4 (&t2)[2], const f32x4 (&t3)[2],
                const f32x4 (&t4)[2], f32x4 (&P)[2][5]) {
#pragma unroll
  for (int m = 0; m < 2; ++m) {
    const bf16x8 b0 = pack_pair(t0[m], t1[m]), b1 = pack_pair(t2[m], t3[m]);
    const u32x2 b2 = u32x2{pk2(t4[m][0], t4[m][1]), pk2(t4[m][2], t4[m][3])};
#pragma unroll
    for (int t = 0; t < 5; ++t) P[m][t] = mfma(f.a[t][0], b0, zero4());
#pragma unroll
    for (int t = 0; t < 5; ++t) P[m][t] = mfma(f.a[t][1], b1, P[m][t]);
#pragma unroll
    for (int t = 0; t < 5; ++t) P[m][t] = mfma16(f.h[t], b2, P[m][t]);
  }
}
DI int wf_offset(int h, int d, int f) {
  const int col = 18 * h + d, s = col >> 5, ln = 16 * ((col >> 2) & 3) + 4 * h + (f & 3), tt = f >> 2;
  return s < 2 ? (tt * 2 + s) * 1024 + ln * 16 + (((col >> 4) & 1) * 4 + (col & 3)) * 2 : 10240 + tt * 512 + ln * 8 + (col & 3) * 2;
}
template <int N> DI void cwait_lgkm() { __builtin_amdgcn_s_waitcnt(0xC07F | (N << 8)); }
DI void step5(LAS char* lds, f32x4 (&acc)[5][2], const bf16x8 (&wc)[5], bf16x8 (&wn)[5], bf16x8 x0, bf16x8 x1, int next, bool more) {
#pragma unroll
  for (int j = 0; j < 10; ++j) {
    if (j == 6) { if (more) cwait_lgkm<10>(); else cwait_lgkm<0>(); }
    mfma_acc(acc[j >> 1][j & 1], wc[j >> 1], (j & 1) ? x1 : x0);
    if (more && j < 5) wn[j] = lds_tr(lds, next + j * 32, 16 * 160);
    SBAR();
  }
}

template <bool ISB> DI void c_path(const Args& a, LAS char* lds, const int w, const int lane0, const int t, const int g) {
  constexpr int prob = ISB ? 1 : 0;
  constexpr int RP = ISB ? 288 : 160;
  constexpr int C5 = ISB ? 5 : 3;
  constexpr int KVROW = C5 * 16, KVHEAD = ISB ? 1536 : 1024, NCH = 19 * C5;
  char* ws = a.ws;
  const rsrc_t rws = mk_rsrc(ws, WS_USED);
  const unsigned kvp_grp = WS_KVP + (prob * 8 + g) * 16 * KVP_STRIDE;
  const unsigned obuf = WS_O + (prob * 16 + t) * O_STRIDE;
  constexpr int NU = ISB ? 9 : 5, UMAIN = NU - 1;
  f32x4 acc[9][2];
  WF wf;
  unsigned T;
  int a_v[5][2], a_one, a_prow, a_pm, a_pl, a_b0, a_b1, a_mu, a_al, a_la;
  unsigned o_p0, o_p1, o_mu, o_ml;
  STAMP_DECL
  {
    const int lane = lane0, tid = w * 64 + lane, i = lane & 15, gg = lane >> 4;
    STAMP(0); CSTAMP(43);
    T = __hip_atomic_load((GAS unsigned*)(ws + WS_E + prob * 256), RLX_AGENT) + 1u;
    unsigned tl0 = 0u, tl1 = 0u, tl2 = 0u;
    if (TLBW && w == 3) {
      const unsigned wsoff = lane < 3 ? obuf + lane * 65536 : lane < 5 ? kvp_grp + (lane - 3) * 65536
                           : lane == 5 ? WS_H1 + (prob * 8 + g) * 8192 : lane == 6 ? WS_H2 + (prob * 16 + t) * 4096 : OOB;
      tl0 = __builtin_amdgcn_raw_buffer_load_b32(rws, wsoff, 0, 0);
      tl1 = __builtin_amdgcn_raw_buffer_load_b32(mk_rsrc(a.out, 2048 * OUTROW), lane < 6 ? t * 128 * OUTROW + min(lane * 65536, 128 * OUTROW - 4) : OOB, 0, 0);
      tl2 = lane < 32 ? __builtin_amdgcn_raw_buffer_load_b32(mk_rsrc(ISB ? a.wB : a.wA, 576 * WROW), lane * 65536, 0, 0)
                      : __builtin_amdgcn_raw_buffer_load_b32(mk_rsrc(ISB ? a.woB : a.woA, 576 * XROW), (lane - 32) * 65536, 0, 0);
    }
    unsigned short wel[6];
    if constexpr (ISB) {
      const rsrc_t rp = mk_rsrc(a.wphi, 32 * 324 * 2);
#pragma unroll
      for (int k = 0; k < 6; ++k) {
        const int idx = lane + 64 * k;
        wel[k] = __builtin_amdgcn_raw_buffer_load_b16(rp, idx < 324 ? ((4 * g + w) * 324 + idx) * 2 : OOB, 0, 0);
      }
    }
    {
      const u32x4 s = {SENT, SENT, SENT, SENT};
#pragma unroll
      for (int k = 0; k < (KVHEAD / 16 + 63) / 64; ++k) {
        const int n = lane + 64 * k;
        st_sc1(s, rws, n < KVHEAD / 16 ? kvp_grp + t * KVP_STRIDE + w * KVHEAD + n * 16 : OOB);
      }
    }
    if (tid < 64) *(LAS unsigned*)(lds + L_SYNC + tid * 4) = tid == SY_TAG ? T : 0u;
    if (tid < 32) *(LAS unsigned*)(lds + L_ZERO + tid * 4) = 0u;
#pragma unroll
    for (int n = 0; n < 9; ++n) { acc[n][0] = zero4(); acc[n][1] = zero4(); asm volatile("" : "+v"(acc[n][0]), "+v"(acc[n][1])); }
    const int xa0 = (32 * w + i) * 128 + ((gg ^ ((i >> 1) & 7)) * 16), xa1 = xa0 ^ 64;
    const int wa = WPART + (4 * gg + (i >> 2)) * 288 + (i & 3) * 8;
    const int wm = wa + ((i & 2) ? 256 : 128);
    {
      const int xs = ((i >> 2) & 3) << 3;
      const int vrow = L_V + (32 * w + i) * 160;
#pragma unroll
      for (int n = 0; n < 5; ++n) {
#pragma unroll
        for (int pp = 0; pp < 2; ++pp) {
          const int c0 = (n < 4 ? 16 * n : 56) + 2 * pp, h0 = c0 / 18;
          const int thr = (18 * (h0 + 1) - c0 + 3) / 4;
          a_v[n][pp] = opaque(vrow + ((2 * c0 + 4 * h0 + 8 * gg + ((thr < 4 && gg >= thr) ? 4 : 0)) ^ xs));
        }
      }
      a_one = opaque(vrow + ((40 * gg + 36) ^ xs));
      a_prow = opaque(L_PHI + (32 * w + i) * RP);
      a_pm = opaque((UMAIN * gg * 8) ^ xs);
      a_pl = opaque(L_PHI + (32 * w + i) * RP + (((4 * UMAIN + gg) * 8) ^ xs));
      const int q = i >> 2, p = i & 3;
      const int pa = L_PHI + (4 * gg + q) * RP, va = L_V + (4 * gg + q) * 160;
      a_b0 = opaque(pa + ((UMAIN * w + p) ^ gg) * 8); a_b1 = opaque(pa + ((UMAIN * w + 4 + p) ^ gg) * 8);
      a_mu = opaque(pa + ((4 * UMAIN + p) ^ gg) * 8);
      a_al = opaque(va + ((5 * w + p) ^ gg) * 8); a_la = opaque(va + ((5 * p + 4) ^ gg) * 8);
      const unsigned mine = kvp_grp + t * KVP_STRIDE + w * KVHEAD;
      const bool lam = (i >> 2) == w && (i & 3) < 3;
      const int el = 16 + (i & 3), ch = ISB ? ((gg & 1) << 1) | (gg >> 1) : gg >> 1;
      const unsigned o_main = mine + i * KVROW + ch * 16, o_lam = lam ? mine + el * KVROW + ch * 16 : OOB;
      o_p0 = opaque(ISB || !(gg & 1) ? o_main : o_lam); o_p1 = opaque(o_lam);
      o_mu = opaque(gg == w ? mine + i * KVROW + (C5 - 1) * 16 : OOB);
      o_ml = opaque((gg == w && lam) ? mine + el * KVROW + (C5 - 1) * 16 : OOB);
    }
    int a_wf[6];
    {
      const u32x4 z = {0u, 0u, 0u, 0u};
#pragma unroll
      for (int k = 0; k < 3; ++k) {
        const int n = lane + 64 * k, fr = n >> 4, sl = n & 15;
        if (n < 160) *(LAS u32x4*)(lds + L_WF + fr * 1024 + (16 * (sl >> 2) + 4 * w + (sl & 3)) * 16) = z;
      }
#pragma unroll
      for (int k = 0; k < 2; ++k) {
        const int n = lane + 64 * k, fr = n >> 4, sl = n & 15;
        if (n < 80) *(LAS u32x2*)(lds + L_WF + 10240 + fr * 512 + (16 * (sl >> 2) + 4 * w + (sl & 3)) * 8) = u32x2{0u, 0u};
      }
      if constexpr (ISB) {
#pragma unroll
        for (int k = 0; k < 6; ++k) {
          const int idx = lane + 64 * k, d = idx / 18, f = idx - d * 18;
          a_wf[k] = opaque(idx < 324 ? L_WF + wf_offset(w, d, f) : L_ZERO + 124);
        }
      } else if (lane < 18) {
        *(LAS unsigned short*)(lds + L_WF + wf_offset(w, lane, lane)) = (unsigned short)0x3F80;
      }
    }
    bf16x8 wb[2][9], xb[2][2];
    auto kstep = [&](auto SC, int xo, int wo, int wmo) {
      constexpr int S = decltype(SC)::value, N = S ^ 1;
#pragma unroll
      for (int n = 0; n < 9; ++n) {
        mfma_acc(acc[n][0], wb[S][n], xb[S][0]);
        if (n == 0) { xb[N][0] = lds128(lds, xo); xb[N][1] = lds128(lds, xo + 2048); }
        else if (n <= 4) wb[N][2 * n - 1] = lds_tr(lds, 2 * n - 1 < 8 ? wo + (2 * n - 1) * 32 + (2 * n - 1 >= 4 ? 16 : 0) : wmo, 4608);
        mfma_acc(acc[n][1], wb[S][n], xb[S][1]);
        if (n <= 4) wb[N][2 * n] = lds_tr(lds, 2 * n < 8 ? wo + (2 * n) * 32 + (2 * n >= 4 ? 16 : 0) : wmo, 4608);
        SBAR();
      }
    };
    using I0 = std::integral_constant<int, 0>;
    using I1 = std::integral_constant<int, 1>;
    STAMP(1);
    barrier();
    STAMP(2); CSTAMP(44);
    {
      const int xo = opaque(xa0 + 3 * SLOT), wo = opaque(wa + 3 * SLOT), wmo = opaque(wm + 3 * SLOT);
      xb[0][0] = lds128(lds, xo); xb[0][1] = lds128(lds, xo + 2048);
#pragma unroll
      for (int n = 0; n < 9; ++n) wb[0][n] = lds_tr(lds, n < 8 ? wo + n * 32 + (n >= 4 ? 16 : 0) : wmo, 4608);
    }
    int base = 3 * SLOT;
    unsigned pfq = 0u;
#pragma unroll
    for (int c = 0; c < 8; ++c) {
      {
        const int xo = opaque(xa1 + base), wo = opaque(wa + base + 9216), wmo = opaque(wm + base + 9216);
        cwait_lgkm0();
        SBAR();
        kstep(I0{}, xo, wo, wmo);
      }
      if (PFQ && c == 1) {
        const int row = 18 * (blockIdx.x >> 3) + tid / 9, ln = tid - 9 * (tid / 9);
        pfq = __builtin_amdgcn_raw_buffer_load_b32(mk_rsrc(ISB ? a.wB : a.wA, 576 * WROW), tid < 162 ? row * WROW + ln * 128 : OOB, 0, 0);
      }
      if constexpr (ISB) {
        if (c == 3) {
#pragma unroll
          for (int k = 0; k < 6; ++k) *(LAS unsigned short*)(lds + a_wf[k]) = wel[k];
        }
      }
      if (c == 5) { asm volatile("" :: "v"(pfq), "v"(tl0), "v"(tl1), "v"(tl2)); wait_vm<0>(); }
      cwait_lgkm0();
      barrier();
      base = base == 3 * SLOT ? 0 : base + SLOT;
      {
        const int xo = opaque(xa0 + base), wo = opaque(wa + base), wmo = opaque(wm + base);
        SBAR();
        kstep(I1{}, xo, wo, wmo);
      }
    }
    {
      const int xo = opaque(xa1 + base), wo = opaque(wa + base + 9216), wmo = opaque(wm + base + 9216);
      cwait_lgkm0();
      SBAR();
      kstep(I0{}, xo, wo, wmo);
      cwait_lgkm0();
      lds_arrive(lds, SY_KV8);
      const int b0 = opaque(L_WF + lane * 16), b1 = opaque(L_WF + 10240 + lane * 8);
      SBAR();
#pragma unroll
      for (int n = 0; n < 9; ++n) {
        mfma_acc(acc[n][0], wb[1][n], xb[1][0]);
        if (n < 5) load_wf_tile(lds, b0, b1, wf, n);
        mfma_acc(acc[n][1], wb[1][n], xb[1][1]);
        SBAR();
      }
      lds_wait(lds, SY_KV8, 8u);
      mfma_fence(acc);
    }
  }
  const int lane = opaque(lane0), tid = w * 64 + lane, i = lane & 15, gg = lane >> 4;
  const unsigned Ts = __builtin_amdgcn_readfirstlane(T);
  __builtin_amdgcn_s_setprio(2);
  STAMP(3); CSTAMP(45);

  {
    f32x4 P[2][5];
    bf16x8 kb[2][2];
    u32x2 kb2[2];
#pragma unroll
    for (int m = 0; m < 2; ++m) {
      kb[m][0] = pack_pair(acc[0][m], acc[1][m]); kb[m][1] = pack_pair(acc[2][m], acc[3][m]);
      kb2[m] = u32x2{pk2(acc[8][m][0], acc[8][m][1]), pk2(acc[8][m][2], acc[8][m][3])};
    }
    SBAR();
#pragma unroll
    for (int st = 0; st < 30; ++st) {
      const int m = st / 15, s = (st % 15) / 5, tt = st % 5;
      if (s == 0) P[m][tt] = mfma(wf.a[tt][0], kb[m][0], zero4());
      if (s == 1) P[m][tt] = mfma(wf.a[tt][1], kb[m][1], P[m][tt]);
      if (s == 2) P[m][tt] = mfma16(wf.h[tt], kb2[m], P[m][tt]);
      if (st < 20) {
        const int n = st >> 2, pp = (st >> 1) & 1, mv = st & 1;
        const f32x4 v = n < 4 ? acc[4 + n][mv] : acc[8][mv];
        const int am = n < 4 ? a_v[n][pp] + mv * 2560 : (gg >= 2 ? a_v[n][pp] + mv * 2560 : L_ZERO + 124);
        *(LAS unsigned*)(lds + am) = pk2(v[2 * pp], v[2 * pp + 1]);
      }
      SBAR();
    }
    *(LAS unsigned*)(lds + a_one) = 0x00003F80u; *(LAS unsigned*)(lds + a_one + 2560) = 0x00003F80u;
    FSTAMP(18);
#pragma unroll
    for (int m = 0; m < 2; ++m) {
      unsigned ph[ISB ? 18 : 10];
      feature_map<ISB>(P[m], ph);
#pragma unroll
      for (int j = 0; j < NU; ++j)
        *(LAS u32x2*)(lds + (j < UMAIN ? a_prow + (a_pm ^ (j * 8)) : a_pl) + m * 16 * RP) = u32x2{ph[2 * j], ph[2 * j + 1]};
      FSTAMP(19 + m);
    }
  }
  STAMP(4);
  lds_arrive(lds, SY_PHI);
  lds_wait(lds, SY_PHI);
  STAMP(5);

  {
    bf16x8 al[4], la[4], b0[4], mu[4], b1[4];
    f32x4 d0 = zero4(), d1 = zero4(), d2 = zero4(), d3 = zero4(), d4 = zero4(), d5 = zero4();
    asm volatile("" : "+v"(d0), "+v"(d1), "+v"(d2), "+v"(d3), "+v"(d4), "+v"(d5));
    auto rd = [&](int ks) {
      al[ks] = lds_tr(lds, a_al + ks * 32 * 160, 16 * 160);
      b0[ks] = lds_tr(lds, a_b0 + ks * 32 * RP, 16 * RP);
      if constexpr (ISB) b1[ks] = lds_tr(lds, a_b1 + ks * 32 * RP, 16 * RP);
      la[ks] = lds_tr(lds, a_la + ks * 32 * 160, 16 * 160);
      mu[ks] = lds_tr(lds, a_mu + ks * 32 * RP, 16 * RP);
      SBAR();
    };
    auto mm = [&](int ks) {
      mfma_acc(d0, b0[ks], al[ks]);
      if constexpr (ISB) mfma_acc(d1, b1[ks], al[ks]);
      mfma_acc(d2, b0[ks], la[ks]);
      if constexpr (ISB) mfma_acc(d3, b1[ks], la[ks]);
      mfma_acc(d4, mu[ks], al[ks]); mfma_acc(d5, mu[ks], la[ks]);
      SBAR();
    };
    constexpr int RD = ISB ? 10 : 8;
    asm volatile("s_nop 1");
    rd(0); rd(1);
    __builtin_amdgcn_s_waitcnt(0xC07F & ~0xF00 | (RD << 8)); SBAR(); mm(0); rd(2);
    __builtin_amdgcn_s_waitcnt(0xC07F & ~0xF00 | (RD << 8)); SBAR(); mm(1); rd(3);
    __builtin_amdgcn_s_waitcnt(0xC07F & ~0xF00 | (RD << 8)); SBAR(); mm(2);
    cwait_lgkm0(); SBAR(); mm(3);
    asm volatile("s_nop 7\n\ts_nop 3" : "+v"(d0), "+v"(d1), "+v"(d2), "+v"(d3), "+v"(d4), "+v"(d5));
    if constexpr (ISB) {
      unsigned x0 = pk2(d0[0], d0[1]), x1 = pk2(d0[2], d0[3]), y0 = pk2(d1[0], d1[1]), y1 = pk2(d1[2], d1[3]);
      swap16(x0, y0); swap16(x1, y1);
      st_sc1(u32x4{nosent(x0), nosent(x1), nosent(y0), nosent(y1)}, rws, o_p0);
      x0 = pk2(d2[0], d2[1]); x1 = pk2(d2[2], d2[3]); y0 = pk2(d3[0], d3[1]); y1 = pk2(d3[2], d3[3]);
      swap16(x0, y0); swap16(x1, y1);
      st_sc1(u32x4{nosent(x0), nosent(x1), nosent(y0), nosent(y1)}, rws, o_p1);
    } else {
      unsigned x0 = pk2(d0[0], d0[1]), x1 = pk2(d0[2], d0[3]), y0 = pk2(d2[0], d2[1]), y1 = pk2(d2[2], d2[3]);
      swap16(x0, y0); swap16(x1, y1);
      st_sc1(u32x4{nosent(x0), nosent(x1), nosent(y0), nosent(y1)}, rws, o_p0);
    }
    st_sc1(u32x4{nosent(pk2(d4[0], d4[1])), nosent(pk2(d4[2], d4[3])), 0u, 0u}, rws, o_mu);
    st_sc1(u32x4{nosent(pk2(d5[0], d5[1])), nosent(pk2(d5[2], d5[3])), 0u, 0u}, rws, o_ml);
    st_tag(rws, WS_H1 + (((prob * 8 + g) * 16 + t) * 4 + w) * 128, T, lane);
    lds_arrive(lds, SY_KVDONE);
  }
  __builtin_amdgcn_s_setprio(0);
  STAMP(6); CSTAMP(46);
  const rsrc_t rwo = mk_rsrc(ISB ? a.woB : a.woA, 576 * XROW);
  {
    STAMP(7);
    const rsrc_t rkv = mk_rsrc(ws + kvp_grp, 16 * KVP_STRIDE);
    constexpr int NG = (NCH + 15) / 16;
    const unsigned one = (i & 1) ? 0x3F800000u : 0x3F80u;
    const bf16x8 sel = frag_of((i >> 1) == 0 ? one : 0u, (i >> 1) == 1 ? one : 0u, (i >> 1) == 2 ? one : 0u, (i >> 1) == 3 ? one : 0u);
    const unsigned off0 = gg * KVP_STRIDE + w * KVHEAD + i * 16;
    const unsigned offl = 16 * (NG - 1) + i < NCH ? off0 + (NG - 1) * 256 : OOB;
    const int kvw = L_PHI + w * KVHEAD + i * 16 + gg * 8;
    unsigned pfo = 0u;
    if (PFO) {
      const int row = 18 * (blockIdx.x >> 3) + tid / 9, ln = tid - 9 * (tid / 9);
      asm volatile("buffer_load_dword %0, %1, %2, 0 offen" : "=v"(pfo) : "v"(tid < 162 ? row * XROW + ln * 128 : OOB), "s"(rwo) : "memory");
    }
    unsigned p0, p1;
    poll_tag(rws, lane < 16 ? WS_H1 + (((prob * 8 + g) * 16 + lane) * 4 + w) * 128 : OOB, Ts, 0xFFFFull, p0, p1);
    __builtin_amdgcn_s_setprio(2);
    STAMP(8);
    f32x4 sum[NG];
    {
      u32x4 v[NG][4];
#pragma unroll
      for (int cg = 0; cg < NG; ++cg) {
        const unsigned off = cg < NG - 1 ? off0 + cg * 256 : offl;
#pragma unroll
        for (int u = 0; u < 4; ++u)
          asm volatile("buffer_load_dwordx4 %0, %1, %2, %3 offen sc1" : "=v"(v[cg][u]) : "v"(off), "s"(rkv), "s"(4 * u * KVP_STRIDE) : "memory");
      }
      {
        const u32x4 s = {SENT, SENT, SENT, SENT};
#pragma unroll
        for (int k = 0; k < 5; ++k) {
          const int n = lane + 64 * k;
          st_sc1(s, rws, n < 288 ? obuf + (2 * w * 72 + 9 * g) * 256 + n * 16 + (n >= 144 ? (72 - 9) * 256 : 0) : OOB);
        }
        SBAR();
      }
      float chk = 0.f;
#pragma unroll
      for (int cg = 0; cg < NG; ++cg) {
        asm volatile("s_waitcnt vmcnt(%7)" : "+v"(v[cg][0]), "+v"(v[cg][1]), "+v"(v[cg][2]), "+v"(v[cg][3]), "+v"(p0), "+v"(p1), "+v"(pfo)
                     : "n"((NG - 1 - cg) * 4 + 5));
        SBAR();
        f32x4 d = zero4();
#pragma unroll
        for (int u = 0; u < 4; ++u) d = mfma(sel, __builtin_bit_cast(bf16x8, v[cg][u]), d);
        sum[cg] = d;
        chk += d[0];
      }
      FSTAMP(21);
      if (!__all(chk == chk)) {
        PCOUNT(22);
#pragma unroll 1
        for (int sp = 0; sp < SPIN_G; ++sp) {
          unsigned mx = 0u;
#pragma unroll 1
          for (int n = 0; n < NG * 4; ++n) {
            asm volatile("" ::: "memory");
            const u32x4 x = ld_sc1(rkv, (n >> 2) < NG - 1 ? off0 + (n >> 2) * 256 : offl, 4 * (n & 3) * KVP_STRIDE);
            mx = umax3(mx, x[0], x[1]); mx = umax3(mx, x[2], x[3]);
          }
          if (__all(mx != SENT)) break;
        }
#pragma unroll
        for (int cg = 0; cg < NG; ++cg) {
          f32x4 d = zero4();
#pragma unroll
          for (int u = 0; u < 4; ++u)
            d = mfma(sel, __builtin_bit_cast(bf16x8, ld_sc1(rkv, cg < NG - 1 ? off0 + cg * 256 : offl, 4 * u * KVP_STRIDE)), d);
          sum[cg] = d;
        }
      }
    }
    lds_wait(lds, SY_KVDONE);
    if (gg < 2) {
#pragma unroll
      for (int cg = 0; cg < NG; ++cg)
        if (cg < NG - 1 || 16 * cg + i < NCH)
          *(LAS u32x2*)(lds + kvw + cg * 256) = u32x2{pk2(sum[cg][0], sum[cg][1]), pk2(sum[cg][2], sum[cg][3])};
    }
    lds_arrive(lds, SY_KVIMG);
    wait_vm<0>();
    lds_arrive(lds, SY_PRE);
  }
  STAMP(9);
  __builtin_amdgcn_s_setprio(0);
  STAMP(10);

  {
    const rsrc_t ro = mk_rsrc(ws + obuf, O_STRIDE);
    const rsrc_t rout = mk_rsrc(a.out, 2048 * OUTROW);
    const unsigned orow = (t * 128 + 32 * w + i) * OUTROW + prob * 1152 + 144 * g;
    const unsigned o_pair = orow + (2 * (gg & 1) + (gg >> 1)) * 16, o_last = gg < 2 ? orow + gg * 16 * OUTROW + 128 : OOB;
    const int wl = (4 * gg + (i >> 2)) * 160 + (i & 3) * 8;
    const int wa2 = opaque(L_WQ + wl), wa3 = opaque(L_WQ + wl + 12 * 5120);
    lds_wait(lds, SY_WO);
    unsigned p0, p1;
    poll_tag(rws, lane < 8 ? WS_H2 + (((prob * 16 + t) * 8 + lane) * 4 + w) * 128 : OOB, Ts, 0xFFull, p0, p1);
    __builtin_amdgcn_s_setprio(2);
    STAMP(11);
    f32x4 oc[5][2];
#pragma unroll 1
    for (int pass = 0; pass < 2; ++pass) {
      asm volatile("" ::: "memory");
      u32x4 of[18][2];
#pragma unroll
      for (int ks = 0; ks < OLOOK; ++ks) {
#pragma unroll
        for (int m = 0; m < 2; ++m) { of[ks][m] = ld_sc1(ro, lane * 16, ((2 * w + m) * 72 + 4 * ks) * 256); SBAR(); }
      }
      bf16x8 wq[2][5];
#pragma unroll
      for (int n = 0; n < 5; ++n) {
        wq[0][n] = lds_tr(lds, wa2 + n * 32, 16 * 160);
        oc[n][0] = zero4(); oc[n][1] = zero4();
        asm volatile("" : "+v"(oc[n][0]), "+v"(oc[n][1]));
      }
      asm volatile("s_nop 1");
      SBAR();
#define OUT_STEP(ks) do { cwait_vm_lgkm4<2 * (((ks) + OLOOK - 1 < 17 ? (ks) + OLOOK - 1 : 17) - (ks))>(); SBAR(); \
      if ((ks) + OLOOK < 18) { of[((ks) + OLOOK) % 18][0] = ld_sc1(ro, lane * 16, ((2 * w) * 72 + 4 * ((ks) + OLOOK)) * 256); \
                               of[((ks) + OLOOK) % 18][1] = ld_sc1(ro, lane * 16, ((2 * w + 1) * 72 + 4 * ((ks) + OLOOK)) * 256); SBAR(); } \
      step5(lds, oc, wq[(ks) & 1], wq[((ks) + 1) & 1], __builtin_bit_cast(bf16x8, of[ks][0]), __builtin_bit_cast(bf16x8, of[ks][1]), \
            (ks) + 1 < 12 ? wa2 + ((ks) + 1) * 5120 : wa3 + ((ks) + 1 - 12) * 5120, (ks) < 17); } while (0)
      OUT_STEP(0);
      asm volatile("" :: "v"(p0), "v"(p1));
      OUT_STEP(1); OUT_STEP(2); OUT_STEP(3); OUT_STEP(4); OUT_STEP(5); OUT_STEP(6); OUT_STEP(7); OUT_STEP(8);
      OUT_STEP(9); OUT_STEP(10); OUT_STEP(11); OUT_STEP(12); OUT_STEP(13); OUT_STEP(14); OUT_STEP(15); OUT_STEP(16); OUT_STEP(17);
#undef OUT_STEP
      mfma_fence(oc);
      const float chk = oc[0][0][0] + oc[0][1][0];
      if (pass == 1 || __all(chk == chk)) break;
      PCOUNT(23);
#pragma unroll 1
      for (int sp = 0; sp < SPIN_G; ++sp) {
        unsigned mx = 0u;
#pragma unroll 1
        for (int n = 0; n < 36; ++n) {
          asm volatile("" ::: "memory");
          const u32x4 x = ld_sc1(ro, lane * 16, ((2 * w + (n & 1)) * 72 + 4 * (n >> 1)) * 256);
          mx = umax3(mx, x[0], x[1]); mx = umax3(mx, x[2], x[3]);
        }
        if (__all(mx != SENT)) break;
      }
    }
    STAMP(12);
#pragma unroll
    for (int m = 0; m < 2; ++m) {
#pragma unroll
      for (int n = 0; n < 4; n += 2) {
        unsigned x0 = pk2(oc[n][m][0], oc[n][m][1]), x1 = pk2(oc[n][m][2], oc[n][m][3]);
        unsigned y0 = pk2(oc[n + 1][m][0], oc[n + 1][m][1]), y1 = pk2(oc[n + 1][m][2], oc[n + 1][m][3]);
        swap16(x0, y0); swap16(x1, y1);
        __builtin_amdgcn_raw_buffer_store_b128(u32x4{x0, x1, y0, y1}, rout, o_pair + m * 16 * OUTROW + n * 32, 0, 0);
      }
    }
    unsigned x0 = pk2(oc[4][0][0], oc[4][0][1]), x1 = pk2(oc[4][0][2], oc[4][0][3]);
    unsigned y0 = pk2(oc[4][1][0], oc[4][1][1]), y1 = pk2(oc[4][1][2], oc[4][1][3]);
    swap16(x0, y0); swap16(x1, y1);
    __builtin_amdgcn_raw_buffer_store_b128(u32x4{x0, x1, y0, y1}, rout, o_last, 0, 0);
    if (t == 0 && g == 0 && tid == 0) __hip_atomic_store((GAS unsigned*)(ws + WS_E + prob * 256), T, RLX_AGENT);
  }
  STAMP(13); CSTAMP(47);
  STAMP_OUT(w);
}

template <bool ISB> DI void h_path(const Args& a, LAS char* lds, const int w, const int lane, const int t, const int g) {
  constexpr int prob = ISB ? 1 : 0;
  constexpr int C5 = ISB ? 5 : 3;
  constexpr int KVROW = C5 * 16, KVHEAD = ISB ? 1536 : 1024;
  const int i = lane & 15, gg = lane >> 4;
  const rsrc_t rx = mk_rsrc((const char*)(ISB ? a.xB : a.xA) + (size_t)t * 128 * XROW, 128 * XROW);
  const rsrc_t rw = mk_rsrc(ISB ? a.wB : a.wA, 576 * WROW);
  const unsigned lb = (unsigned)(size_t)lds;
  STAMP_DECL
  STAMP(0);
  const int rot = ROT == 1 ? (g + 2 * (t & 3) + ((t >> 1) & 1)) % 9 : ROT == 2 ? (g + 8 * (t & 3)) % 9 : 0;
  auto kchunk = [&](int c) { const int k = c + rot; return k >= 9 ? k - 9 : k; };
  unsigned vx[2], vw[5], vq[8];
#pragma unroll
  for (int par = 0; par < 2; ++par) vx[par] = (lane >> 3) * XROW + (((lane & 7) ^ ((4 * par + (lane >> 4)) & 7)) * 16);
  auto issue_x = [&](int c) {
    const unsigned sb = lb + ((c + 3) & 3) * SLOT;
#pragma unroll
    for (int jj = 0; jj < 4; ++jj) dma16(rx, vx[jj & 1], (4 * w + jj) * 8 * XROW + kchunk(c) * 128, sb + (4 * w + jj) * 1024);
  };
  issue_x(0);
#pragma unroll
  for (int n = 0; n < 5; ++n) {
    const int nn = (w + 4 * n) * 64 + lane, r64 = nn / 18, cp = nn - 18 * r64;
    const int co = cp < 9 ? 1152 + 16 * cp : 2304 + 16 * (cp - 9);
    vw[n] = ((r64 & 32) + krow(r64 & 31)) * WROW + 144 * g + co;
  }
  auto issue_w = [&](int c) {
    const unsigned sb = lb + ((c + 3) & 3) * SLOT;
#pragma unroll
    for (int n = 0; n < 5; ++n)
      if (n < 4 || w < 2) dma16(rw, vw[n], kchunk(c) * 64 * WROW, sb + WPART + (w + 4 * n) * 1024);
  };
  issue_w(0); issue_x(1); issue_w(1);
  if (PRO >= 2) { issue_x(2); issue_w(2); }
  if (PRO >= 3) { issue_x(3); issue_w(3); }
  STAMP(1);
  if (PRO == 0) { if (w < 2) wait_vm<9>(); else wait_vm<8>(); }
  if (PRO == 2) { if (w < 2) wait_vm<18>(); else wait_vm<16>(); }
  if (PRO == 3) { if (w < 2) wait_vm<27>(); else wait_vm<24>(); }
  STAMP(2);
  barrier();
  if (PRO < 2) { issue_x(2); issue_w(2); }
#pragma unroll
  for (int n = 0; n < 8; ++n) {
    const int v = w + 4 * n, ks = v / 5, nn = (v - 5 * ks) * 64 + lane, rho = nn / 10, cp = nn - 10 * rho;
    vq[n] = ((ks & 1) * 32 + krow(rho)) * WROW + 144 * g + 16 * min(cp, 8);
  }
  auto issue_wq = [&](int grp) {
#pragma unroll
    for (int n = 0; n < 8; ++n)
      if (n < 7 || w < 2)
        dma16(rw, vq[n], kchunk(3 * grp + ((w + 4 * n) / 5 >> 1)) * 64 * WROW, lb + L_WQ + grp * 30720 + (w + 4 * n) * 1024);
  };
  bf16x8 X[18][2];
  const int xa0 = (32 * w + i) * 128 + ((gg ^ ((i >> 1) & 7)) * 16), xa1 = xa0 ^ 64;
#pragma unroll
  for (int c = 0; c < 9; ++c) {
    const int base = ((c + 3) & 3) * SLOT;
    const int xo0 = opaque(xa0 + base), xo1 = opaque(xa1 + base);
    X[2 * c][0] = lds128(lds, xo0); X[2 * c][1] = lds128(lds, xo0 + 2048);
    X[2 * c + 1][0] = lds128(lds, xo1); X[2 * c + 1][1] = lds128(lds, xo1 + 2048);
    if (c <= 5 && (c > 0 || PRO < 3)) { issue_x(c + 3); issue_w(c + 3); }
    LSTAMP(16 + c);
    if (c <= 5) { if (w < 2) wait_vm<18>(); else wait_vm<16>(); }
    if (c == 6) { if (w < 2) wait_vm<9>(); else wait_vm<8>(); }
    if (c == 7) wait_vm<0>();
    LSTAMP(25 + c);
    if (c < 8) barrier(); else lds_arrive(lds, SY_KV8);
    LSTAMP(34 + c);
  }
  issue_wq(0); issue_wq(1); issue_wq(2);
  __builtin_amdgcn_s_setprio(1);
  STAMP(3);
  f32x4 qa[5][2];
#pragma unroll
  for (int n = 0; n < 5; ++n) { qa[n][0] = zero4(); qa[n][1] = zero4(); asm volatile("" : "+v"(qa[n][0]), "+v"(qa[n][1])); }
  const int wa2 = opaque(L_WQ + (4 * gg + (i >> 2)) * 160 + (i & 3) * 8), wa3 = opaque(wa2 + 12 * 5120);
  bf16x8 wq[2][5];
  if (w < 2) wait_vm<16>(); else wait_vm<14>();
  lds_arrive(lds, SY_WQ0);
  lds_wait(lds, SY_WQ0);
  STAMP(4);
#pragma unroll
  for (int n = 0; n < 5; ++n) wq[0][n] = lds_tr(lds, wa2 + n * 32, 16 * 160);
#pragma unroll
  for (int ks = 0; ks < 18; ++ks) {
    cwait_lgkm<4>();
    if (ks == 5) { if (w < 2) wait_vm<8>(); else wait_vm<7>(); lds_arrive(lds, SY_WQ1); lds_wait(lds, SY_WQ1); STAMP(5); }
    if (ks == 11) { wait_vm<0>(); lds_arrive(lds, SY_WQ2); lds_wait(lds, SY_WQ2); STAMP(6); }
    SBAR();
    step5(lds, qa, wq[ks & 1], wq[(ks + 1) & 1], X[ks][0], X[ks][1], ks + 1 < 12 ? wa2 + (ks + 1) * 5120 : wa3 + (ks + 1 - 12) * 5120, ks < 17);
  }
  WF wf;
  {
    const int b0 = opaque(L_WF + lane * 16), b1 = opaque(L_WF + 10240 + lane * 8);
#pragma unroll
    for (int n = 0; n < 5; ++n) load_wf_tile(lds, b0, b1, wf, n);
  }
  mfma_fence(qa);
  lds_arrive(lds, SY_QDONE);
  STAMP(7);
  const unsigned T = *(LAS unsigned*)(lds + L_SYNC + SY_TAG * 4);
  unsigned ph[2][ISB ? 18 : 10];
  {
    f32x4 P[2][5];
    logits2(wf, qa[0], qa[1], qa[2], qa[3], qa[4], P);
    feature_map<ISB>(P[0], ph[0]);
    feature_map<ISB>(P[1], ph[1]);
#pragma unroll
    for (int j = 0; j < (ISB ? 18 : 10); ++j) asm volatile("" : "+v"(ph[0][j]), "+v"(ph[1][j]));
  }
  int a_kv[5], a_st[9];
  {
    const bool own = (i >> 2) == gg;
#pragma unroll
    for (int n = 0; n < 5; ++n) {
      const int e = 4 * n + (i & 3);
      a_kv[n] = opaque((own && e < 19) ? L_PHI + gg * KVHEAD + e * KVROW : L_ZERO);
    }
#pragma unroll
    for (int pp = 0; pp < 9; ++pp) {
      const int cp = 9 * gg + pp;
      a_st[pp] = opaque(L_V + w * 4608 + (cp >> 2) * 256 + i * 16 + (cp & 3) * 4);
    }
  }
  const rsrc_t ro = mk_rsrc(a.ws + WS_O + (prob * 16 + t) * O_STRIDE, O_STRIDE);
  const unsigned o_dst = (2 * w * 72 + 9 * g) * 256 + lane * 16;
  const rsrc_t rwo = mk_rsrc(ISB ? a.woB : a.woA, 576 * XROW);
  unsigned vo[5];
#pragma unroll
  for (int n = 0; n < 5; ++n) {
    const int nn = ((w + 4 * n) % 5) * 64 + lane, rho = nn / 10, cp = nn - 10 * rho;
    vo[n] = (unsigned)opaque(krow(rho) * XROW + 144 * g + 16 * min(cp, 8));
  }
  lds_wait(lds, SY_QDONE);
#pragma unroll
  for (int n = 0; n < WO_FIRST; ++n) { const int v = w + 4 * n; dma16(rwo, vo[n % 5], (v / 5) * 32 * XROW, lb + L_WQ + v * 1024); }
  STAMP(8);
  lds_wait(lds, SY_KVIMG);
  __builtin_amdgcn_s_setprio(3);
  STAMP(9);
  f32x4 O[5][2];
  {
    bf16x8 af[5][C5];
#pragma unroll
    for (int s = 0; s < C5; ++s) {
#pragma unroll
      for (int n = 0; n < 5; ++n) af[n][s] = lds128(lds, a_kv[n] + s * 16);
    }
    SBAR();
    constexpr int NP = ISB ? 18 : 10;
#pragma unroll
    for (int s = 0; s < C5; ++s) {
      bf16x8 bq[2];
#pragma unroll
      for (int m = 0; m < 2; ++m) {
        unsigned u[4];
#pragma unroll
        for (int j = 0; j < 4; ++j) u[j] = 4 * s + j < NP ? ph[m][4 * s + j < NP ? 4 * s + j : 0] : 0u;
        bq[m] = frag_of(u[0], u[1], u[2], u[3]);
      }
#pragma unroll
      for (int n = 4; n >= 0; --n) {
#pragma unroll
        for (int m = 0; m < 2; ++m) O[n][m] = mfma(af[n][s], bq[m], s == 0 ? zero4() : O[n][m]);
      }
    }
  }
  STAMP(10);
#pragma unroll
  for (int m = 0; m < 2; ++m) {
    const float rc = __builtin_amdgcn_rcpf(O[4][m][2] + 1e-6f);
    const f32x2 rc2 = {rc, rc};
#pragma unroll
    for (int pp = 0; pp < 9; ++pp) {
      const int e = 2 * pp;
      const f32x2 y = f32x2{O[e >> 2][m][e & 3], O[(e + 1) >> 2][m][(e + 1) & 3]} * rc2;
      *(LAS unsigned*)(lds + a_st[pp] + m * 2304) = nosent(pk2(y[0], y[1]));
    }
  }
  lds_wait(lds, SY_PRE);
  {
    const int sb = L_V + w * 4608 + lane * 16;
    u32x4 v[5];
#pragma unroll
    for (int k = 0; k < 5; ++k) v[k] = *(LAS u32x4*)(lds + sb + (k < 4 || lane < 32 ? k * 1024 : 0));
#pragma unroll
    for (int k = 0; k < 5; ++k) {
      const int n = lane + 64 * k;
      st_sc1(v[k], ro, n < 288 ? o_dst + k * 1024 + (n >= 144 ? (72 - 9) * 256 : 0) : OOB);
    }
    st_tag(mk_rsrc(a.ws, WS_USED), WS_H2 + (((prob * 16 + t) * 8 + g) * 4 + w) * 128, T, lane);
  }
  STAMP(11);
#pragma unroll
  for (int n = WO_FIRST; n < 23; ++n) {
    const int v = w + 4 * n;
    if (n < 22 || w < 2) dma16(rwo, vo[n % 5], (v / 5) * 32 * XROW, lb + L_WQ + v * 1024);
  }
  wait_vm<0>();
  lds_arrive(lds, SY_WO);
  STAMP(12);
  STAMP_OUT(4 + w);
}

__global__ __launch_bounds__(512, 2) void hedgehog_fused(Args a) {
  extern __shared__ __attribute__((aligned(16))) char smem[];
  LAS char* lds = (LAS char*)smem;
  const int b = blockIdx.x, xcd = b & 7, r = b >> 3;
  const int prob = xcd >> 2, t = (xcd & 3) * 4 + (r >> 3), g = r & 7;
  const int wave = __builtin_amdgcn_readfirstlane(threadIdx.x >> 6), lane = threadIdx.x & 63;
  unsigned cpf = 0u;
  if (CPF) {
    unsigned long long pc;
    asm volatile("s_getpc_b64 %0" : "=s"(pc));
    cpf = *(const GAS unsigned*)((pc & ~127ull) + threadIdx.x * 128u);
  }
  if ((wave < 4) != (HFIRST != 0)) {
    if (prob) c_path<true>(a, lds, wave & 3, lane, t, g); else c_path<false>(a, lds, wave & 3, lane, t, g);
  } else {
    if (prob) h_path<true>(a, lds, wave & 3, lane, t, g); else h_path<false>(a, lds, wave & 3, lane, t, g);
  }
  asm volatile("" :: "v"(cpf));
}

}

extern "C" void kernel_launch(void* const* d_in, const int* in_sizes, int n_in, void* d_out, int out_size, void* d_ws,
                              size_t ws_size, hipStream_t stream) {
  (void)in_sizes; (void)n_in; (void)out_size; (void)ws_size;
  Args a;
  a.xA = d_in[0]; a.wA = d_in[1]; a.woA = d_in[2]; a.xB = d_in[3]; a.wB = d_in[4]; a.wphi = d_in[5]; a.woB = d_in[6];
  a.out = d_out; a.ws = (char*)d_ws;
  (void)hipFuncSetAttribute((const void*)hedgehog_fused, hipFuncAttributeMaxDynamicSharedMemorySize, LDS_BYTES);
  hedgehog_fused<<<256, 512, LDS_BYTES, stream>>>(a);
}
```

```cpp
#include <hip/hip_runtime.h>
#include <hip/hip_bf16.h>
#include <stdint.h>
#include <type_traits>

typedef __bf16 bf16x8 __attribute__((ext_vector_type(8)));
typedef __bf16 bf16x4 __attribute__((ext_vector_type(4)));
typedef __bf16 bf16x2 __attribute__((ext_vector_type(2)));
typedef short s16x4 __attribute__((ext_vector_type(4)));
typedef float f32x4 __attribute__((ext_vector_type(4)));
typedef float f32x2 __attribute__((ext_vector_type(2)));
typedef unsigned u32x4 __attribute__((ext_vector_type(4)));
typedef unsigned u32x2 __attribute__((ext_vector_type(2)));
#define LAS __attribute__((address_space(3)))
#define GAS __attribute__((address_space(1)))
#define DI __device__ __forceinline__
#define RLX_AGENT __ATOMIC_RELAXED, __HIP_MEMORY_SCOPE_AGENT
#define SBAR() __builtin_amdgcn_sched_barrier(0)
typedef __amdgpu_buffer_rsrc_t rsrc_t;

#define DBG 0
#define PROF 0
#define STAMP(n)
#define CSTAMP(n)
#define STAMP_DECL
#define STAMP_OUT(wv)
#define PCOUNT(n)
#define LSTAMP(n)
#define FSTAMP(n)
#define PFQ 0
#define PFO 1
#define WO_FIRST 11
#define TLBW 0
#define PRO 0
#define CPF 0
#define HFIRST 0
#define OLOOK 4
#define XFIRST 2
#define KV_UNROLL 1
#define ROT 1

namespace {

constexpr int XROW = 1152;
constexpr int WROW = 3456;
constexpr int OUTROW = 2304;
constexpr int SLOT = 34816, WPART = 16384;
constexpr int L_WQ = 0;
constexpr int L_PHI = 92160;
constexpr int L_V = 129024;
constexpr int L_WF = 149504;
constexpr int L_SYNC = 162304;
constexpr int L_ZERO = 162560;
constexpr int LDS_BYTES = 163840;
enum { SY_WQ0 = 0, SY_WQ1, SY_WQ2, SY_QDONE, SY_PHI, SY_KVDONE, SY_KVIMG, SY_WO, SY_KV8, SY_PRE, SY_TAG = 63 };
constexpr int WS_E = 0, WS_KVP = 65536, KVP_STRIDE = 6144, WS_O = 2097152, O_STRIDE = 147456;
constexpr int WS_H1 = WS_O + 32 * O_STRIDE, WS_H2 = WS_H1 + 131072;
constexpr int WS_USED = WS_H2 + 131072;
constexpr unsigned SENT = 0xFFFFFFFFu;
constexpr unsigned OOB = 0x7FFF0000u;
constexpr int SPIN_G = 1 << 18, SPIN_L = 1 << 22;
constexpr float LOG2E = 1.4426950408889634f;

struct Args {
  const void* xA; const void* wA; const void* woA; const void* xB; const void* wB; const void* wphi; const void* woB;
  void* out; char* ws;
};

DI f32x4 mfma(bf16x8 a, bf16x8 b, f32x4 c) { return __builtin_amdgcn_mfma_f32_16x16x32_bf16(a, b, c, 0, 0, 0); }
DI f32x4 mfma16(u32x2 a, u32x2 b, f32x4 c) {
  return __builtin_amdgcn_mfma_f32_16x16x16bf16_1k(__builtin_bit_cast(s16x4, a), __builtin_bit_cast(s16x4, b), c, 0, 0, 0);
}
DI void mfma_acc(f32x4& c, bf16x8 a, bf16x8 b) { asm volatile("v_mfma_f32_16x16x32_bf16 %0, %1, %2, %0" : "+v"(c) : "v"(a), "v"(b)); }
template <int N> DI void mfma_fence(f32x4 (&acc)[N][2]) {
  asm volatile("s_nop 7\n\ts_nop 3" : "+v"(acc[N - 1][0]), "+v"(acc[N - 1][1]) :: "memory");
#pragma unroll
  for (int n = 0; n < N - 1; ++n) asm volatile("" : "+v"(acc[n][0]), "+v"(acc[n][1]));
}
DI unsigned pk2(float a, float b) { f32x2 p = {a, b}; return __builtin_bit_cast(unsigned, __builtin_convertvector(p, bf16x2)); }
DI bf16x8 frag_of(unsigned a, unsigned b, unsigned c, unsigned d) { u32x4 v = {a, b, c, d}; return __builtin_bit_cast(bf16x8, v); }
DI bf16x8 pack_pair(f32x4 lo, f32x4 hi) { return frag_of(pk2(lo[0], lo[1]), pk2(lo[2], lo[3]), pk2(hi[0], hi[1]), pk2(hi[2], hi[3])); }
DI bf16x8 lds128(LAS char* l, int off) { return *(LAS bf16x8*)(l + off); }
DI bf16x8 lds_tr(LAS char* l, int off, int second) {
  bf16x4 a = __builtin_amdgcn_ds_read_tr16_b64_v4bf16((LAS bf16x4*)(l + off));
  bf16x4 b = __builtin_amdgcn_ds_read_tr16_b64_v4bf16((LAS bf16x4*)(l + off + second));
  return __builtin_shufflevector(a, b, 0, 1, 2, 3, 4, 5, 6, 7);
}
DI void dma16(rsrc_t r, unsigned voff, unsigned soff, unsigned ldsaddr) {
  asm volatile("s_mov_b32 m0, %0\n\ts_nop 0\n\tbuffer_load_dwordx4 %1, %2, %3 offen lds" ::"s"(ldsaddr), "v"(voff), "s"(r), "s"(soff) : "memory");
}
template <int N> DI void wait_vm() { asm volatile("s_waitcnt vmcnt(%0)" ::"n"(N) : "memory"); }
DI void cwait_lgkm0() { __builtin_amdgcn_s_waitcnt(0xC07F); }
template <int N> DI void cwait_vm_lgkm4() { __builtin_amdgcn_s_waitcnt((N & 15) | 0x70 | (4 << 8) | ((N >> 4) << 14)); }
DI void barrier() { asm volatile("s_waitcnt lgkmcnt(0)\n\ts_barrier" ::: "memory"); }
DI void lds_arrive(LAS char* l, int id) {
  const unsigned addr = (unsigned)(size_t)l + L_SYNC + id * 4, one = 1u;
  unsigned long long save;
  asm volatile("s_waitcnt lgkmcnt(0)\n\ts_mov_b64 %0, exec\n\ts_mov_b64 exec, 1\n\tds_add_u32 %1, %2\n\ts_mov_b64 exec, %0"
               : "=&s"(save) : "v"(addr), "v"(one) : "memory");
}
DI void lds_wait(LAS char* l, int id, unsigned n = 4u) {
  const unsigned addr = (unsigned)(size_t)l + L_SYNC + id * 4;
#pragma unroll 1
  for (int sp = 0; sp < SPIN_L; ++sp) {
    unsigned v;
    asm volatile("ds_read_b32 %0, %1\n\ts_waitcnt lgkmcnt(0)" : "=v"(v) : "v"(addr) : "memory");
    if ((unsigned)__builtin_amdgcn_readfirstlane(v) >= n) break;
    __builtin_amdgcn_s_sleep(1);
  }
}
DI rsrc_t mk_rsrc(const void* p, int bytes) { return __builtin_amdgcn_make_buffer_rsrc((void*)p, 0, bytes, 0x00020000); }
DI void st_sc1(u32x4 v, rsrc_t r, unsigned off) { __builtin_amdgcn_raw_buffer_store_b128(v, r, off, 0, 16); }
DI u32x4 ld_sc1(rsrc_t r, unsigned off, unsigned soff) { return __builtin_amdgcn_raw_buffer_load_b128(r, off, soff, 16); }
DI int opaque(int x) { asm volatile("" : "+v"(x)); return x; }
DI unsigned umax3(unsigned a, unsigned b, unsigned c) { return max(max(a, b), c); }
DI unsigned nosent(unsigned x) { return min(x, 0xFFFFFFFEu); }
DI void swap16(unsigned& a, unsigned& b) { auto r = __builtin_amdgcn_permlane16_swap(a, b, false, false); a = r[0]; b = r[1]; }
DI f32x4 zero4() { return f32x4{0.f, 0.f, 0.f, 0.f}; }
DI float fmax3(float a, float b, float c) { return fmaxf(fmaxf(a, b), c); }
DI float fmin3(float a, float b, float c) { return fminf(fminf(a, b), c); }
DI void poll_tag(rsrc_t r, unsigned off, unsigned tag, unsigned long long mask, unsigned& p0, unsigned& p1) {
  unsigned cnt = SPIN_G;
  asm volatile(
      "buffer_load_dword %0, %3, %4, 0 offen sc1\n"
      "L_poll_%=:\n\t"
      "buffer_load_dword %1, %3, %4, 0 offen sc1\n\t"
      "s_waitcnt vmcnt(1)\n\t"
      "v_cmp_ne_u32 vcc, %5, %0\n\t"
      "s_nop 1\n\t"
      "s_and_b64 vcc, vcc, %6\n\t"
      "s_cbranch_vccz L_done_%=\n\t"
      "buffer_load_dword %0, %3, %4, 0 offen sc1\n\t"
      "s_waitcnt vmcnt(1)\n\t"
      "v_cmp_ne_u32 vcc, %5, %1\n\t"
      "s_nop 1\n\t"
      "s_and_b64 vcc, vcc, %6\n\t"
      "s_cbranch_vccz L_done_%=\n\t"
      "s_sub_u32 %2, %2, 1\n\t"
      "s_cmp_lg_u32 %2, 0\n\t"
      "s_cbranch_scc1 L_poll_%=\n"
      "L_done_%=:"
      : "=&v"(p0), "=&v"(p1), "+s"(cnt) : "v"(off), "s"(r), "s"(tag), "s"(mask) : "vcc", "scc", "memory");
}
DI void st_tag(rsrc_t r, unsigned line, unsigned tag, int lane) { st_sc1(u32x4{tag, tag, tag, tag}, r, lane < 8 ? line + lane * 16 : OOB); }

DI int krow(int rho) { return 8 * ((rho >> 2) & 3) + 4 * (rho >> 4) + (rho & 3); }

template <bool ISB> DI void feature_map(const f32x4 (&P)[5], unsigned (&ph)[ISB ? 18 : 10]) {
  float p[18];
#pragma unroll
  for (int f = 0; f < 18; ++f) p[f] = P[f >> 2][f & 3];
  if constexpr (ISB) {
    float mx = fmax3(p[0], p[1], p[2]), mn = fmin3(p[0], p[1], p[2]);
#pragma unroll
    for (int f = 3; f < 17; f += 2) { mx = fmax3(mx, p[f], p[f + 1]); mn = fmin3(mn, p[f], p[f + 1]); }
    mx = fmax3(mx, p[17], p[17]); mn = fmin3(mn, p[17], p[17]);
    const f32x2 cp = {-mx * LOG2E, -mx * LOG2E}, cn = {mn * LOG2E, mn * LOG2E};
    f32x2 lp = {LOG2E, LOG2E}, ln = {-LOG2E, -LOG2E};
    asm("" : "+v"(lp), "+v"(ln));
    f32x2 ep[9], en[9];
#pragma unroll
    for (int f = 0; f < 9; ++f) {
      const f32x2 x = {p[2 * f], p[2 * f + 1]};
      const f32x2 a = __builtin_elementwise_fma(x, lp, cp), b = __builtin_elementwise_fma(x, ln, cn);
      ep[f] = f32x2{__builtin_amdgcn_exp2f(a[0]), __builtin_amdgcn_exp2f(a[1])};
      en[f] = f32x2{__builtin_amdgcn_exp2f(b[0]), __builtin_amdgcn_exp2f(b[1])};
    }
    f32x2 sp = ep[0], sn = en[0];
#pragma unroll
    for (int f = 1; f < 9; ++f) { sp += ep[f]; sn += en[f]; }
    const float rp = __builtin_amdgcn_rcpf(sp[0] + sp[1]), rn = __builtin_amdgcn_rcpf(sn[0] + sn[1]);
#pragma unroll
    for (int f = 0; f < 9; ++f) {
      const f32x2 a = ep[f] * rp, b = en[f] * rn;
      ph[f] = pk2(a[0], a[1]);
      ph[9 + f] = pk2(b[0], b[1]);
    }
  } else {
    f32x2 l2 = {LOG2E, LOG2E}, eps = {1e-6f, 1e-6f};
    asm("" : "+v"(l2), "+v"(eps));
#pragma unroll
    for (int f = 0; f < 9; ++f) {
      const f32x2 lo = f32x2{fminf(p[2 * f], 0.f), fminf(p[2 * f + 1], 0.f)} * l2;
      const f32x2 hi = f32x2{fmaxf(p[2 * f], 0.f), fmaxf(p[2 * f + 1], 0.f)};
      const f32x2 y = (hi + f32x2{__builtin_amdgcn_exp2f(lo[0]), __builtin_amdgcn_exp2f(lo[1])}) + eps;
      ph[f] = pk2(y[0], y[1]);
    }
    ph[9] = 0u;
  }
}

struct WF { bf16x8 a[5][2]; u32x2 h[5]; };
DI void load_wf_tile(LAS char* lds, int b0, int b1, WF& f, int t) {
  f.a[t][0] = lds128(lds, b0 + (2 * t) * 1024);
  f.a[t][1] = lds128(lds, b0 + (2 * t + 1) * 1024);
  f.h[t] = *(LAS u32x2*)(lds + b1 + t * 512);
}
DI void logits2(const WF& f, const f32x4 (&t0)[2], const f32x4 (&t1)[2], const f32x4 (&t2)[2], const f32x4 (&t3)[2],
                const f32x4 (&t4)[2], f32x4 (&P)[2][5]) {
#pragma unroll
  for (int m = 0; m < 2; ++m) {
    const bf16x8 b0 = pack_pair(t0[m], t1[m]), b1 = pack_pair(t2[m], t3[m]);
    const u32x2 b2 = u32x2{pk2(t4[m][0], t4[m][1]), pk2(t4[m][2], t4[m][3])};
#pragma unroll
    for (int t = 0; t < 5; ++t) P[m][t] = mfma(f.a[t][0], b0, zero4());
#pragma unroll
    for (int t = 0; t < 5; ++t) P[m][t] = mfma(f.a[t][1], b1, P[m][t]);
#pragma unroll
    for (int t = 0; t < 5; ++t) P[m][t] = mfma16(f.h[t], b2, P[m][t]);
  }
}
DI int wf_offset(int h, int d, int f) {
  const int col = 18 * h + d, s = col >> 5, ln = 16 * ((col >> 2) & 3) + 4 * h + (f & 3), tt = f >> 2;
  return s < 2 ? (tt * 2 + s) * 1024 + ln * 16 + (((col >> 4) & 1) * 4 + (col & 3)) * 2 : 10240 + tt * 512 + ln * 8 + (col & 3) * 2;
}
template <int N> DI void cwait_lgkm() { __builtin_amdgcn_s_waitcnt(0xC07F | (N << 8)); }
DI void step5(LAS char* lds, f32x4 (&acc)[5][2], const bf16x8 (&wc)[5], bf16x8 (&wn)[5], bf16x8 x0, bf16x8 x1, int next, bool more) {
#pragma unroll
  for (int j = 0; j < 10; ++j) {
    if (j == 6) { if (more) cwait_lgkm<10>(); else cwait_lgkm<0>(); }
    mfma_acc(acc[j >> 1][j & 1], wc[j >> 1], (j & 1) ? x1 : x0);
    if (more && j < 5) wn[j] = lds_tr(lds, next + j * 32, 16 * 160);
    SBAR();
  }
}

template <bool ISB> DI void c_path(const Args& a, LAS char* lds, const int w, const int lane0, const int t, const int g) {
  constexpr int prob = ISB ? 1 : 0;
  constexpr int RP = ISB ? 288 : 160;
  constexpr int C5 = ISB ? 5 : 3;
  constexpr int KVROW = C5 * 16, KVHEAD = ISB ? 1536 : 1024, NCH = 19 * C5;
  char* ws = a.ws;
  const rsrc_t rws = mk_rsrc(ws, WS_USED);
  const unsigned kvp_grp = WS_KVP + (prob * 8 + g) * 16 * KVP_STRIDE;
  const unsigned obuf = WS_O + (prob * 16 + t) * O_STRIDE;
  constexpr int NU = ISB ? 9 : 5, UMAIN = NU - 1;
  f32x4 acc[9][2];
  WF wf;
  unsigned T;
  int a_v[5][2], a_one, a_prow, a_pm, a_pl, a_b0, a_b1, a_mu, a_al, a_la;
  unsigned o_p0, o_p1, o_mu, o_ml;
  STAMP_DECL
  {
    const int lane = lane0, tid = w * 64 + lane, i = lane & 15, gg = lane >> 4;
    STAMP(0); CSTAMP(43);
    T = __hip_atomic_load((GAS unsigned*)(ws + WS_E + prob * 256), RLX_AGENT) + 1u;
    unsigned tl0 = 0u, tl1 = 0u, tl2 = 0u;
    if (TLBW && w == 3) {
      const unsigned wsoff = lane < 3 ? obuf + lane * 65536 : lane < 5 ? kvp_grp + (lane - 3) * 65536
                           : lane == 5 ? WS_H1 + (prob * 8 + g) * 8192 : lane == 6 ? WS_H2 + (prob * 16 + t) * 4096 : OOB;
      tl0 = __builtin_amdgcn_raw_buffer_load_b32(rws, wsoff, 0, 0);
      tl1 = __builtin_amdgcn_raw_buffer_load_b32(mk_rsrc(a.out, 2048 * OUTROW), lane < 6 ? t * 128 * OUTROW + min(lane * 65536, 128 * OUTROW - 4) : OOB, 0, 0);
      tl2 = lane < 32 ? __builtin_amdgcn_raw_buffer_load_b32(mk_rsrc(ISB ? a.wB : a.wA, 576 * WROW), lane * 65536, 0, 0)
                      : __builtin_amdgcn_raw_buffer_load_b32(mk_rsrc(ISB ? a.woB : a.woA, 576 * XROW), (lane - 32) * 65536, 0, 0);
    }
    unsigned short wel[6];
    if constexpr (ISB) {
      const rsrc_t rp = mk_rsrc(a.wphi, 32 * 324 * 2);
#pragma unroll
      for (int k = 0; k < 6; ++k) {
        const int idx = lane + 64 * k;
        wel[k] = __builtin_amdgcn_raw_buffer_load_b16(rp, idx < 324 ? ((4 * g + w) * 324 + idx) * 2 : OOB, 0, 0);
      }
    }
    {
      const u32x4 s = {SENT, SENT, SENT, SENT};
#pragma unroll
      for (int k = 0; k < (KVHEAD / 16 + 63) / 64; ++k) {
        const int n = lane + 64 * k;
        st_sc1(s, rws, n < KVHEAD / 16 ? kvp_grp + t * KVP_STRIDE + w * KVHEAD + n * 16 : OOB);
      }
    }
    if (tid < 64) *(LAS unsigned*)(lds + L_SYNC + tid * 4) = tid == SY_TAG ? T : 0u;
    if (tid < 32) *(LAS unsigned*)(lds + L_ZERO + tid * 4) = 0u;
#pragma unroll
    for (int n = 0; n < 9; ++n) { acc[n][0] = zero4(); acc[n][1] = zero4(); asm volatile("" : "+v"(acc[n][0]), "+v"(acc[n][1])); }
    const int xa0 = (32 * w + i) * 128 + ((gg ^ ((i >> 1) & 7)) * 16), xa1 = xa0 ^ 64;
    const int wa = WPART + (4 * gg + (i >> 2)) * 288 + (i & 3) * 8;
    const int wm = wa + ((i & 2) ? 256 : 128);
    {
      const int xs = ((i >> 2) & 3) << 3;
      const int vrow = L_V + (32 * w + i) * 160;
#pragma unroll
      for (int n = 0; n < 5; ++n) {
#pragma unroll
        for (int pp = 0; pp < 2; ++pp) {
          const int c0 = (n < 4 ? 16 * n : 56) + 2 * pp, h0 = c0 / 18;
          const int thr = (18 * (h0 + 1) - c0 + 3) / 4;
          a_v[n][pp] = opaque(vrow + ((2 * c0 + 4 * h0 + 8 * gg + ((thr < 4 && gg >= thr) ? 4 : 0)) ^ xs));
        }
      }
      a_one = opaque(vrow + ((40 * gg + 36) ^ xs));
      a_prow = opaque(L_PHI + (32 * w + i) * RP);
      a_pm = opaque((UMAIN * gg * 8) ^ xs);
      a_pl = opaque(L_PHI + (32 * w + i) * RP + (((4 * UMAIN + gg) * 8) ^ xs));
      const int q = i >> 2, p = i & 3;
      const int pa = L_PHI + (4 * gg + q) * RP, va = L_V + (4 * gg + q) * 160;
      a_b0 = opaque(pa + ((UMAIN * w + p) ^ gg) * 8); a_b1 = opaque(pa + ((UMAIN * w + 4 + p) ^ gg) * 8);
      a_mu = opaque(pa + ((4 * UMAIN + p) ^ gg) * 8);
      a_al = opaque(va + ((5 * w + p) ^ gg) * 8); a_la = opaque(va + ((5 * p + 4) ^ gg) * 8);
      const unsigned mine = kvp_grp + t * KVP_STRIDE + w * KVHEAD;
      const bool lam = (i >> 2) == w && (i & 3) < 3;
      const int el = 16 + (i & 3), ch = ISB ? ((gg & 1) << 1) | (gg >> 1) : gg >> 1;
      const unsigned o_main = mine + i * KVROW + ch * 16, o_lam = lam ? mine + el * KVROW + ch * 16 : OOB;
      o_p0 = opaque(ISB || !(gg & 1) ? o_main : o_lam); o_p1 = opaque(o_lam);
      o_mu = opaque(gg == w ? mine + i * KVROW + (C5 - 1) * 16 : OOB);
      o_ml = opaque((gg == w && lam) ? mine + el * KVROW + (C5 - 1) * 16 : OOB);
    }
    int a_wf[6];
    {
      const u32x4 z = {0u, 0u, 0u, 0u};
#pragma unroll
      for (int k = 0; k < 3; ++k) {
        const int n = lane + 64 * k, fr = n >> 4, sl = n & 15;
        if (n < 160) *(LAS u32x4*)(lds + L_WF + fr * 1024 + (16 * (sl >> 2) + 4 * w + (sl & 3)) * 16) = z;
      }
#pragma unroll
      for (int k = 0; k < 2; ++k) {
        const int n = lane + 64 * k, fr = n >> 4, sl = n & 15;
        if (n < 80) *(LAS u32x2*)(lds + L_WF + 10240 + fr * 512 + (16 * (sl >> 2) + 4 * w + (sl & 3)) * 8) = u32x2{0u, 0u};
      }
      if constexpr (ISB) {
#pragma unroll
        for (int k = 0; k < 6; ++k) {
          const int idx = lane + 64 * k, d = idx / 18, f = idx - d * 18;
          a_wf[k] = opaque(idx < 324 ? L_WF + wf_offset(w, d, f) : L_ZERO + 124);
        }
      } else if (lane < 18) {
        *(LAS unsigned short*)(lds + L_WF + wf_offset(w, lane, lane)) = (unsigned short)0x3F80;
      }
    }
    bf16x8 wb[2][9], xb[2][2];
    auto kstep = [&](auto SC, int xo, int wo, int wmo) {
      constexpr int S = decltype(SC)::value, N = S ^ 1;
#pragma unroll
      for (int n = 0; n < 9; ++n) {
        mfma_acc(acc[n][0], wb[S][n], xb[S][0]);
        if (n == 0) { xb[N][0] = lds128(lds, xo); xb[N][1] = lds128(lds, xo + 2048); }
        else if (n <= 4) wb[N][2 * n - 1] = lds_tr(lds, 2 * n - 1 < 8 ? wo + (2 * n - 1) * 32 + (2 * n - 1 >= 4 ? 16 : 0) : wmo, 4608);
        mfma_acc(acc[n][1], wb[S][n], xb[S][1]);
        if (n <= 4) wb[N][2 * n] = lds_tr(lds, 2 * n < 8 ? wo + (2 * n) * 32 + (2 * n >= 4 ? 16 : 0) : wmo, 4608);
        SBAR();
      }
    };
    using I0 = std::integral_constant<int, 0>;
    using I1 = std::integral_constant<int, 1>;
    STAMP(1);
    barrier();
    STAMP(2); CSTAMP(44);
    {
      const int xo = opaque(xa0 + 3 * SLOT), wo = opaque(wa + 3 * SLOT), wmo = opaque(wm + 3 * SLOT);
      xb[0][0] = lds128(lds, xo); xb[0][1] = lds128(lds, xo + 2048);
#pragma unroll
      for (int n = 0; n < 9; ++n) wb[0][n] = lds_tr(lds, n < 8 ? wo + n * 32 + (n >= 4 ? 16 : 0) : wmo, 4608);
    }
    int base = 3 * SLOT;
    unsigned pfq = 0u;
#pragma unroll
    for (int c = 0; c < 8; ++c) {
      {
        const int xo = opaque(xa1 + base), wo = opaque(wa + base + 9216), wmo = opaque(wm + base + 9216);
        cwait_lgkm0();
        SBAR();
        kstep(I0{}, xo, wo, wmo);
      }
      if (PFQ && c == 1) {
        const int row = 18 * (blockIdx.x >> 3) + tid / 9, ln = tid - 9 * (tid / 9);
        pfq = __builtin_amdgcn_raw_buffer_load_b32(mk_rsrc(ISB ? a.wB : a.wA, 576 * WROW), tid < 162 ? row * WROW + ln * 128 : OOB, 0, 0);
      }
      if constexpr (ISB) {
        if (c == 0) {
#pragma unroll
          for (int k = 0; k < 6; ++k) *(LAS unsigned short*)(lds + a_wf[k]) = wel[k];
        }
      }
      if (c == 5) { asm volatile("" :: "v"(pfq), "v"(tl0), "v"(tl1), "v"(tl2)); wait_vm<0>(); }
      cwait_lgkm0();
      barrier();
      base = base == 3 * SLOT ? 0 : base + SLOT;
      {
        const int xo = opaque(xa0 + base), wo = opaque(wa + base), wmo = opaque(wm + base);
        SBAR();
        kstep(I1{}, xo, wo, wmo);
      }
    }
    {
      const int xo = opaque(xa1 + base), wo = opaque(wa + base + 9216), wmo = opaque(wm + base + 9216);
      cwait_lgkm0();
      SBAR();
      kstep(I0{}, xo, wo, wmo);
      cwait_lgkm0();
      lds_arrive(lds, SY_KV8);
      const int b0 = opaque(L_WF + lane * 16), b1 = opaque(L_WF + 10240 + lane * 8);
      SBAR();
#pragma unroll
      for (int n = 0; n < 9; ++n) {
        mfma_acc(acc[n][0], wb[1][n], xb[1][0]);
        if (n < 5) load_wf_tile(lds, b0, b1, wf, n);
        mfma_acc(acc[n][1], wb[1][n], xb[1][1]);
        SBAR();
      }
      lds_wait(lds, SY_KV8, 8u);
      mfma_fence(acc);
    }
  }
  const int lane = opaque(lane0), tid = w * 64 + lane, i = lane & 15, gg = lane >> 4;
  const unsigned Ts = __builtin_amdgcn_readfirstlane(T);
  __builtin_amdgcn_s_setprio(2);
  STAMP(3); CSTAMP(45);

  {
    f32x4 P[2][5];
    bf16x8 kb[2][2];
    u32x2 kb2[2];
#pragma unroll
    for (int m = 0; m < 2; ++m) {
      kb[m][0] = pack_pair(acc[0][m], acc[1][m]); kb[m][1] = pack_pair(acc[2][m], acc[3][m]);
      kb2[m] = u32x2{pk2(acc[8][m][0], acc[8][m][1]), pk2(acc[8][m][2], acc[8][m][3])};
    }
    SBAR();
#pragma unroll
    for (int st = 0; st < 30; ++st) {
      const int m = st / 15, s = (st % 15) / 5, tt = st % 5;
      if (s == 0) P[m][tt] = mfma(wf.a[tt][0], kb[m][0], zero4());
      if (s == 1) P[m][tt] = mfma(wf.a[tt][1], kb[m][1], P[m][tt]);
      if (s == 2) P[m][tt] = mfma16(wf.h[tt], kb2[m], P[m][tt]);
      if (st < 20) {
        const int n = st >> 2, pp = (st >> 1) & 1, mv = st & 1;
        const f32x4 v = n < 4 ? acc[4 + n][mv] : acc[8][mv];
        const int am = n < 4 ? a_v[n][pp] + mv * 2560 : (gg >= 2 ? a_v[n][pp] + mv * 2560 : L_ZERO + 124);
        *(LAS unsigned*)(lds + am) = pk2(v[2 * pp], v[2 * pp + 1]);
      }
      SBAR();
    }
    *(LAS unsigned*)(lds + a_one) = 0x00003F80u; *(LAS unsigned*)(lds + a_one + 2560) = 0x00003F80u;
    FSTAMP(18);
#pragma unroll
    for (int m = 0; m < 2; ++m) {
      unsigned ph[ISB ? 18 : 10];
      feature_map<ISB>(P[m], ph);
#pragma unroll
      for (int j = 0; j < NU; ++j)
        *(LAS u32x2*)(lds + (j < UMAIN ? a_prow + (a_pm ^ (j * 8)) : a_pl) + m * 16 * RP) = u32x2{ph[2 * j], ph[2 * j + 1]};
      FSTAMP(19 + m);
    }
  }
  STAMP(4);
  lds_arrive(lds, SY_PHI);
  lds_wait(lds, SY_PHI);
  STAMP(5);

  {
    bf16x8 al[4], la[4], b0[4], mu[4], b1[4];
    f32x4 d0 = zero4(), d1 = zero4(), d2 = zero4(), d3 = zero4(), d4 = zero4(), d5 = zero4();
    asm volatile("" : "+v"(d0), "+v"(d1), "+v"(d2), "+v"(d3), "+v"(d4), "+v"(d5));
    auto rd = [&](int ks) {
      al[ks] = lds_tr(lds, a_al + ks * 32 * 160, 16 * 160);
      b0[ks] = lds_tr(lds, a_b0 + ks * 32 * RP, 16 * RP);
      if constexpr (ISB) b1[ks] = lds_tr(lds, a_b1 + ks * 32 * RP, 16 * RP);
      la[ks] = lds_tr(lds, a_la + ks * 32 * 160, 16 * 160);
      mu[ks] = lds_tr(lds, a_mu + ks * 32 * RP, 16 * RP);
      SBAR();
    };
    auto mm = [&](int ks) {
      mfma_acc(d0, b0[ks], al[ks]);
      if constexpr (ISB) mfma_acc(d1, b1[ks], al[ks]);
      mfma_acc(d2, b0[ks], la[ks]);
      if constexpr (ISB) mfma_acc(d3, b1[ks], la[ks]);
      mfma_acc(d4, mu[ks], al[ks]); mfma_acc(d5, mu[ks], la[ks]);
      SBAR();
    };
    constexpr int RD = ISB ? 10 : 8;
    asm volatile("s_nop 1");
    rd(0); rd(1);
    __builtin_amdgcn_s_waitcnt(0xC07F & ~0xF00 | (RD << 8)); SBAR(); mm(0); rd(2);
    __builtin_amdgcn_s_waitcnt(0xC07F & ~0xF00 | (RD << 8)); SBAR(); mm(1); rd(3);
    __builtin_amdgcn_s_waitcnt(0xC07F & ~0xF00 | (RD << 8)); SBAR(); mm(2);
    cwait_lgkm0(); SBAR(); mm(3);
    asm volatile("s_nop 7\n\ts_nop 3" : "+v"(d0), "+v"(d1), "+v"(d2), "+v"(d3), "+v"(d4), "+v"(d5));
    if constexpr (ISB) {
      unsigned x0 = pk2(d0[0], d0[1]), x1 = pk2(d0[2], d0[3]), y0 = pk2(d1[0], d1[1]), y1 = pk2(d1[2], d1[3]);
      swap16(x0, y0); swap16(x1, y1);
      st_sc1(u32x4{nosent(x0), nosent(x1), nosent(y0), nosent(y1)}, rws, o_p0);
      x0 = pk2(d2[0], d2[1]); x1 = pk2(d2[2], d2[3]); y0 = pk2(d3[0], d3[1]); y1 = pk2(d3[2], d3[3]);
      swap16(x0, y0); swap16(x1, y1);
      st_sc1(u32x4{nosent(x0), nosent(x1), nosent(y0), nosent(y1)}, rws, o_p1);
    } else {
      unsigned x0 = pk2(d0[0], d0[1]), x1 = pk2(d0[2], d0[3]), y0 = pk2(d2[0], d2[1]), y1 = pk2(d2[2], d2[3]);
      swap16(x0, y0); swap16(x1, y1);
      st_sc1(u32x4{nosent(x0), nosent(x1), nosent(y0), nosent(y1)}, rws, o_p0);
    }
    st_sc1(u32x4{nosent(pk2(d4[0], d4[1])), nosent(pk2(d4[2], d4[3])), 0u, 0u}, rws, o_mu);
    st_sc1(u32x4{nosent(pk2(d5[0], d5[1])), nosent(pk2(d5[2], d5[3])), 0u, 0u}, rws, o_ml);
    st_tag(rws, WS_H1 + (((prob * 8 + g) * 16 + t) * 4 + w) * 128, T, lane);
    lds_arrive(lds, SY_KVDONE);
  }
  __builtin_amdgcn_s_setprio(0);
  STAMP(6); CSTAMP(46);
  const rsrc_t rwo = mk_rsrc(ISB ? a.woB : a.woA, 576 * XROW);
  {
    STAMP(7);
    const rsrc_t rkv = mk_rsrc(ws + kvp_grp, 16 * KVP_STRIDE);
    constexpr int NG = (NCH + 15) / 16;
    const unsigned one = (i & 1) ? 0x3F800000u : 0x3F80u;
    const bf16x8 sel = frag_of((i >> 1) == 0 ? one : 0u, (i >> 1) == 1 ? one : 0u, (i >> 1) == 2 ? one : 0u, (i >> 1) == 3 ? one : 0u);
    const unsigned off0 = gg * KVP_STRIDE + w * KVHEAD + i * 16;
    const unsigned offl = 16 * (NG - 1) + i < NCH ? off0 + (NG - 1) * 256 : OOB;
    const int kvw = L_PHI + w * KVHEAD + i * 16 + gg * 8;
    unsigned pfo = 0u;
    if (PFO) {
      const int row = 18 * (blockIdx.x >> 3) + tid / 9, ln = tid - 9 * (tid / 9);
      asm volatile("buffer_load_dword %0, %1, %2, 0 offen" : "=v"(pfo) : "v"(tid < 162 ? row * XROW + ln * 128 : OOB), "s"(rwo) : "memory");
    }
    unsigned p0, p1;
    poll_tag(rws, lane < 16 ? WS_H1 + (((prob * 8 + g) * 16 + lane) * 4 + w) * 128 : OOB, Ts, 0xFFFFull, p0, p1);
    __builtin_amdgcn_s_setprio(2);
    STAMP(8);
    f32x4 sum[NG];
    {
      u32x4 v[NG][4];
#pragma unroll
      for (int cg = 0; cg < NG; ++cg) {
        const unsigned off = cg < NG - 1 ? off0 + cg * 256 : offl;
#pragma unroll
        for (int u = 0; u < 4; ++u)
          asm volatile("buffer_load_dwordx4 %0, %1, %2, %3 offen sc1" : "=v"(v[cg][u]) : "v"(off), "s"(rkv), "s"(4 * u * KVP_STRIDE) : "memory");
      }
      {
        const u32x4 s = {SENT, SENT, SENT, SENT};
#pragma unroll
        for (int k = 0; k < 5; ++k) {
          const int n = lane + 64 * k;
          st_sc1(s, rws, n < 288 ? obuf + (2 * w * 72 + 9 * g) * 256 + n * 16 + (n >= 144 ? (72 - 9) * 256 : 0) : OOB);
        }
        SBAR();
      }
      float chk = 0.f;
#pragma unroll
      for (int cg = 0; cg < NG; ++cg) {
        asm volatile("s_waitcnt vmcnt(%7)" : "+v"(v[cg][0]), "+v"(v[cg][1]), "+v"(v[cg][2]), "+v"(v[cg][3]), "+v"(p0), "+v"(p1), "+v"(pfo)
                     : "n"((NG - 1 - cg) * 4 + 5));
        SBAR();
        f32x4 d = zero4();
#pragma unroll
        for (int u = 0; u < 4; ++u) d = mfma(sel, __builtin_bit_cast(bf16x8, v[cg][u]), d);
        sum[cg] = d;
        chk += d[0];
      }
      FSTAMP(21);
      if (!__all(chk == chk)) {
        PCOUNT(22);
#pragma unroll 1
        for (int sp = 0; sp < SPIN_G; ++sp) {
          unsigned mx = 0u;
#pragma unroll 1
          for (int n = 0; n < NG * 4; ++n) {
            asm volatile("" ::: "memory");
            const u32x4 x = ld_sc1(rkv, (n >> 2) < NG - 1 ? off0 + (n >> 2) * 256 : offl, 4 * (n & 3) * KVP_STRIDE);
            mx = umax3(mx, x[0], x[1]); mx = umax3(mx, x[2], x[3]);
          }
          if (__all(mx != SENT)) break;
        }
#pragma unroll
        for (int cg = 0; cg < NG; ++cg) {
          f32x4 d = zero4();
#pragma unroll
          for (int u = 0; u < 4; ++u)
            d = mfma(sel, __builtin_bit_cast(bf16x8, ld_sc1(rkv, cg < NG - 1 ? off0 + cg * 256 : offl, 4 * u * KVP_STRIDE)), d);
          sum[cg] = d;
        }
      }
    }
    lds_wait(lds, SY_KVDONE);
    if (gg < 2) {
#pragma unroll
      for (int cg = 0; cg < NG; ++cg)
        if (cg < NG - 1 || 16 * cg + i < NCH)
          *(LAS u32x2*)(lds + kvw + cg * 256) = u32x2{pk2(sum[cg][0], sum[cg][1]), pk2(sum[cg][2], sum[cg][3])};
    }
    lds_arrive(lds, SY_KVIMG);
    wait_vm<0>();
    lds_arrive(lds, SY_PRE);
  }
  STAMP(9);
  __builtin_amdgcn_s_setprio(0);
  STAMP(10);

  {
    const rsrc_t ro = mk_rsrc(ws + obuf, O_STRIDE);
    const rsrc_t rout = mk_rsrc(a.out, 2048 * OUTROW);
    const unsigned orow = (t * 128 + 32 * w + i) * OUTROW + prob * 1152 + 144 * g;
    const unsigned o_pair = orow + (2 * (gg & 1) + (gg >> 1)) * 16, o_last = gg < 2 ? orow + gg * 16 * OUTROW + 128 : OOB;
    const int wl = (4 * gg + (i >> 2)) * 160 + (i & 3) * 8;
    const int wa2 = opaque(L_WQ + wl), wa3 = opaque(L_WQ + wl + 12 * 5120);
    lds_wait(lds, SY_WO);
    unsigned p0, p1;
    poll_tag(rws, lane < 8 ? WS_H2 + (((prob * 16 + t) * 8 + lane) * 4 + w) * 128 : OOB, Ts, 0xFFull, p0, p1);
    __builtin_amdgcn_s_setprio(2);
    STAMP(11);
    f32x4 oc[5][2];
#pragma unroll 1
    for (int pass = 0; pass < 2; ++pass) {
      asm volatile("" ::: "memory");
      u32x4 of[18][2];
#pragma unroll
      for (int ks = 0; ks < OLOOK; ++ks) {
#pragma unroll
        for (int m = 0; m < 2; ++m) { of[ks][m] = ld_sc1(ro, lane * 16, ((2 * w + m) * 72 + 4 * ks) * 256); SBAR(); }
      }
      bf16x8 wq[2][5];
#pragma unroll
      for (int n = 0; n < 5; ++n) {
        wq[0][n] = lds_tr(lds, wa2 + n * 32, 16 * 160);
        oc[n][0] = zero4(); oc[n][1] = zero4();
        asm volatile("" : "+v"(oc[n][0]), "+v"(oc[n][1]));
      }
      asm volatile("s_nop 1");
      SBAR();
#define OUT_STEP(ks) do { cwait_vm_lgkm4<2 * (((ks) + OLOOK - 1 < 17 ? (ks) + OLOOK - 1 : 17) - (ks))>(); SBAR(); \
      if ((ks) + OLOOK < 18) { of[((ks) + OLOOK) % 18][0] = ld_sc1(ro, lane * 16, ((2 * w) * 72 + 4 * ((ks) + OLOOK)) * 256); \
                               of[((ks) + OLOOK) % 18][1] = ld_sc1(ro, lane * 16, ((2 * w + 1) * 72 + 4 * ((ks) + OLOOK)) * 256); SBAR(); } \
      step5(lds, oc, wq[(ks) & 1], wq[((ks) + 1) & 1], __builtin_bit_cast(bf16x8, of[ks][0]), __builtin_bit_cast(bf16x8, of[ks][1]), \
            (ks) + 1 < 12 ? wa2 + ((ks) + 1) * 5120 : wa3 + ((ks) + 1 - 12) * 5120, (ks) < 17); } while (0)
      OUT_STEP(0);
      asm volatile("" :: "v"(p0), "v"(p1));
      OUT_STEP(1); OUT_STEP(2); OUT_STEP(3); OUT_STEP(4); OUT_STEP(5); OUT_STEP(6); OUT_STEP(7); OUT_STEP(8);
      OUT_STEP(9); OUT_STEP(10); OUT_STEP(11); OUT_STEP(12); OUT_STEP(13); OUT_STEP(14); OUT_STEP(15); OUT_STEP(16); OUT_STEP(17);
#undef OUT_STEP
      mfma_fence(oc);
      const float chk = oc[0][0][0] + oc[0][1][0];
      if (pass == 1 || __all(chk == chk)) break;
      PCOUNT(23);
#pragma unroll 1
      for (int sp = 0; sp < SPIN_G; ++sp) {
        unsigned mx = 0u;
#pragma unroll 1
        for (int n = 0; n < 36; ++n) {
          asm volatile("" ::: "memory");
          const u32x4 x = ld_sc1(ro, lane * 16, ((2 * w + (n & 1)) * 72 + 4 * (n >> 1)) * 256);
          mx = umax3(mx, x[0], x[1]); mx = umax3(mx, x[2], x[3]);
        }
        if (__all(mx != SENT)) break;
      }
    }
    STAMP(12);
#pragma unroll
    for (int m = 0; m < 2; ++m) {
#pragma unroll
      for (int n = 0; n < 4; n += 2) {
        unsigned x0 = pk2(oc[n][m][0], oc[n][m][1]), x1 = pk2(oc[n][m][2], oc[n][m][3]);
        unsigned y0 = pk2(oc[n + 1][m][0], oc[n + 1][m][1]), y1 = pk2(oc[n + 1][m][2], oc[n + 1][m][3]);
        swap16(x0, y0); swap16(x1, y1);
        __builtin_amdgcn_raw_buffer_store_b128(u32x4{x0, x1, y0, y1}, rout, o_pair + m * 16 * OUTROW + n * 32, 0, 0);
      }
    }
    unsigned x0 = pk2(oc[4][0][0], oc[4][0][1]), x1 = pk2(oc[4][0][2], oc[4][0][3]);
    unsigned y0 = pk2(oc[4][1][0], oc[4][1][1]), y1 = pk2(oc[4][1][2], oc[4][1][3]);
    swap16(x0, y0); swap16(x1, y1);
    __builtin_amdgcn_raw_buffer_store_b128(u32x4{x0, x1, y0, y1}, rout, o_last, 0, 0);
    if (t == 0 && g == 0 && tid == 0) __hip_atomic_store((GAS unsigned*)(ws + WS_E + prob * 256), T, RLX_AGENT);
  }
  STAMP(13); CSTAMP(47);
  STAMP_OUT(w);
}

template <bool ISB> DI void h_path(const Args& a, LAS char* lds, const int w, const int lane, const int t, const int g) {
  constexpr int prob = ISB ? 1 : 0;
  constexpr int C5 = ISB ? 5 : 3;
  constexpr int KVROW = C5 * 16, KVHEAD = ISB ? 1536 : 1024;
  const int i = lane & 15, gg = lane >> 4;
  const rsrc_t rx = mk_rsrc((const char*)(ISB ? a.xB : a.xA) + (size_t)t * 128 * XROW, 128 * XROW);
  const rsrc_t rw = mk_rsrc(ISB ? a.wB : a.wA, 576 * WROW);
  const unsigned lb = (unsigned)(size_t)lds;
  STAMP_DECL
  STAMP(0);
  const int rot = ROT == 1 ? (g + 2 * (t & 3) + ((t >> 1) & 1)) % 9 : ROT == 2 ? (g + 8 * (t & 3)) % 9 : 0;
  auto kchunk = [&](int c) { const int k = c + rot; return k >= 9 ? k - 9 : k; };
  unsigned vx[2], vw[5], vq[8];
#pragma unroll
  for (int par = 0; par < 2; ++par) vx[par] = (lane >> 3) * XROW + (((lane & 7) ^ ((4 * par + (lane >> 4)) & 7)) * 16);
  auto issue_x = [&](int c, int j0 = 0, int j1 = 4) {
    const unsigned sb = lb + ((c + 3) & 3) * SLOT;
#pragma unroll
    for (int jj = 0; jj < 4; ++jj)
      if (jj >= j0 && jj < j1) dma16(rx, vx[jj & 1], (4 * w + jj) * 8 * XROW + kchunk(c) * 128, sb + (4 * w + jj) * 1024);
  };
  issue_x(0);
#pragma unroll
  for (int n = 0; n < 5; ++n) {
    const int nn = (w + 4 * n) * 64 + lane, r64 = nn / 18, cp = nn - 18 * r64;
    const int co = cp < 9 ? 1152 + 16 * cp : 2304 + 16 * (cp - 9);
    vw[n] = ((r64 & 32) + krow(r64 & 31)) * WROW + 144 * g + co;
  }
  auto issue_w = [&](int c) {
    const unsigned sb = lb + ((c + 3) & 3) * SLOT;
#pragma unroll
    for (int n = 0; n < 5; ++n)
      if (n < 4 || w < 2) dma16(rw, vw[n], kchunk(c) * 64 * WROW, sb + WPART + (w + 4 * n) * 1024);
  };
  issue_w(0); issue_x(1); issue_w(1);
  if (PRO >= 2) { issue_x(2); issue_w(2); }
  if (PRO >= 3) { issue_x(3); issue_w(3); }
  STAMP(1);
  if (PRO == 0) { if (w < 2) wait_vm<9>(); else wait_vm<8>(); }
  if (PRO == 2) { if (w < 2) wait_vm<18>(); else wait_vm<16>(); }
  if (PRO == 3) { if (w < 2) wait_vm<27>(); else wait_vm<24>(); }
  STAMP(2);
  barrier();
  if (PRO < 2) { issue_x(2); issue_w(2); }
#pragma unroll
  for (int n = 0; n < 8; ++n) {
    const int v = w + 4 * n, ks = v / 5, nn = (v - 5 * ks) * 64 + lane, rho = nn / 10, cp = nn - 10 * rho;
    vq[n] = ((ks & 1) * 32 + krow(rho)) * WROW + 144 * g + 16 * min(cp, 8);
  }
  auto issue_wq = [&](int grp) {
#pragma unroll
    for (int n = 0; n < 8; ++n)
      if (n < 7 || w < 2)
        dma16(rw, vq[n], kchunk(3 * grp + ((w + 4 * n) / 5 >> 1)) * 64 * WROW, lb + L_WQ + grp * 30720 + (w + 4 * n) * 1024);
  };
  bf16x8 X[18][2];
  const int xa0 = (32 * w + i) * 128 + ((gg ^ ((i >> 1) & 7)) * 16), xa1 = xa0 ^ 64;
#pragma unroll
  for (int c = 0; c < 9; ++c) {
    const int base = ((c + 3) & 3) * SLOT;
    if (c <= 5 && (c > 0 || PRO < 3)) issue_x(c + 3, 0, XFIRST);
    SBAR();
    const int xo0 = opaque(xa0 + base), xo1 = opaque(xa1 + base);
    X[2 * c][0] = lds128(lds, xo0); X[2 * c][1] = lds128(lds, xo0 + 2048);
    X[2 * c + 1][0] = lds128(lds, xo1); X[2 * c + 1][1] = lds128(lds, xo1 + 2048);
    SBAR();
    if (c <= 5 && (c > 0 || PRO < 3)) { issue_x(c + 3, XFIRST, 4); issue_w(c + 3); }
    LSTAMP(16 + c);
    if (c <= 5) { if (w < 2) wait_vm<18>(); else wait_vm<16>(); }
    if (c == 6) { if (w < 2) wait_vm<9>(); else wait_vm<8>(); }
    if (c == 7) wait_vm<0>();
    LSTAMP(25 + c);
    if (c < 8) barrier(); else lds_arrive(lds, SY_KV8);
    LSTAMP(34 + c);
  }
  issue_wq(0); issue_wq(1); issue_wq(2);
  __builtin_amdgcn_s_setprio(1);
  STAMP(3);
  f32x4 qa[5][2];
#pragma unroll
  for (int n = 0; n < 5; ++n) { qa[n][0] = zero4(); qa[n][1] = zero4(); asm volatile("" : "+v"(qa[n][0]), "+v"(qa[n][1])); }
  const int wa2 = opaque(L_WQ + (4 * gg + (i >> 2)) * 160 + (i & 3) * 8), wa3 = opaque(wa2 + 12 * 5120);
  bf16x8 wq[2][5];
  if (w < 2) wait_vm<16>(); else wait_vm<14>();
  lds_arrive(lds, SY_WQ0);
  lds_wait(lds, SY_WQ0);
  STAMP(4);
#pragma unroll
  for (int n = 0; n < 5; ++n) wq[0][n] = lds_tr(lds, wa2 + n * 32, 16 * 160);
#pragma unroll
  for (int ks = 0; ks < 18; ++ks) {
    cwait_lgkm<4>();
    if (ks == 5) { if (w < 2) wait_vm<8>(); else wait_vm<7>(); lds_arrive(lds, SY_WQ1); lds_wait(lds, SY_WQ1); STAMP(5); }
    if (ks == 11) { wait_vm<0>(); lds_arrive(lds, SY_WQ2); lds_wait(lds, SY_WQ2); STAMP(6); }
    SBAR();
    step5(lds, qa, wq[ks & 1], wq[(ks + 1) & 1], X[ks][0], X[ks][1], ks + 1 < 12 ? wa2 + (ks + 1) * 5120 : wa3 + (ks + 1 - 12) * 5120, ks < 17);
  }
  WF wf;
  {
    const int b0 = opaque(L_WF + lane * 16), b1 = opaque(L_WF + 10240 + lane * 8);
#pragma unroll
    for (int n = 0; n < 5; ++n) load_wf_tile(lds, b0, b1, wf, n);
  }
  mfma_fence(qa);
  lds_arrive(lds, SY_QDONE);
  STAMP(7);
  const unsigned T = *(LAS unsigned*)(lds + L_SYNC + SY_TAG * 4);
  unsigned ph[2][ISB ? 18 : 10];
  {
    f32x4 P[2][5];
    logits2(wf, qa[0], qa[1], qa[2], qa[3], qa[4], P);
    feature_map<ISB>(P[0], ph[0]);
    feature_map<ISB>(P[1], ph[1]);
#pragma unroll
    for (int j = 0; j < (ISB ? 18 : 10); ++j) asm volatile("" : "+v"(ph[0][j]), "+v"(ph[1][j]));
  }
  int a_kv[5], a_st[9];
  {
    const bool own = (i >> 2) == gg;
#pragma unroll
    for (int n = 0; n < 5; ++n) {
      const int e = 4 * n + (i & 3);
      a_kv[n] = opaque((own && e < 19) ? L_PHI + gg * KVHEAD + e * KVROW : L_ZERO);
    }
#pragma unroll
    for (int pp = 0; pp < 9; ++pp) {
      const int cp = 9 * gg + pp;
      a_st[pp] = opaque(L_V + w * 4608 + (cp >> 2) * 256 + i * 16 + (cp & 3) * 4);
    }
  }
  const rsrc_t ro = mk_rsrc(a.ws + WS_O + (prob * 16 + t) * O_STRIDE, O_STRIDE);
  const unsigned o_dst = (2 * w * 72 + 9 * g) * 256 + lane * 16;
  const rsrc_t rwo = mk_rsrc(ISB ? a.woB : a.woA, 576 * XROW);
  unsigned vo[5];
#pragma unroll
  for (int n = 0; n < 5; ++n) {
    const int nn = ((w + 4 * n) % 5) * 64 + lane, rho = nn / 10, cp = nn - 10 * rho;
    vo[n] = (unsigned)opaque(krow(rho) * XROW + 144 * g + 16 * min(cp, 8));
  }
  lds_wait(lds, SY_QDONE);
#pragma unroll
  for (int n = 0; n < WO_FIRST; ++n) { const int v = w + 4 * n; dma16(rwo, vo[n % 5], (v / 5) * 32 * XROW, lb + L_WQ + v * 1024); }
  STAMP(8);
  lds_wait(lds, SY_KVIMG);
  __builtin_amdgcn_s_setprio(3);
  STAMP(9);
  f32x4 O[5][2];
  {
    bf16x8 af[5][C5];
#pragma unroll
    for (int s = 0; s < C5; ++s) {
#pragma unroll
      for (int n = 0; n < 5; ++n) af[n][s] = lds128(lds, a_kv[n] + s * 16);
    }
    SBAR();
    constexpr int NP = ISB ? 18 : 10;
#pragma unroll
    for (int s = 0; s < C5; ++s) {
      bf16x8 bq[2];
#pragma unroll
      for (int m = 0; m < 2; ++m) {
        unsigned u[4];
#pragma unroll
        for (int j = 0; j < 4; ++j) u[j] = 4 * s + j < NP ? ph[m][4 * s + j < NP ? 4 * s + j : 0] : 0u;
        bq[m] = frag_of(u[0], u[1], u[2], u[3]);
      }
#pragma unroll
      for (int n = 4; n >= 0; --n) {
#pragma unroll
        for (int m = 0; m < 2; ++m) O[n][m] = mfma(af[n][s], bq[m], s == 0 ? zero4() : O[n][m]);
      }
    }
  }
  STAMP(10);
#pragma unroll
  for (int m = 0; m < 2; ++m) {
    const float rc = __builtin_amdgcn_rcpf(O[4][m][2] + 1e-6f);
    const f32x2 rc2 = {rc, rc};
#pragma unroll
    for (int pp = 0; pp < 9; ++pp) {
      const int e = 2 * pp;
      const f32x2 y = f32x2{O[e >> 2][m][e & 3], O[(e + 1) >> 2][m][(e + 1) & 3]} * rc2;
      *(LAS unsigned*)(lds + a_st[pp] + m * 2304) = nosent(pk2(y[0], y[1]));
    }
  }
  lds_wait(lds, SY_PRE);
  {
    const int sb = L_V + w * 4608 + lane * 16;
    u32x4 v[5];
#pragma unroll
    for (int k = 0; k < 5; ++k) v[k] = *(LAS u32x4*)(lds + sb + (k < 4 || lane < 32 ? k * 1024 : 0));
#pragma unroll
    for (int k = 0; k < 5; ++k) {
      const int n = lane + 64 * k;
      st_sc1(v[k], ro, n < 288 ? o_dst + k * 1024 + (n >= 144 ? (72 - 9) * 256 : 0) : OOB);
    }
    st_tag(mk_rsrc(a.ws, WS_USED), WS_H2 + (((prob * 16 + t) * 8 + g) * 4 + w) * 128, T, lane);
  }
  STAMP(11);
#pragma unroll
  for (int n = WO_FIRST; n < 23; ++n) {
    const int v = w + 4 * n;
    if (n < 22 || w < 2) dma16(rwo, vo[n % 5], (v / 5) * 32 * XROW, lb + L_WQ + v * 1024);
  }
  wait_vm<0>();
  lds_arrive(lds, SY_WO);
  STAMP(12);
  STAMP_OUT(4 + w);
}

__global__ __launch_bounds__(512, 2) void hedgehog_fused(Args a) {
  extern __shared__ __attribute__((aligned(16))) char smem[];
  LAS char* lds = (LAS char*)smem;
  const int b = blockIdx.x, xcd = b & 7, r = b >> 3;
  const int prob = xcd >> 2, t = (xcd & 3) * 4 + (r >> 3), g = r & 7;
  const int wave = __builtin_amdgcn_readfirstlane(threadIdx.x >> 6), lane = threadIdx.x & 63;
  unsigned cpf = 0u;
  if (CPF) {
    unsigned long long pc;
    asm volatile("s_getpc_b64 %0" : "=s"(pc));
    cpf = *(const GAS unsigned*)((pc & ~127ull) + threadIdx.x * 128u);
  }
  if ((wave < 4) != (HFIRST != 0)) {
    if (prob) c_path<true>(a, lds, wave & 3, lane, t, g); else c_path<false>(a, lds, wave & 3, lane, t, g);
  } else {
    if (prob) h_path<true>(a, lds, wave & 3, lane, t, g); else h_path<false>(a, lds, wave & 3, lane, t, g);
  }
  asm volatile("" :: "v"(cpf));
}

}

extern "C" void kernel_launch(void* const* d_in, const int* in_sizes, int n_in, void* d_out, int out_size, void* d_ws,
                              size_t ws_size, hipStream_t stream) {
  (void)in_sizes; (void)n_in; (void)out_size; (void)ws_size;
  Args a;
  a.xA = d_in[0]; a.wA = d_in[1]; a.woA = d_in[2]; a.xB = d_in[3]; a.wB = d_in[4]; a.wphi = d_in[5]; a.woB = d_in[6];
  a.out = d_out; a.ws = (char*)d_ws;
  (void)hipFuncSetAttribute((const void*)hedgehog_fused, hipFuncAttributeMaxDynamicSharedMemorySize, LDS_BYTES);
  hedgehog_fused<<<256, 512, LDS_BYTES, stream>>>(a);
}
```

```cpp
#include <hip/hip_runtime.h>
#include <hip/hip_bf16.h>
#include <stdint.h>
#include <type_traits>

typedef __bf16 bf16x8 __attribute__((ext_vector_type(8)));
typedef __bf16 bf16x4 __attribute__((ext_vector_type(4)));
typedef __bf16 bf16x2 __attribute__((ext_vector_type(2)));
typedef short s16x4 __attribute__((ext_vector_type(4)));
typedef float f32x4 __attribute__((ext_vector_type(4)));
typedef float f32x2 __attribute__((ext_vector_type(2)));
typedef unsigned u32x4 __attribute__((ext_vector_type(4)));
typedef unsigned u32x2 __attribute__((ext_vector_type(2)));
#define LAS __attribute__((address_space(3)))
#define GAS __attribute__((address_space(1)))
#define DI __device__ __forceinline__
#define RLX_AGENT __ATOMIC_RELAXED, __HIP_MEMORY_SCOPE_AGENT
#define SBAR() __builtin_amdgcn_sched_barrier(0)
typedef __amdgpu_buffer_rsrc_t rsrc_t;

namespace {

constexpr int XROW = 1152;
constexpr int WROW = 3456;
constexpr int OUTROW = 2304;
constexpr int SLOT = 34816, WPART = 16384;
constexpr int L_WQ = 0;
constexpr int L_PHI = 92160;
constexpr int L_V = 129024;
constexpr int L_WF = 149504;
constexpr int L_SYNC = 162304;
constexpr int L_ZERO = 162560;
constexpr int LDS_BYTES = 163840;
enum { SY_WQ0 = 0, SY_WQ1, SY_WQ2, SY_QDONE, SY_PHI, SY_KVDONE, SY_KVIMG, SY_WO, SY_KV8, SY_PRE, SY_TAG = 63 };
constexpr int WS_E = 0, WS_KVP = 65536, KVP_STRIDE = 6144, WS_O = 2097152, O_STRIDE = 147456;
constexpr int WS_H1 = WS_O + 32 * O_STRIDE, WS_H2 = WS_H1 + 131072;
constexpr int WS_USED = WS_H2 + 131072;
constexpr unsigned SENT = 0xFFFFFFFFu;
constexpr unsigned OOB = 0x7FFF0000u;
constexpr int SPIN_G = 1 << 18, SPIN_L = 1 << 22;
constexpr int OLOOK = 4;
constexpr int WO_FIRST = 11;
constexpr float LOG2E = 1.4426950408889634f;

struct Args {
  const void* xA; const void* wA; const void* woA; const void* xB; const void* wB; const void* wphi; const void* woB;
  void* out; char* ws;
};

DI f32x4 mfma(bf16x8 a, bf16x8 b, f32x4 c) { return __builtin_amdgcn_mfma_f32_16x16x32_bf16(a, b, c, 0, 0, 0); }
DI f32x4 mfma16(u32x2 a, u32x2 b, f32x4 c) {
  return __builtin_amdgcn_mfma_f32_16x16x16bf16_1k(__builtin_bit_cast(s16x4, a), __builtin_bit_cast(s16x4, b), c, 0, 0, 0);
}
DI void mfma_acc(f32x4& c, bf16x8 a, bf16x8 b) { asm volatile("v_mfma_f32_16x16x32_bf16 %0, %1, %2, %0" : "+v"(c) : "v"(a), "v"(b)); }
template <int N> DI void mfma_fence(f32x4 (&acc)[N][2]) {
  asm volatile("s_nop 7\n\ts_nop 3" : "+v"(acc[N - 1][0]), "+v"(acc[N - 1][1]) :: "memory");
#pragma unroll
  for (int n = 0; n < N - 1; ++n) asm volatile("" : "+v"(acc[n][0]), "+v"(acc[n][1]));
}
DI unsigned pk2(float a, float b) { f32x2 p = {a, b}; return __builtin_bit_cast(unsigned, __builtin_convertvector(p, bf16x2)); }
DI bf16x8 frag_of(unsigned a, unsigned b, unsigned c, unsigned d) { u32x4 v = {a, b, c, d}; return __builtin_bit_cast(bf16x8, v); }
DI bf16x8 pack_pair(f32x4 lo, f32x4 hi) { return frag_of(pk2(lo[0], lo[1]), pk2(lo[2], lo[3]), pk2(hi[0], hi[1]), pk2(hi[2], hi[3])); }
template <class T> DI LAS T* lp(int addr) { return (LAS T*)(size_t)(unsigned)addr; }
DI bf16x8 lds128(LAS char*, int off) { return *lp<bf16x8>(off); }
DI bf16x8 lds_tr(LAS char*, int off, int second) {
  bf16x4 a = __builtin_amdgcn_ds_read_tr16_b64_v4bf16(lp<bf16x4>(off));
  bf16x4 b = __builtin_amdgcn_ds_read_tr16_b64_v4bf16(lp<bf16x4>(off + second));
  return __builtin_shufflevector(a, b, 0, 1, 2, 3, 4, 5, 6, 7);
}
DI void dma16(rsrc_t r, unsigned voff, unsigned soff, unsigned ldsaddr) {
  asm volatile("s_mov_b32 m0, %0\n\ts_nop 0\n\tbuffer_load_dwordx4 %1, %2, %3 offen lds" ::"s"(ldsaddr), "v"(voff), "s"(r), "s"(soff) : "memory");
}
template <int N> DI void wait_vm() { asm volatile("s_waitcnt vmcnt(%0)" ::"n"(N) : "memory"); }
DI void cwait_lgkm0() { __builtin_amdgcn_s_waitcnt(0xC07F); }
template <int N> DI void cwait_lgkm() { __builtin_amdgcn_s_waitcnt(0xC07F | (N << 8)); }
template <int N> DI void cwait_vm_lgkm4() { __builtin_amdgcn_s_waitcnt((N & 15) | 0x70 | (4 << 8) | ((N >> 4) << 14)); }
DI void barrier() { asm volatile("s_waitcnt lgkmcnt(0)\n\ts_barrier" ::: "memory"); }
DI void lds_arrive(LAS char* l, int id) {
  const unsigned addr = L_SYNC + id * 4, one = 1u;
  unsigned long long save;
  asm volatile("s_waitcnt lgkmcnt(0)\n\ts_mov_b64 %0, exec\n\ts_mov_b64 exec, 1\n\tds_add_u32 %1, %2\n\ts_mov_b64 exec, %0"
               : "=&s"(save) : "v"(addr), "v"(one) : "memory");
}
DI void lds_wait(LAS char* l, int id, unsigned n = 4u) {
  const unsigned addr = L_SYNC + id * 4;
#pragma unroll 1
  for (int sp = 0; sp < SPIN_L; ++sp) {
    unsigned v;
    asm volatile("ds_read_b32 %0, %1\n\ts_waitcnt lgkmcnt(0)" : "=v"(v) : "v"(addr) : "memory");
    if ((unsigned)__builtin_amdgcn_readfirstlane(v) >= n) break;
  }
}
DI rsrc_t mk_rsrc(const void* p, int bytes) { return __builtin_amdgcn_make_buffer_rsrc((void*)p, 0, bytes, 0x00020000); }
DI void st_sc1(u32x4 v, rsrc_t r, unsigned off) { __builtin_amdgcn_raw_buffer_store_b128(v, r, off, 0, 16); }
DI u32x4 ld_sc1(rsrc_t r, unsigned off, unsigned soff) { return __builtin_amdgcn_raw_buffer_load_b128(r, off, soff, 16); }
DI int opaque(int x) { asm volatile("" : "+v"(x)); return x; }
DI unsigned umax3(unsigned a, unsigned b, unsigned c) { return max(max(a, b), c); }
DI unsigned nosent(unsigned x) { return min(x, 0xFFFFFFFEu); }
DI void swap16(unsigned& a, unsigned& b) { auto r = __builtin_amdgcn_permlane16_swap(a, b, false, false); a = r[0]; b = r[1]; }
DI f32x4 zero4() { return f32x4{0.f, 0.f, 0.f, 0.f}; }
DI float fmax3(float a, float b, float c) { return fmaxf(fmaxf(a, b), c); }
DI float fmin3(float a, float b, float c) { return fminf(fminf(a, b), c); }
DI void poll_tag(rsrc_t r, unsigned off, unsigned tag, unsigned long long mask) {
  unsigned cnt = SPIN_G, v;
  asm volatile(
      "L_poll_%=:\n\t"
      "buffer_load_dword %0, %2, %3, 0 offen sc1\n\t"
      "s_waitcnt vmcnt(0)\n\t"
      "v_cmp_ne_u32 vcc, %4, %0\n\t"
      "s_nop 1\n\t"
      "s_and_b64 vcc, vcc, %5\n\t"
      "s_cbranch_vccz L_done_%=\n\t"
      "s_sub_u32 %1, %1, 1\n\t"
      "s_cmp_lg_u32 %1, 0\n\t"
      "s_cbranch_scc1 L_poll_%=\n"
      "L_done_%=:"
      : "=&v"(v), "+s"(cnt) : "v"(off), "s"(r), "s"(tag), "s"(mask) : "vcc", "scc", "memory");
}
DI void st_tag(rsrc_t r, unsigned line, unsigned tag, int lane) { st_sc1(u32x4{tag, tag, tag, tag}, r, lane < 8 ? line + lane * 16 : OOB); }

DI int krow(int rho) { return 8 * ((rho >> 2) & 3) + 4 * (rho >> 4) + (rho & 3); }

template <bool ISB> DI void feature_map(const f32x4 (&P)[5], unsigned (&ph)[ISB ? 18 : 10]) {
  float p[18];
#pragma unroll
  for (int f = 0; f < 18; ++f) p[f] = P[f >> 2][f & 3];
  if constexpr (ISB) {
    float mx = fmax3(p[0], p[1], p[2]), mn = fmin3(p[0], p[1], p[2]);
#pragma unroll
    for (int f = 3; f < 17; f += 2) { mx = fmax3(mx, p[f], p[f + 1]); mn = fmin3(mn, p[f], p[f + 1]); }
    mx = fmax3(mx, p[17], p[17]); mn = fmin3(mn, p[17], p[17]);
    const f32x2 cp = {-mx * LOG2E, -mx * LOG2E}, cn = {mn * LOG2E, mn * LOG2E};
    f32x2 lp = {LOG2E, LOG2E}, ln = {-LOG2E, -LOG2E};
    asm("" : "+v"(lp), "+v"(ln));
    f32x2 ep[9], en[9];
#pragma unroll
    for (int f = 0; f < 9; ++f) {
      const f32x2 x = {p[2 * f], p[2 * f + 1]};
      const f32x2 a = __builtin_elementwise_fma(x, lp, cp), b = __builtin_elementwise_fma(x, ln, cn);
      ep[f] = f32x2{__builtin_amdgcn_exp2f(a[0]), __builtin_amdgcn_exp2f(a[1])};
      en[f] = f32x2{__builtin_amdgcn_exp2f(b[0]), __builtin_amdgcn_exp2f(b[1])};
    }
    f32x2 sp = ep[0], sn = en[0];
#pragma unroll
    for (int f = 1; f < 9; ++f) { sp += ep[f]; sn += en[f]; }
    const float rp = __builtin_amdgcn_rcpf(sp[0] + sp[1]), rn = __builtin_amdgcn_rcpf(sn[0] + sn[1]);
#pragma unroll
    for (int f = 0; f < 9; ++f) {
      const f32x2 a = ep[f] * rp, b = en[f] * rn;
      ph[f] = pk2(a[0], a[1]);
      ph[9 + f] = pk2(b[0], b[1]);
    }
  } else {
    f32x2 l2 = {LOG2E, LOG2E}, eps = {1e-6f, 1e-6f};
    asm("" : "+v"(l2), "+v"(eps));
#pragma unroll
    for (int f = 0; f < 9; ++f) {
      const f32x2 lo = f32x2{fminf(p[2 * f], 0.f), fminf(p[2 * f + 1], 0.f)} * l2;
      const f32x2 hi = f32x2{fmaxf(p[2 * f], 0.f), fmaxf(p[2 * f + 1], 0.f)};
      const f32x2 y = (hi + f32x2{__builtin_amdgcn_exp2f(lo[0]), __builtin_amdgcn_exp2f(lo[1])}) + eps;
      ph[f] = pk2(y[0], y[1]);
    }
    ph[9] = 0u;
  }
}

struct WF { bf16x8 a[5][2]; u32x2 h[5]; };
DI void load_wf_tile(LAS char* lds, int b0, int b1, WF& f, int t) {
  f.a[t][0] = lds128(lds, b0 + (2 * t) * 1024);
  f.a[t][1] = lds128(lds, b0 + (2 * t + 1) * 1024);
  f.h[t] = *lp<u32x2>(b1 + t * 512);
}
DI void logits2(const WF& f, const f32x4 (&t0)[2], const f32x4 (&t1)[2], const f32x4 (&t2)[2], const f32x4 (&t3)[2],
                const f32x4 (&t4)[2], f32x4 (&P)[2][5]) {
#pragma unroll
  for (int m = 0; m < 2; ++m) {
    const bf16x8 b0 = pack_pair(t0[m], t1[m]), b1 = pack_pair(t2[m], t3[m]);
    const u32x2 b2 = u32x2{pk2(t4[m][0], t4[m][1]), pk2(t4[m][2], t4[m][3])};
#pragma unroll
    for (int t = 0; t < 5; ++t) P[m][t] = mfma(f.a[t][0], b0, zero4());
#pragma unroll
    for (int t = 0; t < 5; ++t) P[m][t] = mfma(f.a[t][1], b1, P[m][t]);
#pragma unroll
    for (int t = 0; t < 5; ++t) P[m][t] = mfma16(f.h[t], b2, P[m][t]);
  }
}
DI int wf_offset(int h, int d, int f) {
  const int col = 18 * h + d, s = col >> 5, ln = 16 * ((col >> 2) & 3) + 4 * h + (f & 3), tt = f >> 2;
  return s < 2 ? (tt * 2 + s) * 1024 + ln * 16 + (((col >> 4) & 1) * 4 + (col & 3)) * 2 : 10240 + tt * 512 + ln * 8 + (col & 3) * 2;
}
DI void step5(LAS char* lds, f32x4 (&acc)[5][2], const bf16x8 (&wc)[5], bf16x8 (&wn)[5], bf16x8 x0, bf16x8 x1, int next, bool more) {
#pragma unroll
  for (int j = 0; j < 10; ++j) {
    if (j == 6) { if (more) cwait_lgkm<10>(); else cwait_lgkm<0>(); }
    mfma_acc(acc[j >> 1][j & 1], wc[j >> 1], (j & 1) ? x1 : x0);
    if (more && j < 5) wn[j] = lds_tr(lds, next + j * 32, 16 * 160);
    SBAR();
  }
}

template <bool ISB> DI void c_path(const Args& a, LAS char* lds, const int w, const int lane0, const int t, const int g) {
  constexpr int prob = ISB ? 1 : 0;
  constexpr int RP = ISB ? 288 : 160;
  constexpr int C5 = ISB ? 5 : 3;
  constexpr int KVROW = C5 * 16, KVHEAD = ISB ? 1536 : 1024, NCH = 19 * C5;
  char* ws = a.ws;
  const rsrc_t rws = mk_rsrc(ws, WS_USED);
  const unsigned kvp_grp = WS_KVP + (prob * 8 + g) * 16 * KVP_STRIDE;
  const unsigned obuf = WS_O + (prob * 16 + t) * O_STRIDE;
  constexpr int NU = ISB ? 9 : 5, UMAIN = NU - 1;
  f32x4 acc[9][2];
  WF wf;
  unsigned T;
  int a_v[5][2], a_one, a_prow, a_pm, a_pl, a_b0, a_b1, a_mu, a_al, a_la;
  unsigned o_p0, o_p1, o_mu, o_ml;
  {
    const int lane = lane0, tid = w * 64 + lane, i = lane & 15, gg = lane >> 4;
    T = __hip_atomic_load((GAS unsigned*)(ws + WS_E + prob * 256), RLX_AGENT) + 1u;
    unsigned short wel[6];
    if constexpr (ISB) {
      const rsrc_t rp = mk_rsrc(a.wphi, 32 * 324 * 2);
#pragma unroll
      for (int k = 0; k < 6; ++k) {
        const int idx = lane + 64 * k;
        wel[k] = __builtin_amdgcn_raw_buffer_load_b16(rp, idx < 324 ? ((4 * g + w) * 324 + idx) * 2 : OOB, 0, 0);
      }
    }
    {
      const u32x4 s = {SENT, SENT, SENT, SENT};
#pragma unroll
      for (int k = 0; k < (KVHEAD / 16 + 63) / 64; ++k) {
        const int n = lane + 64 * k;
        st_sc1(s, rws, n < KVHEAD / 16 ? kvp_grp + t * KVP_STRIDE + w * KVHEAD + n * 16 : OOB);
      }
    }
    if (tid < 64) *lp<unsigned>(L_SYNC + tid * 4) = tid == SY_TAG ? T : 0u;
    if (tid < 32) *lp<unsigned>(L_ZERO + tid * 4) = 0u;
#pragma unroll
    for (int n = 0; n < 9; ++n) { acc[n][0] = zero4(); acc[n][1] = zero4(); asm volatile("" : "+v"(acc[n][0]), "+v"(acc[n][1])); }
    const int xa0 = (32 * w + i) * 128 + ((gg ^ ((i >> 1) & 7)) * 16), xa1 = xa0 ^ 64;
    const int wa = WPART + (4 * gg + (i >> 2)) * 288 + (i & 3) * 8;
    const int wm = wa + ((i & 2) ? 256 : 128);
    {
      const int xs = ((i >> 2) & 3) << 3;
      const int vrow = L_V + (32 * w + i) * 160;
#pragma unroll
      for (int n = 0; n < 5; ++n) {
#pragma unroll
        for (int pp = 0; pp < 2; ++pp) {
          const int c0 = (n < 4 ? 16 * n : 56) + 2 * pp, h0 = c0 / 18;
          const int thr = (18 * (h0 + 1) - c0 + 3) / 4;
          a_v[n][pp] = opaque(vrow + ((2 * c0 + 4 * h0 + 8 * gg + ((thr < 4 && gg >= thr) ? 4 : 0)) ^ xs));
        }
      }
      a_one = opaque(vrow + ((40 * gg + 36) ^ xs));
      a_prow = opaque(L_PHI + (32 * w + i) * RP);
      a_pm = opaque((UMAIN * gg * 8) ^ xs);
      a_pl = opaque(L_PHI + (32 * w + i) * RP + (((4 * UMAIN + gg) * 8) ^ xs));
      const int q = i >> 2, p = i & 3;
      const int pa = L_PHI + (4 * gg + q) * RP, va = L_V + (4 * gg + q) * 160;
      a_b0 = opaque(pa + ((UMAIN * w + p) ^ gg) * 8); a_b1 = opaque(pa + ((UMAIN * w + 4 + p) ^ gg) * 8);
      a_mu = opaque(pa + ((4 * UMAIN + p) ^ gg) * 8);
      a_al = opaque(va + ((5 * w + p) ^ gg) * 8); a_la = opaque(va + ((5 * p + 4) ^ gg) * 8);
      const unsigned mine = kvp_grp + t * KVP_STRIDE + w * KVHEAD;
      const bool lam = (i >> 2) == w && (i & 3) < 3;
      const int el = 16 + (i & 3), ch = ISB ? ((gg & 1) << 1) | (gg >> 1) : gg >> 1;
      const unsigned o_main = mine + i * KVROW + ch * 16, o_lam = lam ? mine + el * KVROW + ch * 16 : OOB;
      o_p0 = opaque(ISB || !(gg & 1) ? o_main : o_lam); o_p1 = opaque(o_lam);
      o_mu = opaque(gg == w ? mine + i * KVROW + (C5 - 1) * 16 : OOB);
      o_ml = opaque((gg == w && lam) ? mine + el * KVROW + (C5 - 1) * 16 : OOB);
    }
    int a_wf[6];
    {
      const u32x4 z = {0u, 0u, 0u, 0u};
#pragma unroll
      for (int k = 0; k < 3; ++k) {
        const int n = lane + 64 * k, fr = n >> 4, sl = n & 15;
        if (n < 160) *lp<u32x4>(L_WF + fr * 1024 + (16 * (sl >> 2) + 4 * w + (sl & 3)) * 16) = z;
      }
#pragma unroll
      for (int k = 0; k < 2; ++k) {
        const int n = lane + 64 * k, fr = n >> 4, sl = n & 15;
        if (n < 80) *lp<u32x2>(L_WF + 10240 + fr * 512 + (16 * (sl >> 2) + 4 * w + (sl & 3)) * 8) = u32x2{0u, 0u};
      }
      if constexpr (ISB) {
#pragma unroll
        for (int k = 0; k < 6; ++k) {
          const int idx = lane + 64 * k, d = idx / 18, f = idx - d * 18;
          a_wf[k] = opaque(idx < 324 ? L_WF + wf_offset(w, d, f) : L_ZERO + 124);
        }
      } else if (lane < 18) {
        *lp<unsigned short>(L_WF + wf_offset(w, lane, lane)) = (unsigned short)0x3F80;
      }
    }
    bf16x8 wb[2][9], xb[2][2];
    auto kstep = [&](auto SC, int xo, int wo, int wmo) {
      constexpr int S = decltype(SC)::value, N = S ^ 1;
      auto rdw = [&](int f) { wb[N][f] = lds_tr(lds, f < 8 ? wo + f * 32 + (f >= 4 ? 16 : 0) : wmo, 4608); };
#pragma unroll
      for (int n = 0; n < 9; ++n) {
        if (n == 4) cwait_lgkm<8>();
        mfma_acc(acc[n][0], wb[S][n], xb[S][0]);
        if (n == 0) { xb[N][0] = lds128(lds, xo); xb[N][1] = lds128(lds, xo + 2048); }
        else if (n <= 5) rdw(n - 1);
        else rdw(n);
        mfma_acc(acc[n][1], wb[S][n], xb[S][1]);
        if (n == 5) rdw(5);
        SBAR();
      }
    };
    using I0 = std::integral_constant<int, 0>;
    using I1 = std::integral_constant<int, 1>;
    barrier();
    {
      const int xo = opaque(xa0 + 3 * SLOT), wo = opaque(wa + 3 * SLOT), wmo = opaque(wm + 3 * SLOT);
      xb[0][0] = lds128(lds, xo); xb[0][1] = lds128(lds, xo + 2048);
#pragma unroll
      for (int n = 0; n < 9; ++n) wb[0][n] = lds_tr(lds, n < 8 ? wo + n * 32 + (n >= 4 ? 16 : 0) : wmo, 4608);
    }
    int base = 3 * SLOT;
#pragma unroll
    for (int c = 0; c < 8; ++c) {
      {
        const int xo = opaque(xa1 + base), wo = opaque(wa + base + 9216), wmo = opaque(wm + base + 9216);
        cwait_lgkm<10>();
        SBAR();
        kstep(I0{}, xo, wo, wmo);
      }
      if constexpr (ISB) {
        if (c == 0) {
#pragma unroll
          for (int k = 0; k < 6; ++k) *lp<unsigned short>(a_wf[k]) = wel[k];
        }
      }
      if (c == 5) wait_vm<0>();
      cwait_lgkm0();
      barrier();
      base = base == 3 * SLOT ? 0 : base + SLOT;
      {
        const int xo = opaque(xa0 + base), wo = opaque(wa + base), wmo = opaque(wm + base);
        SBAR();
        kstep(I1{}, xo, wo, wmo);
      }
    }
    {
      const int xo = opaque(xa1 + base), wo = opaque(wa + base + 9216), wmo = opaque(wm + base + 9216);
      cwait_lgkm0();
      SBAR();
      kstep(I0{}, xo, wo, wmo);
      cwait_lgkm0();
      lds_arrive(lds, SY_KV8);
      const int b0 = opaque(L_WF + lane * 16), b1 = opaque(L_WF + 10240 + lane * 8);
      SBAR();
#pragma unroll
      for (int n = 0; n < 9; ++n) {
        mfma_acc(acc[n][0], wb[1][n], xb[1][0]);
        if (n < 5) load_wf_tile(lds, b0, b1, wf, n);
        mfma_acc(acc[n][1], wb[1][n], xb[1][1]);
        SBAR();
      }
      lds_wait(lds, SY_KV8, 8u);
      mfma_fence(acc);
    }
  }
  const int lane = opaque(lane0), tid = w * 64 + lane, i = lane & 15, gg = lane >> 4;
  const unsigned Ts = __builtin_amdgcn_readfirstlane(T);
  __builtin_amdgcn_s_setprio(2);

  {
    f32x4 P[2][5];
    bf16x8 kb[2][2];
    u32x2 kb2[2];
#pragma unroll
    for (int m = 0; m < 2; ++m) {
      kb[m][0] = pack_pair(acc[0][m], acc[1][m]); kb[m][1] = pack_pair(acc[2][m], acc[3][m]);
      kb2[m] = u32x2{pk2(acc[8][m][0], acc[8][m][1]), pk2(acc[8][m][2], acc[8][m][3])};
    }
    SBAR();
#pragma unroll
    for (int st = 0; st < 30; ++st) {
      const int m = st / 15, s = (st % 15) / 5, tt = st % 5;
      if (s == 0) P[m][tt] = mfma(wf.a[tt][0], kb[m][0], zero4());
      if (s == 1) P[m][tt] = mfma(wf.a[tt][1], kb[m][1], P[m][tt]);
      if (s == 2) P[m][tt] = mfma16(wf.h[tt], kb2[m], P[m][tt]);
      if (st < 20) {
        const int n = st >> 2, pp = (st >> 1) & 1, mv = st & 1;
        const f32x4 v = n < 4 ? acc[4 + n][mv] : acc[8][mv];
        const int am = n < 4 ? a_v[n][pp] + mv * 2560 : (gg >= 2 ? a_v[n][pp] + mv * 2560 : L_ZERO + 124);
        *lp<unsigned>(am) = pk2(v[2 * pp], v[2 * pp + 1]);
      }
      SBAR();
    }
    *lp<unsigned>(a_one) = 0x00003F80u; *lp<unsigned>(a_one + 2560) = 0x00003F80u;
#pragma unroll
    for (int m = 0; m < 2; ++m) {
      unsigned ph[ISB ? 18 : 10];
      feature_map<ISB>(P[m], ph);
#pragma unroll
      for (int j = 0; j < NU; ++j)
        *lp<u32x2>((j < UMAIN ? a_prow + (a_pm ^ (j * 8)) : a_pl) + m * 16 * RP) = u32x2{ph[2 * j], ph[2 * j + 1]};
    }
  }
  lds_arrive(lds, SY_PHI);
  lds_wait(lds, SY_PHI);

  {
    bf16x8 al[4], la[4], b0[4], mu[4], b1[4];
    f32x4 d0, d1, d2, d3, d4, d5;
    auto rd = [&](int ks) {
      al[ks] = lds_tr(lds, a_al + ks * 32 * 160, 16 * 160);
      b0[ks] = lds_tr(lds, a_b0 + ks * 32 * RP, 16 * RP);
      if constexpr (ISB) b1[ks] = lds_tr(lds, a_b1 + ks * 32 * RP, 16 * RP);
      la[ks] = lds_tr(lds, a_la + ks * 32 * 160, 16 * 160);
      mu[ks] = lds_tr(lds, a_mu + ks * 32 * RP, 16 * RP);
      SBAR();
    };
    auto mm = [&](int ks) {
      if (ks == 0) {
        d0 = mfma(b0[0], al[0], zero4());
        if constexpr (ISB) d1 = mfma(b1[0], al[0], zero4()); else d1 = zero4();
        d2 = mfma(b0[0], la[0], zero4());
        if constexpr (ISB) d3 = mfma(b1[0], la[0], zero4()); else d3 = zero4();
        d4 = mfma(mu[0], al[0], zero4()); d5 = mfma(mu[0], la[0], zero4());
        SBAR();
        return;
      }
      mfma_acc(d0, b0[ks], al[ks]);
      if constexpr (ISB) mfma_acc(d1, b1[ks], al[ks]);
      mfma_acc(d2, b0[ks], la[ks]);
      if constexpr (ISB) mfma_acc(d3, b1[ks], la[ks]);
      mfma_acc(d4, mu[ks], al[ks]); mfma_acc(d5, mu[ks], la[ks]);
      SBAR();
    };
    constexpr int RD = ISB ? 10 : 8;
    rd(0); rd(1);
    __builtin_amdgcn_s_waitcnt(0xC07F & ~0xF00 | (RD << 8)); SBAR(); mm(0); rd(2);
    __builtin_amdgcn_s_waitcnt(0xC07F & ~0xF00 | (RD << 8)); SBAR(); mm(1); rd(3);
    __builtin_amdgcn_s_waitcnt(0xC07F & ~0xF00 | (RD << 8)); SBAR(); mm(2);
    cwait_lgkm0(); SBAR(); mm(3);
    asm volatile("s_nop 7\n\ts_nop 3" : "+v"(d0), "+v"(d1), "+v"(d2), "+v"(d3), "+v"(d4), "+v"(d5));
    if constexpr (ISB) {
      unsigned x0 = pk2(d0[0], d0[1]), x1 = pk2(d0[2], d0[3]), y0 = pk2(d1[0], d1[1]), y1 = pk2(d1[2], d1[3]);
      swap16(x0, y0); swap16(x1, y1);
      st_sc1(u32x4{nosent(x0), nosent(x1), nosent(y0), nosent(y1)}, rws, o_p0);
      x0 = pk2(d2[0], d2[1]); x1 = pk2(d2[2], d2[3]); y0 = pk2(d3[0], d3[1]); y1 = pk2(d3[2], d3[3]);
      swap16(x0, y0); swap16(x1, y1);
      st_sc1(u32x4{nosent(x0), nosent(x1), nosent(y0), nosent(y1)}, rws, o_p1);
    } else {
      unsigned x0 = pk2(d0[0], d0[1]), x1 = pk2(d0[2], d0[3]), y0 = pk2(d2[0], d2[1]), y1 = pk2(d2[2], d2[3]);
      swap16(x0, y0); swap16(x1, y1);
      st_sc1(u32x4{nosent(x0), nosent(x1), nosent(y0), nosent(y1)}, rws, o_p0);
    }
    st_sc1(u32x4{nosent(pk2(d4[0], d4[1])), nosent(pk2(d4[2], d4[3])), 0u, 0u}, rws, o_mu);
    st_sc1(u32x4{nosent(pk2(d5[0], d5[1])), nosent(pk2(d5[2], d5[3])), 0u, 0u}, rws, o_ml);
    st_tag(rws, WS_H1 + (((prob * 8 + g) * 16 + t) * 4 + w) * 128, T, lane);
    lds_arrive(lds, SY_KVDONE);
  }
  __builtin_amdgcn_s_setprio(0);
  const rsrc_t rwo = mk_rsrc(ISB ? a.woB : a.woA, 576 * XROW);
  {
    const rsrc_t rkv = mk_rsrc(ws + kvp_grp, 16 * KVP_STRIDE);
    constexpr int NG = (NCH + 15) / 16;
    const unsigned one = (i & 1) ? 0x3F800000u : 0x3F80u;
    const bf16x8 sel = frag_of((i >> 1) == 0 ? one : 0u, (i >> 1) == 1 ? one : 0u, (i >> 1) == 2 ? one : 0u, (i >> 1) == 3 ? one : 0u);
    const unsigned off0 = gg * KVP_STRIDE + w * KVHEAD + i * 16;
    const unsigned offl = 16 * (NG - 1) + i < NCH ? off0 + (NG - 1) * 256 : OOB;
    const int kvw = L_PHI + w * KVHEAD + i * 16 + gg * 8;
    unsigned pfo;
    {
      const int row = 18 * (blockIdx.x >> 3) + tid / 9, ln = tid - 9 * (tid / 9);
      asm volatile("buffer_load_dword %0, %1, %2, 0 offen" : "=v"(pfo) : "v"(tid < 162 ? row * XROW + ln * 128 : OOB), "s"(rwo) : "memory");
    }
    poll_tag(rws, lane < 16 ? WS_H1 + (((prob * 8 + g) * 16 + lane) * 4 + w) * 128 : OOB, Ts, 0xFFFFull);
    asm volatile("" :: "v"(pfo));
    __builtin_amdgcn_s_setprio(2);
    f32x4 sum[NG];
    {
      u32x4 v[NG][4];
#pragma unroll
      for (int cg = 0; cg < NG; ++cg) {
        const unsigned off = cg < NG - 1 ? off0 + cg * 256 : offl;
#pragma unroll
        for (int u = 0; u < 4; ++u)
          asm volatile("buffer_load_dwordx4 %0, %1, %2, %3 offen sc1" : "=v"(v[cg][u]) : "v"(off), "s"(rkv), "s"(4 * u * KVP_STRIDE) : "memory");
      }
      {
        const u32x4 s = {SENT, SENT, SENT, SENT};
#pragma unroll
        for (int k = 0; k < 5; ++k) {
          const int n = lane + 64 * k;
          st_sc1(s, rws, n < 288 ? obuf + (2 * w * 72 + 9 * g) * 256 + n * 16 + (n >= 144 ? (72 - 9) * 256 : 0) : OOB);
        }
        SBAR();
      }
      lds_wait(lds, SY_KVDONE);
      float chk = 0.f;
#pragma unroll
      for (int cg = 0; cg < NG; ++cg) {
        asm volatile("s_waitcnt vmcnt(%4)" : "+v"(v[cg][0]), "+v"(v[cg][1]), "+v"(v[cg][2]), "+v"(v[cg][3]) : "n"((NG - 1 - cg) * 4 + 5));
        SBAR();
        f32x4 d = zero4();
#pragma unroll
        for (int u = 0; u < 4; ++u) d = mfma(sel, __builtin_bit_cast(bf16x8, v[cg][u]), d);
        sum[cg] = d;
        chk += d[0];
      }
      if (!__all(chk == chk)) {
#pragma unroll 1
        for (int sp = 0; sp < SPIN_G; ++sp) {
          unsigned mx = 0u;
#pragma unroll 1
          for (int n = 0; n < NG * 4; ++n) {
            asm volatile("" ::: "memory");
            const u32x4 x = ld_sc1(rkv, (n >> 2) < NG - 1 ? off0 + (n >> 2) * 256 : offl, 4 * (n & 3) * KVP_STRIDE);
            mx = umax3(mx, x[0], x[1]); mx = umax3(mx, x[2], x[3]);
          }
          if (__all(mx != SENT)) break;
        }
#pragma unroll
        for (int cg = 0; cg < NG; ++cg) {
          f32x4 d = zero4();
#pragma unroll
          for (int u = 0; u < 4; ++u)
            d = mfma(sel, __builtin_bit_cast(bf16x8, ld_sc1(rkv, cg < NG - 1 ? off0 + cg * 256 : offl, 4 * u * KVP_STRIDE)), d);
          sum[cg] = d;
        }
      }
    }
    if (gg < 2) {
#pragma unroll
      for (int cg = 0; cg < NG; ++cg)
        if (cg < NG - 1 || 16 * cg + i < NCH)
          *lp<u32x2>(kvw + cg * 256) = u32x2{pk2(sum[cg][0], sum[cg][1]), pk2(sum[cg][2], sum[cg][3])};
    }
    lds_arrive(lds, SY_KVIMG);
    wait_vm<0>();
    lds_arrive(lds, SY_PRE);
  }
  __builtin_amdgcn_s_setprio(0);

  {
    const rsrc_t ro = mk_rsrc(ws + obuf, O_STRIDE);
    const rsrc_t rout = mk_rsrc(a.out, 2048 * OUTROW);
    const unsigned orow = (t * 128 + 32 * w + i) * OUTROW + prob * 1152 + 144 * g;
    const unsigned o_pair = orow + (2 * (gg & 1) + (gg >> 1)) * 16, o_last = gg < 2 ? orow + gg * 16 * OUTROW + 128 : OOB;
    const int wl = (4 * gg + (i >> 2)) * 160 + (i & 3) * 8;
    const int wa2 = opaque(L_WQ + wl), wa3 = opaque(L_WQ + wl + 12 * 5120);
    lds_wait(lds, SY_WO);
    poll_tag(rws, lane < 8 ? WS_H2 + (((prob * 16 + t) * 8 + lane) * 4 + w) * 128 : OOB, Ts, 0xFFull);
    __builtin_amdgcn_s_setprio(2);
    f32x4 oc[5][2];
#pragma unroll 1
    for (int pass = 0; pass < 2; ++pass) {
      asm volatile("" ::: "memory");
      u32x4 of[18][2];
#pragma unroll
      for (int ks = 0; ks < OLOOK; ++ks) {
#pragma unroll
        for (int m = 0; m < 2; ++m) { of[ks][m] = ld_sc1(ro, lane * 16, ((2 * w + m) * 72 + 4 * ks) * 256); SBAR(); }
      }
      bf16x8 wq[2][5];
#pragma unroll
      for (int n = 0; n < 5; ++n) {
        wq[0][n] = lds_tr(lds, wa2 + n * 32, 16 * 160);
        oc[n][0] = zero4(); oc[n][1] = zero4();
        asm volatile("" : "+v"(oc[n][0]), "+v"(oc[n][1]));
      }
      asm volatile("s_nop 1");
      SBAR();
#define OUT_STEP(ks) do { cwait_vm_lgkm4<2 * (((ks) + OLOOK - 1 < 17 ? (ks) + OLOOK - 1 : 17) - (ks))>(); SBAR(); \
      if ((ks) + OLOOK < 18) { of[((ks) + OLOOK) % 18][0] = ld_sc1(ro, lane * 16, ((2 * w) * 72 + 4 * ((ks) + OLOOK)) * 256); \
                               of[((ks) + OLOOK) % 18][1] = ld_sc1(ro, lane * 16, ((2 * w + 1) * 72 + 4 * ((ks) + OLOOK)) * 256); SBAR(); } \
      step5(lds, oc, wq[(ks) & 1], wq[((ks) + 1) & 1], __builtin_bit_cast(bf16x8, of[ks][0]), __builtin_bit_cast(bf16x8, of[ks][1]), \
            (ks) + 1 < 12 ? wa2 + ((ks) + 1) * 5120 : wa3 + ((ks) + 1 - 12) * 5120, (ks) < 17); } while (0)
      OUT_STEP(0); OUT_STEP(1); OUT_STEP(2); OUT_STEP(3); OUT_STEP(4); OUT_STEP(5); OUT_STEP(6); OUT_STEP(7); OUT_STEP(8);
      OUT_STEP(9); OUT_STEP(10); OUT_STEP(11); OUT_STEP(12); OUT_STEP(13); OUT_STEP(14); OUT_STEP(15); OUT_STEP(16); OUT_STEP(17);
#undef OUT_STEP
      mfma_fence(oc);
      const float chk = oc[0][0][0] + oc[0][1][0];
      if (pass == 1 || __all(chk == chk)) break;
#pragma unroll 1
      for (int sp = 0; sp < SPIN_G; ++sp) {
        unsigned mx = 0u;
#pragma unroll 1
        for (int n = 0; n < 36; ++n) {
          asm volatile("" ::: "memory");
          const u32x4 x = ld_sc1(ro, lane * 16, ((2 * w + (n & 1)) * 72 + 4 * (n >> 1)) * 256);
          mx = umax3(mx, x[0], x[1]); mx = umax3(mx, x[2], x[3]);
        }
        if (__all(mx != SENT)) break;
      }
    }
#pragma unroll
    for (int m = 0; m < 2; ++m) {
#pragma unroll
      for (int n = 0; n < 4; n += 2) {
        unsigned x0 = pk2(oc[n][m][0], oc[n][m][1]), x1 = pk2(oc[n][m][2], oc[n][m][3]);
        unsigned y0 = pk2(oc[n + 1][m][0], oc[n + 1][m][1]), y1 = pk2(oc[n + 1][m][2], oc[n + 1][m][3]);
        swap16(x0, y0); swap16(x1, y1);
        __builtin_amdgcn_raw_buffer_store_b128(u32x4{x0, x1, y0, y1}, rout, o_pair + m * 16 * OUTROW + n * 32, 0, 0);
      }
    }
    unsigned x0 = pk2(oc[4][0][0], oc[4][0][1]), x1 = pk2(oc[4][0][2], oc[4][0][3]);
    unsigned y0 = pk2(oc[4][1][0], oc[4][1][1]), y1 = pk2(oc[4][1][2], oc[4][1][3]);
    swap16(x0, y0); swap16(x1, y1);
    __builtin_amdgcn_raw_buffer_store_b128(u32x4{x0, x1, y0, y1}, rout, o_last, 0, 0);
    if (t == 0 && g == 0 && tid == 0) __hip_atomic_store((GAS unsigned*)(ws + WS_E + prob * 256), T, RLX_AGENT);
  }
}

template <bool ISB> DI void h_path(const Args& a, LAS char* lds, const int w, const int lane, const int t, const int g) {
  constexpr int prob = ISB ? 1 : 0;
  constexpr int C5 = ISB ? 5 : 3;
  constexpr int KVROW = C5 * 16, KVHEAD = ISB ? 1536 : 1024;
  const int i = lane & 15, gg = lane >> 4;
  const rsrc_t rx = mk_rsrc((const char*)(ISB ? a.xB : a.xA) + (size_t)t * 128 * XROW, 128 * XROW);
  const rsrc_t rw = mk_rsrc(ISB ? a.wB : a.wA, 576 * WROW);
  const unsigned lb = 0u;
  const int rot = (g + 2 * (t & 3) + ((t >> 1) & 1)) % 9;
  auto kchunk = [&](int c) { const int k = c + rot; return k >= 9 ? k - 9 : k; };
  unsigned vx[2], vw[5], vq[8];
#pragma unroll
  for (int par = 0; par < 2; ++par) vx[par] = (lane >> 3) * XROW + (((lane & 7) ^ ((4 * par + (lane >> 4)) & 7)) * 16);
  auto issue_x = [&](int c, int j0 = 0, int j1 = 4) {
    const unsigned sb = lb + ((c + 3) & 3) * SLOT;
#pragma unroll
    for (int jj = 0; jj < 4; ++jj)
      if (jj >= j0 && jj < j1) dma16(rx, vx[jj & 1], (4 * w + jj) * 8 * XROW + kchunk(c) * 128, sb + (4 * w + jj) * 1024);
  };
  issue_x(0);
#pragma unroll
  for (int n = 0; n < 5; ++n) {
    const int nn = (w + 4 * n) * 64 + lane, r64 = nn / 18, cp = nn - 18 * r64;
    const int co = cp < 9 ? 1152 + 16 * cp : 2304 + 16 * (cp - 9);
    vw[n] = ((r64 & 32) + krow(r64 & 31)) * WROW + 144 * g + co;
  }
  auto issue_w = [&](int c) {
    const unsigned sb = lb + ((c + 3) & 3) * SLOT;
#pragma unroll
    for (int n = 0; n < 5; ++n)
      if (n < 4 || w < 2) dma16(rw, vw[n], kchunk(c) * 64 * WROW, sb + WPART + (w + 4 * n) * 1024);
  };
  issue_w(0); issue_x(1); issue_w(1);
  if (w < 2) wait_vm<9>(); else wait_vm<8>();
  barrier();
  issue_x(2); issue_w(2);
#pragma unroll
  for (int n = 0; n < 8; ++n) {
    const int v = w + 4 * n, ks = v / 5, nn = (v - 5 * ks) * 64 + lane, rho = nn / 10, cp = nn - 10 * rho;
    vq[n] = ((ks & 1) * 32 + krow(rho)) * WROW + 144 * g + 16 * min(cp, 8);
  }
  auto issue_wq = [&](int grp) {
#pragma unroll
    for (int n = 0; n < 8; ++n)
      if (n < 7 || w < 2)
        dma16(rw, vq[n], kchunk(3 * grp + ((w + 4 * n) / 5 >> 1)) * 64 * WROW, lb + L_WQ + grp * 30720 + (w + 4 * n) * 1024);
  };
  bf16x8 X[18][2];
  const int xa0 = (32 * w + i) * 128 + ((gg ^ ((i >> 1) & 7)) * 16), xa1 = xa0 ^ 64;
#pragma unroll
  for (int c = 0; c < 9; ++c) {
    const int base = ((c + 3) & 3) * SLOT;
    if (c <= 5) issue_x(c + 3, 0, 2);
    SBAR();
    const int xo0 = opaque(xa0 + base), xo1 = opaque(xa1 + base);
    X[2 * c][0] = lds128(lds, xo0); X[2 * c][1] = lds128(lds, xo0 + 2048);
    X[2 * c + 1][0] = lds128(lds, xo1); X[2 * c + 1][1] = lds128(lds, xo1 + 2048);
    SBAR();
    if (c <= 5) { issue_x(c + 3, 2, 4); issue_w(c + 3); }
    if (c <= 5) { if (w < 2) wait_vm<18>(); else wait_vm<16>(); }
    if (c == 6) { if (w < 2) wait_vm<9>(); else wait_vm<8>(); }
    if (c == 7) wait_vm<0>();
    if (c < 8) barrier(); else lds_arrive(lds, SY_KV8);
  }
  issue_wq(0); issue_wq(1); issue_wq(2);
  __builtin_amdgcn_s_setprio(1);
  f32x4 qa[5][2];
#pragma unroll
  for (int n = 0; n < 5; ++n) { qa[n][0] = zero4(); qa[n][1] = zero4(); asm volatile("" : "+v"(qa[n][0]), "+v"(qa[n][1])); }
  const int wa2 = opaque(L_WQ + (4 * gg + (i >> 2)) * 160 + (i & 3) * 8), wa3 = opaque(wa2 + 12 * 5120);
  bf16x8 wq[2][5];
  if (w < 2) wait_vm<16>(); else wait_vm<14>();
  lds_arrive(lds, SY_WQ0);
  lds_wait(lds, SY_WQ0);
  lds_wait(lds, SY_PHI);
#pragma unroll
  for (int n = 0; n < 5; ++n) wq[0][n] = lds_tr(lds, wa2 + n * 32, 16 * 160);
#pragma unroll
  for (int ks = 0; ks < 18; ++ks) {
    cwait_lgkm<4>();
    if (ks == 5) { if (w < 2) wait_vm<8>(); else wait_vm<7>(); lds_arrive(lds, SY_WQ1); lds_wait(lds, SY_WQ1); }
    if (ks == 11) { wait_vm<0>(); lds_arrive(lds, SY_WQ2); lds_wait(lds, SY_WQ2); }
    SBAR();
    step5(lds, qa, wq[ks & 1], wq[(ks + 1) & 1], X[ks][0], X[ks][1], ks + 1 < 12 ? wa2 + (ks + 1) * 5120 : wa3 + (ks + 1 - 12) * 5120, ks < 17);
  }
  WF wf;
  {
    const int b0 = opaque(L_WF + lane * 16), b1 = opaque(L_WF + 10240 + lane * 8);
#pragma unroll
    for (int n = 0; n < 5; ++n) load_wf_tile(lds, b0, b1, wf, n);
  }
  mfma_fence(qa);
  lds_arrive(lds, SY_QDONE);
  const unsigned T = *lp<unsigned>(L_SYNC + SY_TAG * 4);
  unsigned ph[2][ISB ? 18 : 10];
  {
    f32x4 P[2][5];
    logits2(wf, qa[0], qa[1], qa[2], qa[3], qa[4], P);
    feature_map<ISB>(P[0], ph[0]);
    feature_map<ISB>(P[1], ph[1]);
#pragma unroll
    for (int j = 0; j < (ISB ? 18 : 10); ++j) asm volatile("" : "+v"(ph[0][j]), "+v"(ph[1][j]));
  }
  int a_kv[5], a_st[9];
  {
    const bool own = (i >> 2) == gg;
#pragma unroll
    for (int n = 0; n < 5; ++n) {
      const int e = 4 * n + (i & 3);
      a_kv[n] = opaque((own && e < 19) ? L_PHI + gg * KVHEAD + e * KVROW : L_ZERO);
    }
#pragma unroll
    for (int pp = 0; pp < 9; ++pp) {
      const int cp = 9 * gg + pp;
      a_st[pp] = opaque(L_V + w * 4608 + (cp >> 2) * 256 + i * 16 + (cp & 3) * 4);
    }
  }
  const rsrc_t ro = mk_rsrc(a.ws + WS_O + (prob * 16 + t) * O_STRIDE, O_STRIDE);
  const unsigned o_dst = (2 * w * 72 + 9 * g) * 256 + lane * 16;
  const rsrc_t rwo = mk_rsrc(ISB ? a.woB : a.woA, 576 * XROW);
  unsigned vo[5];
#pragma unroll
  for (int n = 0; n < 5; ++n) {
    const int nn = ((w + 4 * n) % 5) * 64 + lane, rho = nn / 10, cp = nn - 10 * rho;
    vo[n] = (unsigned)opaque(krow(rho) * XROW + 144 * g + 16 * min(cp, 8));
  }
  lds_wait(lds, SY_QDONE);
#pragma unroll
  for (int n = 0; n < WO_FIRST; ++n) { const int v = w + 4 * n; dma16(rwo, vo[n % 5], (v / 5) * 32 * XROW, lb + L_WQ + v * 1024); }
  lds_wait(lds, SY_KVIMG);
  __builtin_amdgcn_s_setprio(3);
  f32x4 O[5][2];
  {
    bf16x8 af[5][C5];
#pragma unroll
    for (int s = 0; s < C5; ++s) {
#pragma unroll
      for (int n = 0; n < 5; ++n) af[n][s] = lds128(lds, a_kv[n] + s * 16);
    }
    SBAR();
    constexpr int NP = ISB ? 18 : 10;
#pragma unroll
    for (int m = 0; m < 2; ++m) {
#pragma unroll
      for (int s = 0; s < C5; ++s) {
        unsigned u[4];
#pragma unroll
        for (int j = 0; j < 4; ++j) u[j] = 4 * s + j < NP ? ph[m][4 * s + j < NP ? 4 * s + j : 0] : 0u;
        const bf16x8 bq = frag_of(u[0], u[1], u[2], u[3]);
#pragma unroll
        for (int n = 4; n >= 0; --n) O[n][m] = mfma(af[n][s], bq, s == 0 ? zero4() : O[n][m]);
      }
    }
  }
#pragma unroll
  for (int m = 0; m < 2; ++m) {
    const float rc = __builtin_amdgcn_rcpf(O[4][m][2] + 1e-6f);
    const f32x2 rc2 = {rc, rc};
#pragma unroll
    for (int pp = 0; pp < 9; ++pp) {
      const int e = 2 * pp;
      const f32x2 y = f32x2{O[e >> 2][m][e & 3], O[(e + 1) >> 2][m][(e + 1) & 3]} * rc2;
      *lp<unsigned>(a_st[pp] + m * 2304) = nosent(pk2(y[0], y[1]));
    }
  }
  lds_wait(lds, SY_PRE);
  {
    const int sb = L_V + w * 4608 + lane * 16;
    u32x4 v[5];
#pragma unroll
    for (int k = 0; k < 5; ++k) v[k] = *lp<u32x4>(sb + (k < 4 || lane < 32 ? k * 1024 : 0));
#pragma unroll
    for (int k = 0; k < 5; ++k) {
      const int n = lane + 64 * k;
      st_sc1(v[k], ro, n < 288 ? o_dst + k * 1024 + (n >= 144 ? (72 - 9) * 256 : 0) : OOB);
    }
    st_tag(mk_rsrc(a.ws, WS_USED), WS_H2 + (((prob * 16 + t) * 8 + g) * 4 + w) * 128, T, lane);
  }
#pragma unroll
  for (int n = WO_FIRST; n < 23; ++n) {
    const int v = w + 4 * n;
    if (n < 22 || w < 2) dma16(rwo, vo[n % 5], (v / 5) * 32 * XROW, lb + L_WQ + v * 1024);
  }
  wait_vm<0>();
  lds_arrive(lds, SY_WO);
}

__global__ __launch_bounds__(512, 2) void hedgehog_fused(Args a) {
  extern __shared__ __attribute__((aligned(16))) char smem[];
  LAS char* lds = (LAS char*)smem;
  if ((unsigned)(size_t)lds != 0u) return;
  const int b = blockIdx.x, xcd = b & 7, r = b >> 3;
  const int prob = xcd >> 2, t = (xcd & 3) * 4 + (r >> 3), g = r & 7;
  const int wave = __builtin_amdgcn_readfirstlane(threadIdx.x >> 6), lane = threadIdx.x & 63;
  if (wave < 4) {
    if (prob) c_path<true>(a, lds, wave & 3, lane, t, g); else c_path<false>(a, lds, wave & 3, lane, t, g);
  } else {
    if (prob) h_path<true>(a, lds, wave & 3, lane, t, g); else h_path<false>(a, lds, wave & 3, lane, t, g);
  }
}

}

extern "C" void kernel_launch(void* const* d_in, const int* in_sizes, int n_in, void* d_out, int out_size, void* d_ws,
                              size_t ws_size, hipStream_t stream) {
  (void)in_sizes; (void)n_in; (void)out_size; (void)ws_size;
  Args a;
  a.xA = d_in[0]; a.wA = d_in[1]; a.woA = d_in[2]; a.xB = d_in[3]; a.wB = d_in[4]; a.wphi = d_in[5]; a.woB = d_in[6];
  a.out = d_out; a.ws = (char*)d_ws;
  (void)hipFuncSetAttribute((const void*)hedgehog_fused, hipFuncAttributeMaxDynamicSharedMemorySize, LDS_BYTES);
  hedgehog_fused<<<256, 512, LDS_BYTES, stream>>>(a);
}
```
